```python
import torch

def _softmax(x, axis=-1):
    return torch.softmax(x, dim=axis)
def _gelu(x):
    return torch.nn.functional.gelu(x, approximate="tanh")
def _silu(x):
    return torch.nn.functional.silu(x)
def _sigmoid(x):
    return torch.sigmoid(x)
def _layernorm(x, axis=-1, eps=1e-5):
    m = x.mean(dim=axis, keepdim=True)
    v = x.var(dim=axis, keepdim=True, unbiased=False)
    return (x - m) / torch.sqrt(v + eps)
def _scatter_add(tgt, idx, src, axis=0):
    return tgt.clone().index_add_(axis, idx, src)
def _segment_sum(src, seg, n):
    out = torch.zeros((n,) + src.shape[1:], dtype=src.dtype)
    return out.index_add_(0, seg, src)
def _pad(x, pw):
    flat = tuple(v for pair in reversed(pw) for v in pair)
    return torch.nn.functional.pad(x, flat)

kernel_name = 'gated_mix__mha_block__spectral_fnet__prompt'

def setup_inputs():

    _w = (lambda a: (lambda t: t.to(torch.bfloat16) if t.is_floating_point() else t)(torch.as_tensor(a)))
    def _A_setup():
        B, S, D, H = 1, 2048, 640, 32
        return {
            "x": _w((lambda *s: torch.randn(*s, dtype=torch.bfloat16) * 0.05)(B, S, D)),
            "wqkv": _w((lambda *s: torch.randn(*s, dtype=torch.bfloat16) * 1.6)(D, 3 * D)),
            "wo": _w((lambda *s: torch.randn(*s, dtype=torch.bfloat16) * 1.6)(D, D)),
        }
    _A_in = _A_setup()

    def _B_setup():
        B, S, D = 1, 2048, 640
        ns = torch.arange(S)[:, None] * torch.arange(S)[None, :]
        nd = torch.arange(D)[:, None] * torch.arange(D)[None, :]
        Ws_re = torch.cos(-2.0 * torch.pi * ns / S).to(float)
        Ws_im = torch.sin(-2.0 * torch.pi * ns / S).to(float)
        Wd_re = torch.cos(-2.0 * torch.pi * nd / D).to(float)
        Wd_im = torch.sin(-2.0 * torch.pi * nd / D).to(float)
        return {
            "x": _w((lambda *s: torch.randn(*s, dtype=torch.bfloat16) * 0.05)(B, S, D)),
            "dft_seq_re": _w(Ws_re),
            "dft_seq_im": _w(Ws_im),
            "dft_hid_re": _w(Wd_re),
            "dft_hid_im": _w(Wd_im),
        }
    _B_in = _B_setup()

    return {
        "A_x": _A_in["x"],
        "A_wqkv": _A_in["wqkv"],
        "A_wo": _A_in["wo"],
        "B_x": _B_in["x"],
        "B_dft_seq_re": _B_in["dft_seq_re"],
        "B_dft_seq_im": _B_in["dft_seq_im"],
        "B_dft_hid_re": _B_in["dft_hid_re"],
        "B_dft_hid_im": _B_in["dft_hid_im"],
        "gate_w": _w((lambda *s: torch.randn(*s, dtype=torch.bfloat16) * 0.05)(640) * 16.0),
    }

def reference(A_x, A_wqkv, A_wo, B_x, B_dft_seq_re, B_dft_seq_im, B_dft_hid_re, B_dft_hid_im, gate_w):
    def _A_ref(x, wqkv, wo):
        B, S, D = x.shape
        H = 32
        Dh = D // H
        qkv = (lambda x, *d: x.permute(*d))((x @ wqkv).reshape(B, S, 3, H, Dh), 2, 0, 3, 1, 4)
        q, k, v = qkv[0], qkv[1], qkv[2]
        scores = torch.einsum("bhqd,bhkd->bhqk", q, k) * (Dh ** -0.5)
        p = _softmax(scores, axis=-1)
        o = torch.einsum("bhqk,bhkd->bhqd", p, v)
        return (lambda x, *d: x.permute(*d))(o, 0, 2, 1, 3).reshape(B, S, D) @ wo

    def _B_ref(x, dft_seq_re, dft_seq_im, dft_hid_re, dft_hid_im):
        a = torch.einsum("bsd,ed->bse", x, dft_hid_re)
        b = torch.einsum("bsd,ed->bse", x, dft_hid_im)
        y = torch.einsum("ts,bse->bte", dft_seq_re, a) - torch.einsum("ts,bse->bte", dft_seq_im, b)
        return y

    x = A_x
    g = _sigmoid((x * gate_w).sum(-1, keepdims=True))
    a = _A_ref(x, A_wqkv, A_wo)
    b = _B_ref(x, B_dft_seq_re, B_dft_seq_im, B_dft_hid_re, B_dft_hid_im)
    return g * a + (1 - g) * b
```

```cpp
#include <hip/hip_runtime.h>
#include <hip/hip_bf16.h>
#include <stdint.h>

typedef short s16x8 __attribute__((ext_vector_type(8)));
typedef short s16x4 __attribute__((ext_vector_type(4)));
typedef float f32x16 __attribute__((ext_vector_type(16)));
typedef float f32x4 __attribute__((ext_vector_type(4)));
typedef unsigned u32x4 __attribute__((ext_vector_type(4)));
typedef unsigned u32x2 __attribute__((ext_vector_type(2)));
typedef __bf16 bf16x2_t __attribute__((ext_vector_type(2)));
typedef __attribute__((address_space(3))) const char* lds_cp;
typedef __attribute__((address_space(3))) char* lds_p;

#define DI __device__ __forceinline__
#define UNROLL _Pragma("unroll")
#define MFMA32(a, b, c) __builtin_amdgcn_mfma_f32_32x32x16_bf16((a), (b), (c), 0, 0, 0)
#define MFMA32K8(a, b, c) __builtin_amdgcn_mfma_f32_32x32x8bf16_1k((a), (b), (c), 0, 0, 0)
#define MFMA16(a, b, c) __builtin_amdgcn_mfma_f32_16x16x32_bf16((a), (b), (c), 0, 0, 0)
#define SBAR() __builtin_amdgcn_sched_barrier(0)

constexpr int SEQ = 2048, DM = 640, NH = 32;
constexpr size_t QF_TILE = 1280;
constexpr size_t WS_Q = 0;
constexpr size_t KV_TILE = 2592;
constexpr size_t KV_HEAD = 64 * KV_TILE;
constexpr size_t WS_KV = WS_Q + (size_t)NH * 64 * QF_TILE;
constexpr size_t WS_AB = WS_KV + (size_t)NH * KV_HEAD + 4096;
constexpr size_t WS_G = WS_AB + (size_t)20 * 512 * 512;
constexpr size_t WS_O = WS_G + 8192;
constexpr size_t WS_WO = WS_O + (size_t)SEQ * DM * 2;
constexpr size_t WS_Y = WS_WO + (size_t)DM * DM * 2;
constexpr size_t YP = (size_t)SEQ * DM * 2;

DI unsigned pack2(float a, float b) {
    bf16x2_t v = {(__bf16)a, (__bf16)b};
    return __builtin_bit_cast(unsigned, v);
}
DI float bf2f(unsigned short b) { return __uint_as_float((unsigned)b << 16); }
DI float bflo(unsigned u) { return __uint_as_float(u << 16); }
DI float bfhi(unsigned u) { return __uint_as_float(u & 0xFFFF0000u); }

template <int OFF>
DI void dma16(unsigned voff, const void* sbase, unsigned ldsaddr) {
    unsigned keep;
    asm volatile("s_mov_b32 %0, m0\n\ts_mov_b32 m0, %3\n\ts_nop 0\n\tglobal_load_lds_dwordx4 %1, %2 offset:%4\n\ts_mov_b32 m0, %0"
                 : "=&s"(keep) : "v"(voff), "s"(sbase), "s"(ldsaddr), "i"(OFF) : "memory");
}
#define WAITV(n) asm volatile("s_waitcnt vmcnt(" #n ")" ::: "memory")
#define BARRIER() asm volatile("s_waitcnt lgkmcnt(0)\n\ts_barrier" ::: "memory")
#define BARRIER_NOWAIT() asm volatile("s_barrier" ::: "memory")

DI s16x8 lds128(lds_cp p) { return *(const __attribute__((address_space(3))) s16x8*)p; }
DI s16x4 ldstr(lds_cp p) {
    return __builtin_bit_cast(s16x4, __builtin_amdgcn_ds_read_tr16_b64_v4i16((__attribute__((address_space(3))) s16x4*)p));
}
DI s16x8 cat8(s16x4 a, s16x4 b) { return __builtin_shufflevector(a, b, 0, 1, 2, 3, 4, 5, 6, 7); }

constexpr int K1_XB = 16384, K1_STAGE = 16384 + 28672, K1_LDS = 3 * K1_STAGE;

__global__ __launch_bounds__(512, 2) void k1_proj(const char* __restrict__ x, const char* __restrict__ wqkv,
                                                   const char* __restrict__ wr, const char* __restrict__ wi,
                                                   char* __restrict__ ws) {
    extern __shared__ __attribute__((aligned(16))) char smem[];
    const int tid = threadIdx.x, lane = tid & 63;
    const int wid = __builtin_amdgcn_readfirstlane(tid >> 6);
    const int xcd = blockIdx.x & 7, idx = blockIdx.x >> 3;
    const int mt = (xcd & 1) * 8 + (idx & 7), ct = (xcd >> 1) * 4 + (idx >> 3);
    const unsigned lds0 = (unsigned)(uintptr_t)smem;
    const bool isqkv = ct < 10;
    const int dt = ct - 10;
    const int u0 = (dt % 3) * 7;
    const int ns = (dt % 3) == 2 ? 6 : 7;

    if (wid >= 4) {
        const int lw = wid - 4;
        unsigned xoff[4], woff[7];
        UNROLL for (int i = 0; i < 4; ++i) {
            int row = (4 * lw + i) * 8 + (lane >> 3);
            int c = (lane & 7) ^ ((row >> 1) & 7);
            xoff[i] = row * 1280 + c * 16;
        }
        const char* xb = x + (size_t)mt * 128 * 1280;
        if (isqkv) {
            UNROLL for (int i = 0; i < 6; ++i) {
                int p = lw * 6 + i, g = p >> 3, k = (p & 7) * 8 + (lane >> 3);
                int c = (lane & 7) ^ (((k >> 1) & 1) << 2);
                woff[i] = k * 3840 + g * 128 + c * 16;
            }
            const char* wb = wqkv + ct * 384;
            auto issue = [&](int st, int slot) {
                const char* xs = xb + st * 128;
                const char* wsrc = wb + (size_t)st * 64 * 3840;
                unsigned l = lds0 + slot * K1_STAGE;
                UNROLL for (int i = 0; i < 4; ++i) dma16<0>(xoff[i], xs, l + (4 * lw + i) * 1024);
                UNROLL for (int i = 0; i < 6; ++i) dma16<0>(woff[i], wsrc, l + K1_XB + (lw * 6 + i) * 1024);
            };
            issue(0, 0); issue(1, 1); issue(2, 2);
            WAITV(20); BARRIER_NOWAIT();
            int slot = 0;
            for (int k = 1; k < 10; ++k) {
                if (k < 9) { WAITV(10); } else { WAITV(0); }
                BARRIER_NOWAIT();
                if (k + 2 < 10) issue(k + 2, slot);
                slot = slot == 2 ? 0 : slot + 1;
            }
        } else {
            UNROLL for (int i = 0; i < 7; ++i) {
                int row = (lw * 7 + i) * 8 + (lane >> 3);
                int c = (lane & 7) ^ ((row >> 1) & 7);
                int rr = row < ns * 32 ? row : 0;
                woff[i] = rr * 1280 + c * 16;
            }
            const char* wb = (dt < 3 ? wr : wi) + (size_t)u0 * 32 * 1280;
            auto issue = [&](int st, int slot) {
                const char* xs = xb + st * 128;
                const char* wsrc = wb + st * 128;
                unsigned l = lds0 + slot * K1_STAGE;
                UNROLL for (int i = 0; i < 4; ++i) dma16<0>(xoff[i], xs, l + (4 * lw + i) * 1024);
                UNROLL for (int i = 0; i < 7; ++i) dma16<0>(woff[i], wsrc, l + K1_XB + (lw * 7 + i) * 1024);
            };
            issue(0, 0); issue(1, 1); issue(2, 2);
            WAITV(22); BARRIER_NOWAIT();
            int slot = 0;
            for (int k = 1; k < 10; ++k) {
                if (k < 9) { WAITV(11); } else { WAITV(0); }
                BARRIER_NOWAIT();
                if (k + 2 < 10) issue(k + 2, slot);
                slot = slot == 2 ? 0 : slot + 1;
            }
        }
        return;
    }

    const int r = lane & 31, hh = lane >> 5;
    const lds_cp L = (lds_cp)smem;
    unsigned rowa[4];
    UNROLL for (int ks = 0; ks < 4; ++ks) rowa[ks] = r * 128 + (((2 * ks + hh) ^ ((r >> 1) & 7)) << 4);

    if (isqkv) {
        const int wm = wid & 1, wn = wid >> 1;
        f32x16 acc[2][3];
        UNROLL for (int i = 0; i < 2; ++i) UNROLL for (int j = 0; j < 3; ++j) acc[i][j] = f32x16{};
        const int q = (lane & 15) >> 2, p = lane & 3, blk = (lane >> 4) & 1;
        unsigned trb[3];
        UNROLL for (int j = 0; j < 3; ++j) {
            int u = wn * 3 + j;
            trb[j] = K1_XB + (u >> 1) * 8192 + (8 * hh + q) * 128 + (((u & 1) ^ ((q >> 1) & 1)) << 6) + 32 * blk + 8 * p;
        }
        int slot = 0;
        for (int k = 0; k < 10; ++k) {
            BARRIER();
            const lds_cp sp = L + slot * K1_STAGE;
            UNROLL for (int ks = 0; ks < 4; ++ks) {
                s16x8 a[2], b[3];
                UNROLL for (int i = 0; i < 2; ++i) a[i] = lds128(sp + (wm * 2 + i) * 4096 + rowa[ks]);
                UNROLL for (int j = 0; j < 3; ++j) b[j] = cat8(ldstr(sp + trb[j] + ks * 2048), ldstr(sp + trb[j] + ks * 2048 + 512));
                UNROLL for (int i = 0; i < 2; ++i) UNROLL for (int j = 0; j < 3; ++j) acc[i][j] = MFMA32(a[i], b[j], acc[i][j]);
            }
            slot = slot == 2 ? 0 : slot + 1;
        }
        UNROLL for (int j = 0; j < 3; ++j) {
            const int n = ct * 192 + (wn * 3 + j) * 32 + r;
            const int ty = (ct * 6 + wn * 3 + j) / 20;
            const int nn = n - ty * 640;
            const int hd = nn / 20, dim = nn - hd * 20;
            const float sc = ty == 0 ? 0.32259889079f : 1.0f;
            UNROLL for (int i = 0; i < 2; ++i) {
                const int kt = mt * 4 + wm * 2 + i;
                char* dst; int sstr;
                if (ty == 0) { dst = ws + WS_Q + (size_t)(hd * 64 + kt) * QF_TILE + dim * 64 + hh * 16; sstr = 32; }
                else if (ty == 1) { dst = ws + WS_KV + (size_t)hd * KV_HEAD + (size_t)kt * KV_TILE + dim * 64 + hh * 16; sstr = 32; }
                else { dst = ws + WS_KV + (size_t)hd * KV_HEAD + (size_t)kt * KV_TILE + 1280 + hh * 320 + dim * 16; sstr = 656; }
                UNROLL for (int s = 0; s < 2; ++s) {
                    u32x4 v;
                    UNROLL for (int e = 0; e < 4; ++e) v[e] = pack2(acc[i][j][8 * s + 2 * e] * sc, acc[i][j][8 * s + 2 * e + 1] * sc);
                    *(u32x4*)(dst + s * sstr) = v;
                }
                if (ty == 2 && dim == 0) {
                    *(u32x4*)(ws + WS_KV + (size_t)hd * KV_HEAD + (size_t)kt * KV_TILE + 1280 + hh * 656 + 640) =
                        u32x4{0x3F803F80u, 0x3F803F80u, 0x3F803F80u, 0x3F803F80u};
                }
            }
        }
    } else {
        f32x16 acc[7];
        UNROLL for (int j = 0; j < 7; ++j) acc[j] = f32x16{};
        int slot = 0;
        for (int k = 0; k < 10; ++k) {
            BARRIER();
            const lds_cp sp = L + slot * K1_STAGE;
            UNROLL for (int ks = 0; ks < 4; ++ks) {
                s16x8 a = lds128(sp + wid * 4096 + rowa[ks]);
                s16x8 b[7];
                UNROLL for (int j = 0; j < 7; ++j) b[j] = lds128(sp + K1_XB + j * 4096 + rowa[ks]);
                UNROLL for (int j = 0; j < 7; ++j) acc[j] = MFMA32(a, b[j], acc[j]);
            }
            slot = slot == 2 ? 0 : slot + 1;
        }
        const float sg = dt < 3 ? 1.0f : -1.0f;
        const int chunk0 = (dt < 3 ? 0 : 256) + mt * 16 + wid * 4;
        UNROLL for (int j = 0; j < 7; ++j) {
            if (j < ns) {
                char* dst = ws + WS_AB + ((size_t)(u0 + j) * 512 + chunk0) * 512 + r * 16 + hh * 8;
                UNROLL for (int g = 0; g < 4; ++g) {
                    u32x2 v = {pack2(acc[j][4 * g] * sg, acc[j][4 * g + 1] * sg), pack2(acc[j][4 * g + 2] * sg, acc[j][4 * g + 3] * sg)};
                    *(u32x2*)(dst + g * 512) = v;
                }
            }
        }
    }
}

constexpr int K2_GST = 16384 + 20480;
constexpr int K2_KVST = 4 * (int)KV_TILE;
constexpr int K2_KVSLOT = 11264;
constexpr int K2_KV0 = 3 * K2_GST;
constexpr int K2_LDS = K2_KV0 + 4 * K2_KVSLOT;

__global__ __launch_bounds__(768, 3) void k2_attn(const char* __restrict__ cre, const char* __restrict__ cim,
                                                   const char* __restrict__ x, const char* __restrict__ gw,
                                                   const char* __restrict__ wo, char* __restrict__ ws) {
    extern __shared__ __attribute__((aligned(16))) char smem[];
    const int tid = threadIdx.x, lane = tid & 63;
    const int wid = __builtin_amdgcn_readfirstlane(tid >> 6);
    const int xcd = blockIdx.x & 7, idx = blockIdx.x >> 3;
    const int head = xcd * 4 + (idx >> 3), qt = idx & 7;
    const unsigned lds0 = (unsigned)(uintptr_t)smem;
    const lds_cp L = (lds_cp)smem;
    const int r = lane & 31, hh = lane >> 5;
    const char* kvg = ws + WS_KV + (size_t)head * KV_HEAD;

    if (wid >= 8) {
        const int gwv = wid - 8;
        const int ksp = xcd >> 1, mt = (xcd & 1) * 8 + (idx & 7), nt = idx >> 3;
        unsigned coff[4];
        UNROLL for (int i = 0; i < 4; ++i) {
            int row = (4 * gwv + i) * 8 + (lane >> 3);
            int c = (lane & 7) ^ ((row >> 1) & 7);
            coff[i] = row * 4096 + c * 16;
        }
        const char* cb = (ksp < 2 ? cre : cim) + (size_t)mt * 128 * 4096 + (ksp & 1) * 2048;
        const char* ab = ws + WS_AB + ((size_t)(nt * 5) * 512 + ksp * 128) * 512 + gwv * 1024;
        const unsigned lin = lane * 16;
        auto issue = [&](int st, int gslot, int kslot) {
            const char* cs = cb + st * 128;
            const char* as = ab + st * 4096;
            unsigned l = lds0 + gslot * K2_GST;
            UNROLL for (int i = 0; i < 4; ++i) dma16<0>(coff[i], cs, l + (4 * gwv + i) * 1024);
            UNROLL for (int i = 0; i < 5; ++i) dma16<0>(lin, as + (size_t)i * 512 * 512, l + 16384 + i * 4096 + gwv * 1024);
            const char* ks = kvg + (size_t)st * K2_KVST + gwv * 3072;
            unsigned lk = lds0 + K2_KV0 + kslot * K2_KVSLOT + gwv * 3072;
            dma16<0>(lin, ks, lk); dma16<1024>(lin, ks, lk);
            if (gwv < 3) dma16<2048>(lin, ks, lk);
        };
        issue(0, 0, 0); issue(1, 1, 1);
        {
            const int row = blockIdx.x * 8 + gwv * 2 + hh;
            const u32x2* xp = (const u32x2*)(x + (size_t)row * 1280 + r * 40);
            const u32x2* gp = (const u32x2*)(gw + r * 40);
            float s = 0.f;
            UNROLL for (int i = 0; i < 5; ++i) {
                u32x2 a = xp[i], b = gp[i];
                s += bflo(a[0]) * bflo(b[0]) + bfhi(a[0]) * bfhi(b[0]) + bflo(a[1]) * bflo(b[1]) + bfhi(a[1]) * bfhi(b[1]);
            }
            UNROLL for (int o = 16; o; o >>= 1) s += __shfl_xor(s, o);
            if (r == 0) ((float*)(ws + WS_G))[row] = 1.0f / (1.0f + __expf(-s));
            const int t = gwv * 64 + lane;
            if (t < 200) {
                const int id = blockIdx.x * 200 + t;
                const int chunk = id / 640, c = id - chunk * 640;
                const unsigned short* wp = (const unsigned short*)wo + (size_t)chunk * 8 * 640 + c;
                u32x4 v;
                UNROLL for (int e = 0; e < 4; ++e) v[e] = (unsigned)wp[(2 * e) * 640] | ((unsigned)wp[(2 * e + 1) * 640] << 16);
                const int ctile = c / 80, cc = c - ctile * 80;
                *(u32x4*)(ws + WS_WO + ((size_t)(ctile * 80 + chunk) * 80 + cc) * 16) = v;
            }
        }
        f32x16 acc[5];
        UNROLL for (int j = 0; j < 5; ++j) acc[j] = f32x16{};
        unsigned rowa[4];
        UNROLL for (int ks = 0; ks < 4; ++ks) rowa[ks] = gwv * 4096 + r * 128 + (((2 * ks + hh) ^ ((r >> 1) & 7)) << 4);
        const unsigned aoff = 16384 + hh * 512 + r * 16;
        WAITV(0);
        int gslot = 0;
        for (int j = 0; j < 16; ++j) {
            if (j > 0) { if (j < 15) { if (gwv < 3) { WAITV(12); } else { WAITV(11); } } else { WAITV(0); } }
            BARRIER();
            if (j + 2 < 16) issue(j + 2, gslot == 0 ? 2 : gslot - 1, (j + 2) & 3);
            const lds_cp sp = L + gslot * K2_GST;
            UNROLL for (int ks = 0; ks < 4; ++ks) {
                s16x8 cf = lds128(sp + rowa[ks]);
                s16x8 af[5];
                UNROLL for (int u = 0; u < 5; ++u) af[u] = lds128(sp + aoff + u * 4096 + ks * 1024);
                UNROLL for (int u = 0; u < 5; ++u) acc[u] = MFMA32(af[u], cf, acc[u]);
            }
            gslot = gslot == 2 ? 0 : gslot + 1;
        }
        char* yb = ws + WS_Y + (size_t)ksp * YP + (size_t)(mt * 128 + gwv * 32 + r) * 8;
        UNROLL for (int u = 0; u < 5; ++u)
            UNROLL for (int g = 0; g < 4; ++g) {
                u32x2 v = {pack2(acc[u][4 * g], acc[u][4 * g + 1]), pack2(acc[u][4 * g + 2], acc[u][4 * g + 3])};
                *(u32x2*)(yb + (size_t)((nt * 5 + u) * 8 + 2 * g + hh) * (2048 * 8)) = v;
            }
        return;
    }

    const int qb = qt * 8 + wid;
    const int q4 = (lane & 15) >> 2, p4 = lane & 3, blk = (lane >> 4) & 1;
    const int colb = blk * 32 + ((p4 & 1) << 4) + ((p4 >> 1) << 3);
    const unsigned k1off = (8 * hh + q4) * 64 + colb;
    const unsigned k2off = hh == 0 ? (16 + q4) * 64 + colb : 1280 + 640;
    const unsigned voff = r < 20 ? 1280 + hh * 320 + r * 16 : (r < 28 ? 1280 + 640 : 1280 + hh * 320 + 64);
    s16x8 q1; s16x4 q2;
    {
        const unsigned ql = K2_KV0 + 3 * K2_KVSLOT + wid * 1280;
        const char* qg = ws + WS_Q + (size_t)(head * 64 + qb) * QF_TILE;
        dma16<0>(lane * 16, qg, lds0 + ql);
        if (lane < 16) dma16<1024>(lane * 16, qg, lds0 + ql);
        WAITV(0);
        q1 = cat8(ldstr(L + ql + k1off), ldstr(L + ql + k1off + 256));
        q2 = ldstr(L + ql + (16 + q4) * 64 + colb);
        if (hh) q2 = s16x4{0, 0, 0, 0};
    }
    f32x16 o = f32x16{};
    const f32x16 zero16 = f32x16{};
    BARRIER();
    {
        const lds_cp kp = L + K2_KV0;
        s16x8 k1 = cat8(ldstr(kp + k1off), ldstr(kp + k1off + 256));
        s16x4 k2 = ldstr(kp + k2off);
        f32x16 s = MFMA32K8(k2, q2, zero16);
        s = MFMA32(k1, q1, s);
        float m = s[0];
        UNROLL for (int i = 1; i < 16; ++i) m = fmaxf(m, s[i]);
        m = fmaxf(m, __shfl_xor(m, 32));
        if (hh) q2[0] = (short)(pack2(-m, 0.f) & 0xFFFF);
    }
    for (int j = 0; j < 16; ++j) {
        if (j > 0) BARRIER();
        const lds_cp kp = L + K2_KV0 + (j & 3) * K2_KVSLOT;
        UNROLL for (int i = 0; i < 4; ++i) {
            const lds_cp tp = kp + i * (int)KV_TILE;
            s16x8 k1 = cat8(ldstr(tp + k1off), ldstr(tp + k1off + 256));
            s16x4 k2 = ldstr(tp + k2off);
            s16x8 v0 = lds128(tp + voff), v1 = lds128(tp + voff + 656);
            f32x16 s = MFMA32K8(k2, q2, zero16);
            s = MFMA32(k1, q1, s);
            u32x4 p0, p1;
            UNROLL for (int e = 0; e < 4; ++e) p0[e] = pack2(__builtin_amdgcn_exp2f(s[2 * e]), __builtin_amdgcn_exp2f(s[2 * e + 1]));
            UNROLL for (int e = 0; e < 4; ++e) p1[e] = pack2(__builtin_amdgcn_exp2f(s[8 + 2 * e]), __builtin_amdgcn_exp2f(s[8 + 2 * e + 1]));
            o = MFMA32(v0, __builtin_bit_cast(s16x8, p0), o);
            o = MFMA32(v1, __builtin_bit_cast(s16x8, p1), o);
        }
    }
    float l = hh ? o[8] : o[12];
    bool ok = (l > 1e-18f) && (l < 1e30f);
    UNROLL for (int i = 0; i < 12; ++i) ok = ok && (fabsf(o[i]) < 3e38f);
    if (!__all(ok)) {
        if (hh) q2[0] = 0;
        float m = -3e38f;
        o = f32x16{};
        for (int t = 0; t < 64; ++t) {
            const char* tp = kvg + (size_t)t * KV_TILE;
            const int pos = (r & 16) + ((r & 4) << 1) + ((r & 8) >> 1) + (r & 3);
            s16x8 k1; s16x4 k2;
            UNROLL for (int e = 0; e < 8; ++e) k1[e] = *(const short*)(tp + (8 * hh + e) * 64 + pos * 2);
            UNROLL for (int e = 0; e < 4; ++e) k2[e] = *(const short*)(tp + (16 + e) * 64 + pos * 2);
            s16x8 v0 = *(const s16x8*)(tp + voff), v1 = *(const s16x8*)(tp + voff + 656);
            f32x16 s = MFMA32K8(k2, q2, zero16);
            s = MFMA32(k1, q1, s);
            float mx = s[0];
            UNROLL for (int i = 1; i < 16; ++i) mx = fmaxf(mx, s[i]);
            mx = fmaxf(mx, __shfl_xor(mx, 32));
            const float mn = fmaxf(m, mx);
            const float al = __builtin_amdgcn_exp2f(m - mn);
            m = mn;
            UNROLL for (int i = 0; i < 16; ++i) o[i] *= al;
            u32x4 p0, p1;
            UNROLL for (int e = 0; e < 4; ++e) p0[e] = pack2(__builtin_amdgcn_exp2f(s[2 * e] - m), __builtin_amdgcn_exp2f(s[2 * e + 1] - m));
            UNROLL for (int e = 0; e < 4; ++e) p1[e] = pack2(__builtin_amdgcn_exp2f(s[8 + 2 * e] - m), __builtin_amdgcn_exp2f(s[8 + 2 * e + 1] - m));
            o = MFMA32(v0, __builtin_bit_cast(s16x8, p0), o);
            o = MFMA32(v1, __builtin_bit_cast(s16x8, p1), o);
        }
        l = hh ? o[8] : o[12];
    }
    const float rl = 1.0f / l;
    char* ob = ws + WS_O + (size_t)(qb * 2 + (r >> 4)) * 20480 + (r & 15) * 16;
    UNROLL for (int g = 0; g < 3; ++g) {
        const int pc = head * 5 + 2 * g + hh;
        if (g < 2 || hh == 0) {
            u32x2 v = {pack2(o[4 * g] * rl, o[4 * g + 1] * rl), pack2(o[4 * g + 2] * rl, o[4 * g + 3] * rl)};
            *(u32x2*)(ob + (pc >> 1) * 256 + (pc & 1) * 8) = v;
        }
    }
}

constexpr int K3_LDS = 102400;

__global__ __launch_bounds__(256, 1) void k3_out(const char* __restrict__ ws, char* __restrict__ out) {
    extern __shared__ __attribute__((aligned(16))) char smem[];
    const int tid = threadIdx.x, lane = tid & 63;
    const int wid = __builtin_amdgcn_readfirstlane(tid >> 6);
    const int xcd = blockIdx.x & 7, idx = blockIdx.x >> 3;
    const int tt = xcd * 4 + (idx >> 3), ctile = idx & 7;
    const unsigned lds0 = (unsigned)(uintptr_t)smem;
    const lds_cp L = (lds_cp)smem;
    {
        const char* wsrc = ws + WS_WO + (size_t)ctile * 102400 + wid * 1024;
        for (int i = 0; i < 25; ++i) dma16<0>(lane * 16, wsrc + i * 4096, lds0 + wid * 1024 + i * 4096);
    }
    const int tb = tt * 4 + wid;
    const s16x8* op = (const s16x8*)(ws + WS_O + (size_t)tb * 20480) + lane;
    s16x8 of[20];
    UNROLL for (int ks = 0; ks < 20; ++ks) of[ks] = op[ks * 64];
    const int t = tb * 16 + (lane & 15), kg = lane >> 4;
    u32x2 y[5][4];
    UNROLL for (int u = 0; u < 5; ++u)
        UNROLL for (int p = 0; p < 4; ++p)
            y[u][p] = *(const u32x2*)(ws + WS_Y + (size_t)p * YP + ((size_t)(ctile * 20 + u * 4 + kg) * 2048 + t) * 8);
    const float g = ((const float*)(ws + WS_G))[t];
    WAITV(0);
    BARRIER();
    f32x4 acc[5];
    UNROLL for (int u = 0; u < 5; ++u) acc[u] = f32x4{};
    const lds_cp wp = L + kg * 1280 + (lane & 15) * 16;
    UNROLL for (int ks = 0; ks < 20; ++ks)
        UNROLL for (int u = 0; u < 5; ++u) acc[u] = MFMA16(lds128(wp + ks * 5120 + u * 256), of[ks], acc[u]);
    const float h = 1.0f - g;
    char* dst = out + (size_t)t * 1280 + ctile * 160 + kg * 8;
    UNROLL for (int u = 0; u < 5; ++u) {
        float y0 = (bflo(y[u][0][0]) + bflo(y[u][1][0])) + (bflo(y[u][2][0]) + bflo(y[u][3][0]));
        float y1 = (bfhi(y[u][0][0]) + bfhi(y[u][1][0])) + (bfhi(y[u][2][0]) + bfhi(y[u][3][0]));
        float y2 = (bflo(y[u][0][1]) + bflo(y[u][1][1])) + (bflo(y[u][2][1]) + bflo(y[u][3][1]));
        float y3 = (bfhi(y[u][0][1]) + bfhi(y[u][1][1])) + (bfhi(y[u][2][1]) + bfhi(y[u][3][1]));
        u32x2 v = {pack2(g * acc[u][0] + h * y0, g * acc[u][1] + h * y1), pack2(g * acc[u][2] + h * y2, g * acc[u][3] + h * y3)};
        *(u32x2*)(dst + u * 32) = v;
    }
}

extern "C" void kernel_launch(void* const* d_in, const int* in_sizes, int n_in, void* d_out, int out_size,
                              void* d_ws, size_t ws_size, hipStream_t stream) {
    const char* x = (const char*)d_in[0];
    const char* wqkv = (const char*)d_in[1];
    const char* wo = (const char*)d_in[2];
    const char* cre = (const char*)d_in[4];
    const char* cim = (const char*)d_in[5];
    const char* wr = (const char*)d_in[6];
    const char* wi = (const char*)d_in[7];
    const char* gw = (const char*)d_in[8];
    char* ws = (char*)d_ws;
    (void)hipFuncSetAttribute((const void*)k1_proj, hipFuncAttributeMaxDynamicSharedMemorySize, K1_LDS);
    (void)hipFuncSetAttribute((const void*)k2_attn, hipFuncAttributeMaxDynamicSharedMemorySize, K2_LDS);
    (void)hipFuncSetAttribute((const void*)k3_out, hipFuncAttributeMaxDynamicSharedMemorySize, K3_LDS);
    k1_proj<<<256, 512, K1_LDS, stream>>>(x, wqkv, wr, wi, ws);
    k2_attn<<<256, 768, K2_LDS, stream>>>(cre, cim, x, gw, wo, ws);
    k3_out<<<256, 256, K3_LDS, stream>>>(ws, (char*)d_out);
}
```

```cpp
#include <hip/hip_runtime.h>
#include <hip/hip_bf16.h>
#include <stdint.h>

typedef short s16x8 __attribute__((ext_vector_type(8)));
typedef short s16x4 __attribute__((ext_vector_type(4)));
typedef float f32x16 __attribute__((ext_vector_type(16)));
typedef float f32x8 __attribute__((ext_vector_type(8)));
typedef float f32x4 __attribute__((ext_vector_type(4)));
typedef unsigned u32x4 __attribute__((ext_vector_type(4)));
typedef unsigned u32x2 __attribute__((ext_vector_type(2)));
typedef __bf16 bf16x2_t __attribute__((ext_vector_type(2)));
typedef __attribute__((address_space(3))) const char* lds_cp;
typedef __attribute__((address_space(3))) char* lds_p;
typedef __amdgpu_buffer_rsrc_t rsrc_t;

#define DI __device__ __forceinline__
#define UNROLL _Pragma("unroll")
#define NOUNROLL _Pragma("unroll 1")
#define MFMA32(a, b, c) __builtin_amdgcn_mfma_f32_32x32x16_bf16((a), (b), (c), 0, 0, 0)
#define MFMA32K8(a, b, c) __builtin_amdgcn_mfma_f32_32x32x8bf16_1k((a), (b), (c), 0, 0, 0)
#define MFMA16(a, b, c) __builtin_amdgcn_mfma_f32_16x16x32_bf16((a), (b), (c), 0, 0, 0)
#define SBAR() __builtin_amdgcn_sched_barrier(0)
#define PIN(x) asm volatile("" : "+v"(x))
#define EX2(x) __builtin_amdgcn_exp2f(x)
#define SC1 16

constexpr unsigned CH = 2048 * 16;
constexpr unsigned WS_QK = 0;
constexpr unsigned WS_O = 160 * CH;
constexpr unsigned WS_Y = WS_O + 80 * CH;
constexpr unsigned ABROW = 640 * 16;
constexpr unsigned WS_AB = WS_Y + 320 * CH;
constexpr unsigned VROW = 10240;
constexpr unsigned WS_VT = WS_AB + 512 * ABROW;
constexpr unsigned WS_WOT = WS_VT + 256 * VROW;
constexpr unsigned WS_G = WS_WOT + 80 * ABROW;
constexpr unsigned WS_F1 = WS_G + 8192;
constexpr unsigned WS_F2 = WS_F1 + 4096;
constexpr unsigned WS_F3 = WS_F2 + 4096;
constexpr unsigned WS_F4 = WS_F3 + 4096;
constexpr unsigned WS_EP = WS_F4 + 4096;
constexpr unsigned WS_ONE = WS_EP + 4096;
constexpr unsigned WS_END = WS_ONE + 2048 * 128;
constexpr unsigned OOB = 0x80000000u;

constexpr int P1_STAGE = 32768;
constexpr int SQ_STAGE = 36864;
constexpr int GW_LDS = 15360;
constexpr int LDS_EX = 2 * SQ_STAGE;
constexpr int LDS_X9 = 4 * P1_STAGE;
constexpr int LDS_GW = LDS_X9 + 20480;
constexpr int LDS_WT = LDS_GW + 2048;
constexpr int LDS_FL = LDS_WT + 4096;
constexpr int LDS_SINK = LDS_FL + 256;
constexpr int LDS_BYTES = LDS_SINK + 256;
static_assert(LDS_BYTES <= 163840 && LDS_FL >= 4 * SQ_STAGE && LDS_FL >= LDS_EX + 24 * 2688, "lds");
constexpr int FL_FULL = LDS_FL, FL_GO = LDS_FL + 32, FL_EX = LDS_FL + 48, FL_SFULL = LDS_FL + 80,
              FL_SFREE = LDS_FL + 96, FL_FIN = LDS_FL + 112, FL_RED = LDS_FL + 128, FL_FREE = LDS_FL + 144;
#define PUBZ 0
#define AABL 0
#define SABL 0
#define PFX 1
#define CPF 1
#define CODE_LINES 330

#define STAMP(i) do { } while (0)
#define TACC(a) do { } while (0)
#define TDECL() do { } while (0)
#define TOUT(i) do { } while (0)
#define STAMP2(i) do { } while (0)

DI unsigned pack2(float a, float b) {
    bf16x2_t v = {(__bf16)a, (__bf16)b};
    return __builtin_bit_cast(unsigned, v);
}
DI float bflo(unsigned u) { return __uint_as_float(u << 16); }
DI float bfhi(unsigned u) { return __uint_as_float(u & 0xFFFF0000u); }
DI s16x8 as8(u32x4 v) { return __builtin_bit_cast(s16x8, v); }
DI s16x4 as4(u32x2 v) { return __builtin_bit_cast(s16x4, v); }

DI u32x4 mkdesc(const void* p, unsigned bytes) {
    const unsigned long long a = (unsigned long long)p;
    return u32x4{(unsigned)a, (unsigned)(a >> 32) & 0xFFFFu, bytes, 0x00020000u};
}
DI void dma16(unsigned voff, u32x4 desc, unsigned soff, unsigned ldsaddr) {
    unsigned keep;
    asm volatile("s_mov_b32 %0, m0\n\ts_mov_b32 m0, %4\n\ts_nop 0\n\tbuffer_load_dwordx4 %1, %2, %3 offen lds\n\ts_mov_b32 m0, %0"
                 : "=&s"(keep) : "v"(voff), "s"(desc), "s"(soff), "s"(ldsaddr) : "memory");
}
DI void dma16s(unsigned voff, u32x4 desc, unsigned soff, unsigned ldsaddr) {
    unsigned keep;
    asm volatile("s_mov_b32 %0, m0\n\ts_mov_b32 m0, %4\n\ts_nop 0\n\tbuffer_load_dwordx4 %1, %2, %3 offen sc1 lds\n\ts_mov_b32 m0, %0"
                 : "=&s"(keep) : "v"(voff), "s"(desc), "s"(soff), "s"(ldsaddr) : "memory");
}
DI void dma4(unsigned voff, u32x4 desc, unsigned soff, unsigned ldsaddr) {
    unsigned keep;
    asm volatile("s_mov_b32 %0, m0\n\ts_mov_b32 m0, %4\n\ts_nop 0\n\tbuffer_load_dword %1, %2, %3 offen sc1 lds\n\ts_mov_b32 m0, %0"
                 : "=&s"(keep) : "v"(voff), "s"(desc), "s"(soff), "s"(ldsaddr) : "memory");
}
#define WAITV(n) asm volatile("s_waitcnt vmcnt(" #n ")" ::: "memory")
#define WAITL() asm volatile("s_waitcnt lgkmcnt(0)" ::: "memory")
#define BARRIER() asm volatile("s_waitcnt lgkmcnt(0)\n\ts_barrier" ::: "memory")
#define BARRIER_NOWAIT() asm volatile("s_barrier" ::: "memory")

DI s16x8 lds128(lds_cp p) { return *(const __attribute__((address_space(3))) s16x8*)p; }
DI f32x4 ldsf4(lds_cp p) { return *(const __attribute__((address_space(3))) f32x4*)p; }
DI void stsf4(lds_p p, f32x4 v) { *(__attribute__((address_space(3))) f32x4*)p = v; }
DI s16x4 ldstr(lds_cp p) {
    return __builtin_bit_cast(s16x4, __builtin_amdgcn_ds_read_tr16_b64_v4i16((__attribute__((address_space(3))) s16x4*)p));
}
DI s16x8 cat8(s16x4 a, s16x4 b) { return __builtin_shufflevector(a, b, 0, 1, 2, 3, 4, 5, 6, 7); }
DI u32x4 ld128(rsrc_t r, unsigned voff, unsigned soff) { return __builtin_amdgcn_raw_buffer_load_b128(r, voff, soff, SC1); }
DI u32x2 ld64(rsrc_t r, unsigned voff, unsigned soff) { return __builtin_amdgcn_raw_buffer_load_b64(r, voff, soff, SC1); }
DI unsigned ld32(rsrc_t r, unsigned voff, unsigned soff) { return __builtin_amdgcn_raw_buffer_load_b32(r, voff, soff, SC1); }
DI void st128(rsrc_t r, u32x4 v, unsigned voff, unsigned soff) { __builtin_amdgcn_raw_buffer_store_b128(v, r, voff + soff, 0, SC1); }
DI void st64(rsrc_t r, u32x2 v, unsigned voff, unsigned soff) { __builtin_amdgcn_raw_buffer_store_b64(v, r, voff, soff, SC1); }
DI void st32(rsrc_t r, unsigned v, unsigned voff, unsigned soff) { __builtin_amdgcn_raw_buffer_store_b32(v, r, voff, soff, SC1); }

constexpr unsigned SPIN_CAP = 200000;
DI void publish(rsrc_t w, unsigned flag_off, unsigned epoch, int lane) {
    WAITV(0);
    if (lane == 0) st32(w, epoch, flag_off, 0);
}
DI void wait_flags3(rsrc_t w, unsigned o0, unsigned o1, unsigned o2, unsigned epoch) {
    NOUNROLL for (unsigned spins = 0; spins < SPIN_CAP; ++spins) {
        asm volatile("" ::: "memory");
        const unsigned a = ld32(w, o0, 0), b = ld32(w, o1, 0), c = ld32(w, o2, 0);
        if (__all(a == epoch && b == epoch && c == epoch)) break;
        __builtin_amdgcn_s_sleep(2);
    }
    asm volatile("" ::: "memory");
}
DI void wait_flags1(rsrc_t w, unsigned o0, unsigned epoch) {
    NOUNROLL for (unsigned spins = 0; spins < SPIN_CAP; ++spins) {
        asm volatile("" ::: "memory");
        const unsigned a = ld32(w, o0, 0);
        if (__all(a == epoch)) break;
        __builtin_amdgcn_s_sleep(2);
    }
    asm volatile("" ::: "memory");
}
DI void lds_signal(lds_p p, unsigned val = 1u) {
    WAITL();
    *(volatile __attribute__((address_space(3))) unsigned*)p = val;
}
DI void lds_post(lds_p p, unsigned val) { *(volatile __attribute__((address_space(3))) unsigned*)p = val; }
DI unsigned lds_peek(lds_cp p) { return *(const volatile __attribute__((address_space(3))) unsigned*)p; }
DI void lds_wait(lds_cp p, unsigned val = 1u) {
    NOUNROLL for (unsigned spins = 0; spins < 4 * SPIN_CAP; ++spins) {
        const unsigned v = *(const volatile __attribute__((address_space(3))) unsigned*)p;
        if (__all(v >= val)) break;
        __builtin_amdgcn_s_sleep(1);
    }
    asm volatile("" ::: "memory");
}

__global__ __launch_bounds__(512, 2) void fused(const char* __restrict__ x, const char* __restrict__ wqkv,
                                                 const char* __restrict__ wo, const char* __restrict__ cre,
                                                 const char* __restrict__ cim, const char* __restrict__ wr,
                                                 const char* __restrict__ wi, const char* __restrict__ gw,
                                                 char* ws, char* __restrict__ out) {
    extern __shared__ __attribute__((aligned(16))) char smem[];
    unsigned long long pc; asm volatile("s_getpc_b64 %0" : "=s"(pc));
    const int tid = threadIdx.x;
    const int wid = __builtin_amdgcn_readfirstlane(tid >> 6);
    const int gwv = wid & 3;
    const int xcd = blockIdx.x & 7, idx = blockIdx.x >> 3;
    const unsigned lds0 = (unsigned)(uintptr_t)smem;
    const lds_cp L = (lds_cp)smem;
    const lds_p LW = (lds_p)smem;
    const u32x4 dx = mkdesc(x, 2048 * 1280);
    const int mt = (xcd & 1) * 8 + (idx & 7), nt = (xcd >> 1) * 4 + (idx >> 3);
    const u32x4 dw = mkdesc(wqkv, 640 * 3840);
    const rsrc_t W = __builtin_amdgcn_make_buffer_rsrc(ws, 0, WS_END, 0x00020000);
    const unsigned oneoff = WS_ONE + (blockIdx.x * 8 + wid) * 128;
    unsigned epoch;
    {
    const int lane = tid & 63;
    unsigned roff[2];
    UNROLL for (int i = 0; i < 2; ++i) roff[i] = (lane >> 3) * 1280 + (((lane & 7) ^ ((4 * i + (lane >> 4)) & 7)) << 4);
    unsigned woff[2];
    UNROLL for (int i = 0; i < 2; ++i) woff[i] = nt == 15 ? OOB : (lane >> 3) * 3840 + (((lane & 7) ^ (((lane >> 4) & 1) << 2) ^ (i << 1)) << 4);
#define P1_DX(st, p, par) dma16(roff[par], dx, (mt * 128 + (p) * 8) * 1280 + (st) * 128, lds0 + ((st) & 3) * P1_STAGE + (p) * 1024)
#define P1_DW(st, p, par) dma16(woff[par], dw, ((st) * 64 + ((p) & 7) * 8) * 3840 + nt * 256 + ((p) >> 3) * 128, lds0 + ((st) & 3) * P1_STAGE + 16384 + (p) * 1024)
    P1_DX(0, 2 * wid, 0); P1_DW(0, 2 * wid, 0); P1_DX(0, 2 * wid + 1, 1); P1_DW(0, 2 * wid + 1, 1);
    STAMP(0);
    const int hh = lane >> 5;
    const int l15 = lane & 15, kg = lane >> 4;
    const unsigned fl4 = (lane & 3) * 4;
    const u32x4 dh = mkdesc(nt < 8 ? wr : wi, 640 * 1280);
#define GET_EPOCH() do { epoch = ld32(W, WS_EP, 0) + 1u; if (lane < 4) st32(W, 0x3F803F80u, oneoff + lane * 4, 0); } while (0)
#define WH_ADDR(q) ((q) < 32 ? 2 * P1_STAGE + (q) * 1024 : (q) < 48 ? 3 * P1_STAGE + 16384 + ((q) - 32) * 1024 : \
                    (q) < 68 ? LDS_X9 + ((q) - 48) * 1024 : (q) < 84 ? 16384 + ((q) - 68) * 1024 : P1_STAGE + 16384 + ((q) - 84) * 1024)

    if (wid >= 4) {
#define P1_ISSUE(st) do { UNROLL for (int i = 0; i < 4; ++i) { P1_DX(st, 4 * gwv + i, i & 1); P1_DW(st, 4 * gwv + i, i & 1); } } while (0)
#define P1_DH(q, par) do { const int q_ = (q), c_ = q_ / 10, p_ = q_ - c_ * 10; \
            dma16(roff[par], dh, ((nt & 7) * 80 + p_ * 8) * 1280 + c_ * 128, lds0 + WH_ADDR(q_)); } while (0)
        P1_ISSUE(1); P1_ISSUE(2); P1_ISSUE(3);
        WAITV(24); BARRIER_NOWAIT();
        NOUNROLL for (int k = 1; k < 7; ++k) { WAITV(16); BARRIER_NOWAIT(); P1_ISSUE(k + 3); }
        WAITV(16); BARRIER_NOWAIT();
        UNROLL for (int i = 0; i < 8; ++i) P1_DH(gwv * 8 + i, i & 1);
        WAITV(16); BARRIER_NOWAIT();
        UNROLL for (int i = 0; i < 8; ++i) P1_DH(32 + gwv * 8 + i, i & 1);
        WAITV(16); BARRIER_NOWAIT();
        GET_EPOCH();
        WAITV(0); BARRIER_NOWAIT();
        UNROLL for (int i = 0; i < 10; ++i) { const int q = 64 + gwv * 10 + i; P1_DH(q < 100 ? q : 98 + (i & 1), i & 1); }
        WAITV(0);
        lds_post(LW + FL_FULL + gwv * 4, 1);
        STAMP(1);
    } else {
        const unsigned sink = lds0 + LDS_SINK;
        const int pi = xcd * 4 + wid, pst = pi / 5, pu = pi - pst * 5;
        if (PFX) {
            const unsigned b = blockIdx.x;
            if (wid == 0) { dma4(lane * 128, dx, b * 10240, sink); dma4(lane < 16 ? lane * 128 : OOB, dx, b * 10240 + 8192, sink); }
            if (wid == 1) { dma4(lane * 128, dw, b * 9600, sink); dma4(lane < 11 ? lane * 128 : OOB, dw, b * 9600 + 8192, sink); }
            if (wid == 2) { dma4(OOB, dx, 0, sink); dma4(OOB, dx, 0, sink); }
        } else if (wid < 3) { dma4(OOB, dx, 0, sink); dma4(OOB, dx, 0, sink); }
        if (wid == 3) {
            dma4(lane < 12 ? lane * 128 : OOB, mkdesc((const void*)(pc & ~127ull), CODE_LINES * 128), idx * 1536, sink);
            dma16(nt == 15 ? lane * 16 : OOB, mkdesc(gw, 1280), 0, lds0 + LDS_GW);
        }
        if (tid < 64) *(__attribute__((address_space(3))) unsigned*)(LW + LDS_FL + tid * 4) = 0u;
        const int wm = wid & 1, wn = wid >> 1;
        unsigned ra[2];
        UNROLL for (int ks = 0; ks < 2; ++ks) ra[ks] = l15 * 128 + (((4 * ks + kg) ^ ((l15 >> 1) & 7)) << 4);
        const unsigned xk = wm * 8192 + wn * 4096, xo = wm * 8192 + (wn ^ 1) * 4096;
        s16x8 xs[10][2][2];
#define WT_LOAD() dma16((lane >> 1) * 1280 + (lane & 1) * 16, mkdesc(wo, 640 * 1280), \
                  pi < 25 ? ((idx >> 3) * 160 + pst * 32) * 1280 + (idx & 7) * 160 + pu * 32 : OOB, lds0 + LDS_WT + wid * 1024)
#define WT_STORE() do { const lds_cp tp_ = L + LDS_WT + wid * 1024 + (8 * kg + (l15 >> 2)) * 32 + (lane & 3) * 8; \
            st128(W, __builtin_bit_cast(u32x4, cat8(ldstr(tp_), ldstr(tp_ + 128))), \
                  pi < 25 ? WS_WOT + ((idx & 7) * 80 + (idx >> 3) * 20 + pst * 4 + kg) * 1280 + (pu * 16 + l15) * 16 : OOB, 0); } while (0)
        unsigned f1;
        if (nt == 15) {
            const int m0 = mt * 128 + wm * 64 + wn * 32;
            if (wid == 3) dma16(lane * 16, mkdesc(gw, 1280), 1024, lds0 + LDS_GW + 1024);
            GET_EPOCH();
            WAITV(0);
            {
                const unsigned o0 = (mt * 4 + wid) * 12800, v1 = lane < 36 ? lane * 128 : OOB;
                dma4(lane * 128, mkdesc(wr, 640 * 1280), o0, sink); dma4(v1, mkdesc(wr, 640 * 1280), o0 + 8192, sink);
                dma4(lane * 128, mkdesc(wi, 640 * 1280), o0, sink); dma4(v1, mkdesc(wi, 640 * 1280), o0 + 8192, sink);
                dma4(lane * 128, mkdesc(wo, 640 * 1280), o0, sink); dma4(v1, mkdesc(wo, 640 * 1280), o0 + 8192, sink);
            }
            float sum[2] = {0.f, 0.f};
            UNROLL for (int k = 0; k < 10; ++k) {
                BARRIER();
                UNROLL for (int ks = 0; ks < 2; ++ks) {
                    const u32x4 gv = __builtin_bit_cast(u32x4, lds128(L + LDS_GW + k * 128 + (4 * ks + kg) * 16));
                    UNROLL for (int h = 0; h < 2; ++h) {
                        xs[k][ks][h] = lds128(L + (k & 3) * P1_STAGE + xk + h * 2048 + ra[ks]);
                        const u32x4 xv = __builtin_bit_cast(u32x4, xs[k][ks][h]);
                        UNROLL for (int e = 0; e < 4; ++e)
                            { const unsigned xe = xv[e], ge = gv[e]; sum[h] = __builtin_amdgcn_fdot2_f32_bf16(__builtin_bit_cast(bf16x2_t, xe), __builtin_bit_cast(bf16x2_t, ge), sum[h], false); }
                    }
                }
                SBAR();
            }
            UNROLL for (int h = 0; h < 2; ++h) { sum[h] += __shfl_xor(sum[h], 16); sum[h] += __shfl_xor(sum[h], 32); }
            const float sm = kg & 1 ? sum[1] : sum[0];
            if (lane < 32) st32(W, __float_as_uint(1.0f / (1.0f + __expf(-sm))), WS_G + (m0 + lane) * 4, 0);
            f1 = WS_F1 + (30 * 32 + mt * 4 + wm * 2 + wn) * 4;
        } else {
            const int q = l15 >> 2;
            unsigned trb[4];
            UNROLL for (int j = 0; j < 4; ++j)
                trb[j] = 16384 + wn * 8192 + (8 * kg + q) * 128 + ((j ^ (((q >> 1) & 1) << 1) ^ (kg & 1)) << 5) + 8 * (lane & 3);
            f32x4 acc[4][4];
            UNROLL for (int i = 0; i < 4; ++i) UNROLL for (int j = 0; j < 4; ++j) acc[i][j] = f32x4{};
            s16x8 xt[2][2]; s16x4 wv[4][2];
            GET_EPOCH();
            WAITV(2);
#define P1_RW(z, j) do { const lds_cp p_ = L + (((z) >> 1) & 3) * P1_STAGE + trb[j] + ((z) & 1) * 4096; wv[j][0] = ldstr(p_); wv[j][1] = ldstr(p_ + 512); } while (0)
#define P1_RK(z, h) xs[(z) >> 1][(z) & 1][h] = lds128(L + (((z) >> 1) & 3) * P1_STAGE + xk + (h) * 2048 + ra[(z) & 1])
#define P1_RO(z, h) xt[(z) & 1][h] = lds128(L + (((z) >> 1) & 3) * P1_STAGE + xo + (h) * 2048 + ra[(z) & 1])
#define P1_XF(z, i) ((i) < 2 ? xs[(z) >> 1][(z) & 1][(i) & 1] : xt[(z) & 1][(i) & 1])
#define P1_LOOP(MF) \
            BARRIER(); \
            STAMP2(11); \
            P1_RK(0, 0); P1_RW(0, 0); P1_RK(0, 1); P1_RO(0, 0); P1_RO(0, 1); P1_RW(0, 1); P1_RW(0, 2); P1_RW(0, 3); SBAR(); \
            UNROLL for (int z = 0; z < 20; ++z) { \
                UNROLL for (int j = 0; j < 4; ++j) { \
                    MF(z, 0, j); SBAR(); \
                    if (j == 0 && (z & 1) && z < 19) { BARRIER(); } \
                    MF(z, 1, j); SBAR(); \
                    if (z < 19) { if (j == 0) P1_RK(z + 1, 0); if (j == 1) P1_RK(z + 1, 1); if (j == 2) P1_RO(z + 1, 0); if (j == 3) P1_RO(z + 1, 1); SBAR(); } \
                    MF(z, 2, j); SBAR(); \
                    MF(z, 3, j); SBAR(); \
                    if (z < 19) { P1_RW(z + 1, j); SBAR(); } \
                } \
            }
#define P1_MFQ(z, i, j) acc[i][j] = MFMA16(cat8(wv[j][0], wv[j][1]), P1_XF(z, i), acc[i][j])
#define P1_MFV(z, i, j) acc[i][j] = MFMA16(P1_XF(z, i), cat8(wv[j][0], wv[j][1]), acc[i][j])
            if (nt < 10) {
                P1_LOOP(P1_MFQ)
                STAMP(1);
                const float sc = nt < 5 ? 0.32259889079f : 1.0f;
                UNROLL for (int i = 0; i < 4; i += 2) {
                    const unsigned base = WS_QK + (nt * 16 + wn * 8 + (kg >> 1)) * CH + (mt * 128 + wm * 64 + ((i >> 1) ^ wn) * 32 + (kg & 1) * 16 + l15) * 16;
                    UNROLL for (int j = 0; j < 4; ++j) {
                        unsigned a0 = pack2(acc[i][j][0] * sc, acc[i][j][1] * sc), a1 = pack2(acc[i][j][2] * sc, acc[i][j][3] * sc);
                        unsigned b0 = pack2(acc[i + 1][j][0] * sc, acc[i + 1][j][1] * sc), b1 = pack2(acc[i + 1][j][2] * sc, acc[i + 1][j][3] * sc);
                        auto s0 = __builtin_amdgcn_permlane16_swap(a0, b0, false, false);
                        auto s1 = __builtin_amdgcn_permlane16_swap(a1, b1, false, false);
                        st128(W, u32x4{s0[0], s1[0], s0[1], s1[1]}, base, j * 2 * CH);
                    }
                }
            } else {
                P1_LOOP(P1_MFV)
                STAMP(1);
                UNROLL for (int i = 0; i < 4; ++i) {
                    const unsigned base = WS_VT + ((mt * 8 + wm * 4 + ((i >> 1) ^ wn) * 2 + (i & 1)) * 2 + (kg & 1)) * VROW + ((nt - 10) * 128 + wn * 64 + hh * 16 + l15) * 16;
                    UNROLL for (int j = 0; j < 4; j += 2) {
                        unsigned a0 = pack2(acc[i][j][0], acc[i][j][1]), a1 = pack2(acc[i][j][2], acc[i][j][3]);
                        unsigned b0 = pack2(acc[i][j + 1][0], acc[i][j + 1][1]), b1 = pack2(acc[i][j + 1][2], acc[i][j + 1][3]);
                        auto s0 = __builtin_amdgcn_permlane32_swap(a0, b0, false, false);
                        auto s1 = __builtin_amdgcn_permlane32_swap(a1, b1, false, false);
                        st128(W, u32x4{s0[0], s1[0], s0[1], s1[1]}, base, j * 256);
                    }
                }
            }
            f1 = WS_F1 + ((nt * 2 + wn) * 32 + mt * 2 + wm) * 4;
        }
        BARRIER();
        STAMP(7);
        f32x4 hc[2][5];
        UNROLL for (int h = 0; h < 2; ++h) UNROLL for (int u = 0; u < 5; ++u) hc[h][u] = f32x4{};
        UNROLL for (int z = 14; z < 20; ++z) UNROLL for (int h = 0; h < 2; ++h) P1_RK(z, h);
        s16x8 wb[2][5];
        UNROLL for (int z = 0; z < 21; ++z) {
            if (z == PUBZ) { publish(W, f1, epoch, lane); WT_LOAD(); STAMP2(16); }
            if (z == 12) lds_wait(L + FL_FULL + fl4, 1);
            UNROLL for (int u = 0; u < 5; ++u) {
                if (z < 20) { wb[z & 1][u] = lds128(L + WH_ADDR((z >> 1) * 10 + 2 * u) + ra[z & 1]); SBAR(); }
                if (z > 0) {
                    hc[0][u] = MFMA16(xs[(z - 1) >> 1][(z - 1) & 1][0], wb[(z - 1) & 1][u], hc[0][u]);
                    hc[1][u] = MFMA16(xs[(z - 1) >> 1][(z - 1) & 1][1], wb[(z - 1) & 1][u], hc[1][u]); SBAR();
                }
            }
        }
        WAITV(0);
        WT_STORE();
        lds_signal(LW + FL_FREE + wid * 4, 1);
        const float sg = nt < 8 ? 1.0f : -1.0f;
        const unsigned ab = WS_AB + (((nt & 7) >> 1) * 512 + (nt >> 3) * 256 + mt * 16 + wm * 8 + wn * 4 + (kg & 1) * 2 + (kg >> 1)) * 2560 + ((nt & 1) * 80 + l15) * 16;
        UNROLL for (int u = 0; u < 5; ++u) {
            unsigned a0 = pack2(hc[0][u][0] * sg, hc[0][u][1] * sg), a1 = pack2(hc[0][u][2] * sg, hc[0][u][3] * sg);
            unsigned b0 = pack2(hc[1][u][0] * sg, hc[1][u][1] * sg), b1 = pack2(hc[1][u][2] * sg, hc[1][u][3] * sg);
            auto s0 = __builtin_amdgcn_permlane16_swap(a0, b0, false, false);
            auto s1 = __builtin_amdgcn_permlane16_swap(a1, b1, false, false);
            st128(W, u32x4{s0[0], s1[0], s0[1], s1[1]}, ab, u * 256);
        }
    }
    }
    STAMP(2);
    const int lane = __builtin_amdgcn_mbcnt_hi(~0u, __builtin_amdgcn_mbcnt_lo(~0u, 0u));
    const int r = lane & 31, hh = lane >> 5;
    const int l15 = lane & 15, kg = lane >> 4;
    const unsigned fl4 = (lane & 3) * 4;

    const int tt = xcd * 4 + (idx >> 3), ct = idx & 7;
    const int ksp = xcd >> 1, nt4 = idx >> 3;
    const u32x4 dc = mkdesc(ksp < 2 ? cre : cim, 2048 * 4096), dws = mkdesc(ws, WS_END);
    const unsigned cs0 = mt * 128 * 4096 + (ksp & 1) * 2048;
    const unsigned as0 = WS_AB + (nt4 * 512 + ksp * 128) * 2560;
#define SQ_SETUP(ln) \
        unsigned coff[2]; const unsigned lin = (ln) * 16; \
        UNROLL for (int i = 0; i < 2; ++i) coff[i] = ((ln) >> 3) * 4096 + ((((ln) & 7) ^ ((4 * i + ((ln) >> 4)) & 7)) << 4);
#define SQ_ISSUE(n, g) do { \
        const unsigned cb_ = cs0 + (g) * 131072 + (n) * 128, ab_ = as0 + (n) * 20480 + (g) * 5120; \
        const unsigned lc_ = lds0 + ((n) & 3) * SQ_STAGE + (g) * 4096, la_ = lds0 + ((n) & 3) * SQ_STAGE + 16384 + (g) * 5120; \
        UNROLL for (int i = 0; i < 4; ++i) dma16(coff[i & 1], dc, cb_ + i * 32768, lc_ + i * 1024); \
        UNROLL for (int i = 0; i < 5; ++i) dma16s(lin, dws, ab_ + i * 1024, la_ + i * 1024); } while (0)
#define SQ_BURST(n, g) do { \
        const unsigned cb_ = cs0 + ((g) & 1) * 262144 + (n) * 128, ab_ = as0 + (n) * 20480 + ((g) & 1) * 10240; \
        const unsigned lc_ = lds0 + ((n) & 3) * SQ_STAGE + ((g) & 1) * 8192, la_ = lds0 + ((n) & 3) * SQ_STAGE + 16384 + ((g) & 1) * 10240; \
        UNROLL for (int i = 0; i < 8; ++i) dma16(coff[i & 1], dc, cb_ + i * 32768, lc_ + i * 1024); \
        UNROLL for (int i = 0; i < 10; ++i) dma16s(lin, dws, ab_ + i * 1024, la_ + i * 1024); } while (0)

    {
        const int head = xcd * 4 + (idx >> 3), qt = idx & 7;
        const int qh = wid & 1, kq = wid >> 1;
        const int col0 = head * 20, odd = head & 1;
        const int cA = (col0 >> 3) + odd, cH = odd ? (col0 >> 3) : (col0 >> 3) + 2;
        const int q0 = qt * 256 + qh * 128;
        unsigned k1off = WS_QK + (80 + cA + hh) * CH + r * 16;
        unsigned k2off = hh ? OOB : WS_QK + (80 + cH) * CH + r * 16 + odd * 8;
        unsigned q2off = hh ? OOB : k2off - 80 * CH;
        unsigned k2or = hh ? 0x3F80u : 0u;
        const int dv = odd ? (r < 16 ? r + 4 : r - 16) : r;
        const unsigned voff = r < 20 ? WS_VT + hh * VROW + (col0 + dv) * 16 : (r == 20 ? oneoff : OOB);
        unsigned vstep = r < 20 ? 4 * VROW : 0;
        unsigned va = voff + kq * 16 * vstep, vb2 = va + (vstep >> 1);
        unsigned cpf = lane < 4 ? lane * 128 : OOB;
        unsigned gq0 = 0, gq1 = 0;
        constexpr int NB = 4;
        u32x4 k1[NB], v0[NB], v1[NB]; u32x2 k2[NB];
        f32x16 o[4], s[2];
        u32x4 pa = u32x4{}, pd = u32x4{};
        f32x8 e8 = f32x8{};
        UNROLL for (int i = 0; i < 4; ++i) { o[i] = f32x16{}; PIN(o[i]); }
        v0[NB - 1] = u32x4{}; v1[NB - 1] = u32x4{};
        PIN(v0[NB - 1]); PIN(v1[NB - 1]); PIN(pa); PIN(pd); PIN(e8);
        PIN(k1off); PIN(k2off); PIN(q2off); PIN(k2or); PIN(va); PIN(vb2); PIN(vstep); PIN(cpf);
        __builtin_amdgcn_s_setprio(3);
        if (wid == 4) {
            const int nsa = hh ? (col0 + 19) >> 6 : col0 >> 6;
            wait_flags3(W, WS_F1 + ((10 + nsa) * 32 + r) * 4, WS_F1 + ((20 + nsa) * 32 + r) * 4,
                        WS_F1 + (nsa * 32 + qt * 4 + (r & 3)) * 4, epoch);
            lds_post(LW + FL_GO, 1);
        } else {
            lds_wait(L + FL_GO, 1);
        }
        STAMP(3);
        u32x2 kr2 = ld64(W, k2off, 0); u32x4 kr1 = ld128(W, k1off, 0);
        u32x4 q1[4]; u32x2 q2[4];
        UNROLL for (int i = 0; i < 4; ++i) {
            q2[i] = ld64(W, q2off, (q0 + i * 32) * 16);
            q1[i] = ld128(W, k1off - 80 * CH, (q0 + i * 32) * 16);
        }
        const int t0 = kq * 16;
        SBAR();
        UNROLL for (int b = 0; b < NB; ++b) {
            k2[b] = ld64(W, k2off, (t0 + b) * 512); k1[b] = ld128(W, k1off, (t0 + b) * 512); SBAR();
            if (b < NB - 1) { v0[b] = ld128(W, va, 0); v1[b] = ld128(W, vb2, 0); va += vstep; vb2 += vstep; SBAR(); }
        }
        const f32x16 zero16 = f32x16{};
        {
            f32x16 t[4];
            UNROLL for (int i = 0; i < 4; ++i) { t[i] = MFMA32K8(as4(kr2), as4(q2[i]), zero16); t[i] = MFMA32(as8(kr1), as8(q1[i]), t[i]); SBAR(); }
            UNROLL for (int i = 0; i < 4; ++i) {
                float m = t[i][0];
                UNROLL for (int e = 1; e < 16; ++e) m = fmaxf(m, t[i][e]);
                auto mm = __builtin_amdgcn_permlane32_swap(__float_as_uint(m), __float_as_uint(m), false, false);
                m = fmaxf(__uint_as_float(mm[0]), __uint_as_float(mm[1]));
                q2[i][0] = hh ? pack2(-m, 0.f) & 0xFFFFu : q2[i][0];
                if (i == 0) {
                    k2[0][0] |= k2or;
                    s[0] = MFMA32K8(as4(k2[0]), as4(q2[0]), zero16);
                    s[0] = MFMA32(as8(k1[0]), as8(q1[0]), s[0]);
                }
            }
        }
        if (wid < 4) publish(W, WS_F2 + (nt * 64 + mt * 4 + wid) * 4, epoch, lane);
        STAMP(4);
        NOUNROLL for (int tb = 0; tb < 16; tb += NB) {
            STAMP2(12 + (tb >> 2));
            if (wid >= 4) {
                if (tb == 8) {
                    int ln = lane, g = gwv, ix = idx, xc = xcd;
                    asm volatile("" : "+v"(ln), "+s"(g), "+s"(ix), "+s"(xc));
                    lds_wait(L + FL_FREE + (ln & 3) * 4, 1);
                    const int rr = ln & 31, px = rr >> 2;
                    const unsigned fw = WS_F2 + (((px >> 1) * 4 + g) * 64 + ((px & 1) * 8 + (ix & 7)) * 4 + (rr & 3)) * 4;
                    const unsigned f2 = WS_F2 + (((xc >> 2) * 8 + (ix >> 3) * 2 + (ln >> 5)) * 64 + ((xc >> 1) & 1) * 32 + rr) * 4;
                    wait_flags3(W, f2, fw, WS_F1 + (30 * 32 + ln) * 4, epoch);
                    gq0 = ld32(W, WS_G + ((ix & 7) * 256 + (g & 1) * 128 + (g >> 1) * 64 + rr) * 4, 0); gq1 = ld32(W, WS_G + ((ix & 7) * 256 + (g & 1) * 128 + (g >> 1) * 64 + 32 + rr) * 4, 0);
                    PIN(gq0); PIN(gq1);
                    dma4(ln * 128, dws, as0 + ((ix & 7) * 4 + g) * 10240, lds0 + LDS_SINK);
                    dma4(ln < 16 ? ln * 128 : OOB, dws, as0 + ((ix & 7) * 4 + g) * 10240 + 8192, lds0 + LDS_SINK);
                    dma4(ln < 50 ? ln * 128 + (ix >> 3) * 6400 : OOB, dws, WS_WOT + ((ix & 7) * 80 + g * 20) * 1280, lds0 + LDS_SINK);
                    SQ_SETUP(ln);
                    SQ_BURST(g >> 1, g);
                }
                if (tb == 12) { WAITV(16); lds_post(LW + FL_SFULL + gwv * 4, 1); }
            }
            SBAR();
            UNROLL for (int b = 0; b < NB; ++b) {
                const int bn = (b + 1) % NB, bp = (b + NB - 1) % NB;
                if (b == 2) { if (tb == 8) __builtin_amdgcn_s_setprio(2); if (tb == 12) __builtin_amdgcn_s_setprio(1); }
                if (b == 3) { if (tb == 12) __builtin_amdgcn_s_setprio(0); }
                const unsigned kso = tb + b + NB < 16 ? (t0 + tb + b + NB) * 512 : OOB / 2;
                const unsigned vso = tb + b + NB - 1 < 16 ? 0 : OOB / 2;
                UNROLL for (int qi = 0; qi < 4; ++qi) {
                    const int c = qi & 1, n = c ^ 1, qn = (qi + 1) & 3, qp = (qi + 3) & 3;
                    const int kb = qi == 3 ? bn : b, vb = qi == 0 ? bp : b;
                    f32x16& SC = s[c]; f32x16& SN = s[n];
                    SN = MFMA32K8(as4(k2[kb]), as4(q2[qn]), zero16); SBAR();
                    SC[0] = EX2(SC[0]); SC[1] = EX2(SC[1]); SC[2] = EX2(SC[2]); SC[3] = EX2(SC[3]); SBAR();
                    pd[0] = pack2(e8[0], e8[1]); pd[1] = pack2(e8[2], e8[3]); PIN(SC); PIN(pd); SBAR();
                    SN = MFMA32(as8(k1[kb]), as8(q1[qn]), SN); SBAR();
                    if (qi == 2) { k2[b] = ld64(W, k2off, kso); k1[b] = ld128(W, k1off, kso); SBAR(); }
                    if (CPF && qi == 1) { dma4(cpf, dc, cs0 + (nt4 * 32 + wid * 4 + b) * 4096 + tb * 128, lds0 + LDS_SINK); SBAR(); }
                    SC[4] = EX2(SC[4]); SC[5] = EX2(SC[5]); SC[6] = EX2(SC[6]); SC[7] = EX2(SC[7]); SBAR();
                    pd[2] = pack2(e8[4], e8[5]); pd[3] = pack2(e8[6], e8[7]); PIN(SC); PIN(pd); SBAR();
                    o[qp] = MFMA32(as8(v0[vb]), as8(pa), o[qp]); SBAR();
                    e8[0] = EX2(SC[8]); e8[1] = EX2(SC[9]); e8[2] = EX2(SC[10]); e8[3] = EX2(SC[11]); SBAR();
                    pa[0] = pack2(SC[0], SC[1]); pa[1] = pack2(SC[2], SC[3]);
                    if (qi == 2) { k2[bn][0] |= k2or; PIN(k2[bn]); }
                    if (qi == 1) { va += vstep; vb2 += vstep; PIN(va); PIN(vb2); }
                    PIN(e8); PIN(pa); SBAR();
                    o[qp] = MFMA32(as8(v1[vb]), as8(pd), o[qp]); SBAR();
                    if (qi == 0) { v0[bp] = ld128(W, va, vso); v1[bp] = ld128(W, vb2, vso); SBAR(); }
                    e8[4] = EX2(SC[12]); e8[5] = EX2(SC[13]); e8[6] = EX2(SC[14]); e8[7] = EX2(SC[15]); SBAR();
                    pa[2] = pack2(SC[4], SC[5]); pa[3] = pack2(SC[6], SC[7]); PIN(e8); PIN(pa); SBAR();
                }
            }
        }
        UNROLL for (int e = 0; e < 4; ++e) pd[e] = pack2(e8[2 * e], e8[2 * e + 1]);
        o[3] = MFMA32(as8(v0[NB - 1]), as8(pa), o[3]);
        o[3] = MFMA32(as8(v1[NB - 1]), as8(pd), o[3]);
        STAMP(5);
        lds_wait(L + FL_FREE + fl4, 1);
#define EX_PUT(i, slot) do { const lds_p ex = LW + LDS_EX + ((qh * 4 + (i)) * 3 + (slot)) * 2688; \
            stsf4(ex + lane * 16, f32x4{o[i][0], o[i][1], o[i][2], o[i][3]}); \
            stsf4(ex + 1024 + lane * 16, f32x4{o[i][4], o[i][5], o[i][6], o[i][7]}); \
            if (hh == 0) stsf4(ex + 2048 + r * 16, f32x4{o[i][8], o[i][9], o[i][10], o[i][11]}); \
            else *(__attribute__((address_space(3))) float*)(ex + 2560 + r * 4) = o[i][8]; } while (0)
        if (wid < 4) {
            UNROLL for (int i = 0; i < 4; ++i) EX_PUT(i, kq);
            lds_signal(LW + FL_EX + wid * 4);
        } else {
            __builtin_amdgcn_s_setprio(3);
            const int f0 = q0 + (kq & 1) * 64;
            f32x16 fo[2]; u32x4 fq1[2]; u32x2 fq2[2];
            if (kq == 2) { EX_PUT(2, 2); EX_PUT(3, 2); UNROLL for (int j = 0; j < 2; ++j) { fo[j] = o[j]; fq1[j] = q1[j]; fq2[j] = q2[j]; } }
            else { EX_PUT(0, 2); EX_PUT(1, 2); UNROLL for (int j = 0; j < 2; ++j) { fo[j] = o[2 + j]; fq1[j] = q1[2 + j]; fq2[j] = q2[2 + j]; } }
            lds_signal(LW + FL_EX + wid * 4);
            lds_wait(L + FL_EX + ((lane & 3) * 2 + qh) * 4);
            STAMP2(17);
            UNROLL for (int j = 0; j < 2; ++j)
                UNROLL for (int sl = 0; sl < 3; ++sl) {
                    const lds_cp rx = L + LDS_EX + ((qh * 4 + (kq & 1) * 2 + j) * 3 + sl) * 2688;
                    const f32x4 a = ldsf4(rx + lane * 16), b = ldsf4(rx + 1024 + lane * 16), c = ldsf4(rx + 2048 + r * 16);
                    const float d = *(const __attribute__((address_space(3))) float*)(rx + 2560 + r * 4);
                    UNROLL for (int e = 0; e < 4; ++e) { fo[j][e] += a[e]; fo[j][4 + e] += b[e]; }
                    fo[j][8] += hh ? d : c[0];
                    UNROLL for (int e = 1; e < 4; ++e) fo[j][8 + e] += hh ? 0.f : c[e];
                }
            PIN(fo[0]); PIN(fo[1]);
            lds_signal(LW + FL_FIN + gwv * 4);
            STAMP2(18);
            lds_wait(L + FL_FIN + fl4, 1);
            __builtin_amdgcn_s_setprio(0);
            { SQ_SETUP(lane); SQ_ISSUE(2, gwv); SQ_ISSUE(3, gwv); }
            __builtin_amdgcn_s_setprio(3);
            STAMP2(19);
            u32x4 oa[2]; u32x2 ob[2];
            UNROLL for (int j = 0; j < 2; ++j) {
                auto ll = __builtin_amdgcn_permlane32_swap(__float_as_uint(fo[j][8]), __float_as_uint(fo[j][8]), false, false);
                float l = __uint_as_float(ll[1]);
                bool ok = (l > 1e-18f) && (l < 1e30f);
                UNROLL for (int e = 0; e < 12; ++e) ok = ok && (fabsf(fo[j][e]) < 3e38f);
                if (!__all(ok)) {
                    u32x2 qz = fq2[j];
                    if (hh) qz[0] = 0;
                    float m = -3e38f;
                    f32x16 oo = f32x16{};
                    NOUNROLL for (int t = 0; t < 64; ++t) {
                        const u32x4 a1 = ld128(W, k1off, t * 512); const u32x2 a2 = ld64(W, k2off, t * 512);
                        const u32x4 w0 = ld128(W, voff + t * vstep, 0), w1 = ld128(W, voff + t * vstep + (vstep >> 1), 0);
                        f32x16 z = MFMA32K8(as4(a2), as4(qz), zero16);
                        z = MFMA32(as8(a1), as8(fq1[j]), z);
                        float mx = z[0];
                        UNROLL for (int e = 1; e < 16; ++e) mx = fmaxf(mx, z[e]);
                        auto mm = __builtin_amdgcn_permlane32_swap(__float_as_uint(mx), __float_as_uint(mx), false, false);
                        const float mn = fmaxf(m, fmaxf(__uint_as_float(mm[0]), __uint_as_float(mm[1])));
                        const float al = EX2(m - mn);
                        m = mn;
                        UNROLL for (int e = 0; e < 16; ++e) oo[e] *= al;
                        u32x4 p0, p1;
                        UNROLL for (int e = 0; e < 4; ++e) { p0[e] = pack2(EX2(z[2 * e] - m), EX2(z[2 * e + 1] - m)); p1[e] = pack2(EX2(z[8 + 2 * e] - m), EX2(z[9 + 2 * e] - m)); }
                        oo = MFMA32(as8(w0), as8(p0), oo);
                        oo = MFMA32(as8(w1), as8(p1), oo);
                    }
                    fo[j] = oo;
                    auto l2 = __builtin_amdgcn_permlane32_swap(__float_as_uint(oo[8]), __float_as_uint(oo[8]), false, false);
                    l = __uint_as_float(l2[1]);
                }
                const float sc = __uint_as_float(j ? gq1 : gq0) * __builtin_amdgcn_rcpf(l);
                unsigned a0 = pack2(fo[j][0] * sc, fo[j][1] * sc), a1 = pack2(fo[j][2] * sc, fo[j][3] * sc);
                unsigned b0 = pack2(fo[j][4] * sc, fo[j][5] * sc), b1 = pack2(fo[j][6] * sc, fo[j][7] * sc);
                auto s0 = __builtin_amdgcn_permlane32_swap(a0, b0, false, false);
                auto s1 = __builtin_amdgcn_permlane32_swap(a1, b1, false, false);
                oa[j] = u32x4{s0[0], s1[0], s0[1], s1[1]};
                ob[j] = u32x2{pack2(fo[j][8] * sc, fo[j][9] * sc), pack2(fo[j][10] * sc, fo[j][11] * sc)};
                PIN(oa[j]); PIN(ob[j]);
            }
            STAMP2(20);
            UNROLL for (int j = 0; j < 2; ++j) {
                const unsigned row = (f0 + j * 32 + r) * 16;
                st128(W, oa[j], WS_O + (cA + hh) * CH + row, 0);
                st64(W, ob[j], hh ? OOB : WS_O + cH * CH + row + odd * 8, 0);
            }
            __builtin_amdgcn_s_setprio(0);
        }
        STAMP(6);
    }

    if (wid < 4) {
        const int wm = wid & 1, wn = wid >> 1;
        unsigned ra[2];
        UNROLL for (int ks = 0; ks < 2; ++ks) ra[ks] = wm * 8192 + l15 * 128 + (((4 * ks + kg) ^ ((l15 >> 1) & 7)) << 4);
        const unsigned rb = 16384 + kg * 2560 + (wn * 80 + l15) * 16;
        f32x4 acc[4][5];
        UNROLL for (int i = 0; i < 4; ++i) UNROLL for (int u = 0; u < 5; ++u) acc[i][u] = f32x4{};
        s16x8 fc[2][4], fa[2][5];
        UNROLL for (int i = 0; i < 4; ++i) fc[1][i] = s16x8{};
        UNROLL for (int u = 0; u < 5; ++u) fa[1][u] = s16x8{};
        lds_wait(L + FL_SFULL + fl4, 1);
#define SQ_MF(u, i) acc[i][u] = MFMA16(fa[c][u], fc[c][i], acc[i][u]); SBAR()
#define SQ_RC(i) fc[n][i] = lds128(sp + ra[ks] + (i) * 2048); SBAR()
#define SQ_RA(u) fa[n][u] = lds128(sp + rb + ks * 10240 + (u) * 256); SBAR()
        NOUNROLL for (int j = 0; j < 16; ++j) {
            const lds_cp sp = L + (j & 3) * SQ_STAGE;
            UNROLL for (int ks = 0; ks < 2; ++ks) {
                const int n = ks, c = n ^ 1;
                SQ_MF(0, 0); SQ_RC(0); SQ_MF(0, 1); SQ_RA(0); SQ_MF(0, 2); SQ_RC(1); SQ_MF(0, 3); SQ_RC(2);
                SQ_MF(1, 0); SQ_RC(3); SQ_MF(1, 1); SQ_RA(1); SQ_MF(1, 2); SQ_RA(2); SQ_MF(1, 3); SQ_RA(3);
                SQ_MF(2, 0); SQ_RA(4); SQ_MF(2, 1); SQ_MF(2, 2); SQ_MF(2, 3);
                SQ_MF(3, 0); SQ_MF(3, 1); SQ_MF(3, 2); SQ_MF(3, 3);
                SQ_MF(4, 0); SQ_MF(4, 1);
                __builtin_amdgcn_s_waitcnt(0xC07F); SBAR();
                SQ_MF(4, 2);
                if (ks == 1 && j > 0 && j < 15) { BARRIER_NOWAIT(); SBAR(); }
                SQ_MF(4, 3);
            }
        }
        UNROLL for (int u = 0; u < 5; ++u) UNROLL for (int i = 0; i < 4; ++i) acc[i][u] = MFMA16(fa[1][u], fc[1][i], acc[i][u]);
        SBAR();
        __builtin_amdgcn_s_setprio(3);
        lds_post(LW + FL_SFREE + wid * 4, 1);
        STAMP(8);
        const unsigned yb = WS_Y + (ksp * 80 + nt4 * 20 + wn * 10 + (kg >> 1)) * CH + (mt * 128 + wm * 64 + (kg & 1) * 16 + l15) * 16;
        UNROLL for (int u = 0; u < 5; ++u)
            UNROLL for (int i = 0; i < 4; i += 2) {
                unsigned a0 = pack2(acc[i][u][0], acc[i][u][1]), a1 = pack2(acc[i][u][2], acc[i][u][3]);
                unsigned b0 = pack2(acc[i + 1][u][0], acc[i + 1][u][1]), b1 = pack2(acc[i + 1][u][2], acc[i + 1][u][3]);
                auto s0 = __builtin_amdgcn_permlane16_swap(a0, b0, false, false);
                auto s1 = __builtin_amdgcn_permlane16_swap(a1, b1, false, false);
                st128(W, u32x4{s0[0], s1[0], s0[1], s1[1]}, yb, u * 2 * CH + i * 256);
            }
        STAMP(9);
        publish(W, WS_F4 + (((mt * 2 + wm) * 8 + nt4 * 2 + wn) * 4 + ksp) * 4, epoch, lane);
        STAMP(10);
        if (blockIdx.x == 0 && wid == 0) {
            NOUNROLL for (unsigned spins = 0; spins < SPIN_CAP; ++spins) {
                asm volatile("" ::: "memory");
                bool ok = true;
                UNROLL for (int i = 0; i < 8; ++i) {
                    const u32x4 f = ld128(W, WS_F3 + i * 1024 + lane * 16, 0);
                    ok = ok && f[0] == epoch && f[1] == epoch && f[2] == epoch && f[3] == epoch;
                }
                if (__all(ok)) break;
                __builtin_amdgcn_s_sleep(8);
            }
            if (lane == 0) st32(W, epoch, WS_EP, 0);
        }
        return;
    }

    u32x4 wf[5][5] = {};
    const int t0 = tt * 64;
    const unsigned wfo = WS_WOT + (ct * 80 + gwv * 20 + kg) * 1280 + l15 * 16;
    {
        SQ_SETUP(lane);
        WAITV(13); BARRIER_NOWAIT();
        SQ_ISSUE(4, gwv); SQ_ISSUE(5, gwv);
        UNROLL for (int j = 3; j < 16; ++j) {
            if (j == 4) {
                WAITV(0);
                if (lane == 0) st32(W, epoch, WS_F3 + (((idx & 7) * 4 + (wid & 1) * 2 + ((wid >> 1) & 1)) * 32 + xcd * 4 + (idx >> 3)) * 4, 0);
            }
            if (j < 14) { WAITV(18); } else if (j == 14) { WAITV(9); } else { WAITV(0); }
            BARRIER_NOWAIT();
            if (j >= 4 && j <= 12 && (j & 1) == 0) { UNROLL for (int u = 0; u < 5; ++u) wf[(j - 4) / 2][u] = ld128(W, wfo + u * 256, ((j - 4) / 2) * 5120); SBAR(); }
            if (j + 3 < 16) SQ_ISSUE(j + 3, gwv);
        }
    }
    STAMP(7);
    wait_flags1(W, WS_F3 + (tt * 32 + gwv * 8 + (lane & 7)) * 4, epoch);
    const unsigned boff = WS_O + (gwv * 20 + kg) * CH + (t0 + l15) * 16;
    u32x4 bf[4][5];
    UNROLL for (int d = 0; d < 4; ++d)
        UNROLL for (int st = 0; st < 5; ++st) bf[d][st] = ld128(W, boff + ((gwv + d + 1) & 3) * 256, st * 4 * CH);
    const unsigned hraw = ld32(W, WS_G + (t0 + gwv * 16 + l15) * 4, 0);
    SBAR();
    lds_wait(L + FL_SFREE + fl4, 1);
    f32x4 res[5];
    UNROLL for (int d = 0; d < 4; ++d) {
        UNROLL for (int u = 0; u < 5; ++u) res[u] = f32x4{};
        UNROLL for (int st = 0; st < 5; ++st) UNROLL for (int u = 0; u < 5; ++u) res[u] = MFMA16(as8(wf[st][u]), as8(bf[d][st]), res[u]);
        if (d < 3) { UNROLL for (int u = 0; u < 5; ++u) stsf4(LW + ((gwv + d + 1) & 3) * GW_LDS + (d * 5 + u) * 1024 + lane * 16, res[u]); }
        if (d == 2) lds_signal(LW + FL_RED + gwv * 4);
        SBAR();
    }
    STAMP(8);
    lds_wait(L + FL_RED + fl4);
    UNROLL for (int d = 0; d < 3; ++d) UNROLL for (int u = 0; u < 5; ++u) res[u] += ldsf4(L + gwv * GW_LDS + (d * 5 + u) * 1024 + lane * 16);
    UNROLL for (int u = 0; u < 5; ++u) PIN(res[u]);
    STAMP(9);
    wait_flags1(W, WS_F4 + ((tt * 8 + ct) * 4 + (lane & 3)) * 4, epoch);
    const unsigned yo = WS_Y + (ct * 10 + (kg >> 1)) * CH + (t0 + gwv * 16 + l15) * 16 + (kg & 1) * 8;
    u32x2 y[5][4];
    UNROLL for (int u = 0; u < 5; ++u) UNROLL for (int p = 0; p < 4; ++p) y[u][p] = ld64(W, yo, (p * 80 + u * 2) * CH);
    const float h = 1.0f - __uint_as_float(hraw);
    char* dst = out + (size_t)(t0 + gwv * 16 + l15) * 1280 + ct * 160 + kg * 8;
    UNROLL for (int u = 0; u < 5; ++u) {
        const float r0 = res[u][0] + h * ((bflo(y[u][0][0]) + bflo(y[u][1][0])) + (bflo(y[u][2][0]) + bflo(y[u][3][0])));
        const float r1 = res[u][1] + h * ((bfhi(y[u][0][0]) + bfhi(y[u][1][0])) + (bfhi(y[u][2][0]) + bfhi(y[u][3][0])));
        const float r2 = res[u][2] + h * ((bflo(y[u][0][1]) + bflo(y[u][1][1])) + (bflo(y[u][2][1]) + bflo(y[u][3][1])));
        const float r3 = res[u][3] + h * ((bfhi(y[u][0][1]) + bfhi(y[u][1][1])) + (bfhi(y[u][2][1]) + bfhi(y[u][3][1])));
        *(u32x2*)(dst + u * 32) = u32x2{pack2(r0, r1), pack2(r2, r3)};
    }
    STAMP(10);
}

extern "C" void kernel_launch(void* const* d_in, const int* in_sizes, int n_in, void* d_out, int out_size,
                              void* d_ws, size_t ws_size, hipStream_t stream) {
    (void)hipFuncSetAttribute((const void*)fused, hipFuncAttributeMaxDynamicSharedMemorySize, LDS_BYTES);
    fused<<<256, 512, LDS_BYTES, stream>>>((const char*)d_in[0], (const char*)d_in[1], (const char*)d_in[2], (const char*)d_in[4],
                                           (const char*)d_in[5], (const char*)d_in[6], (const char*)d_in[7], (const char*)d_in[8],
                                           (char*)d_ws, (char*)d_out);
}
```

```cpp
#include <hip/hip_runtime.h>
#include <hip/hip_bf16.h>
#include <stdint.h>

typedef short s16x8 __attribute__((ext_vector_type(8)));
typedef short s16x4 __attribute__((ext_vector_type(4)));
typedef float f32x16 __attribute__((ext_vector_type(16)));
typedef float f32x8 __attribute__((ext_vector_type(8)));
typedef float f32x4 __attribute__((ext_vector_type(4)));
typedef unsigned u32x4 __attribute__((ext_vector_type(4)));
typedef unsigned u32x2 __attribute__((ext_vector_type(2)));
typedef __bf16 bf16x2_t __attribute__((ext_vector_type(2)));
typedef __attribute__((address_space(3))) const char* lds_cp;
typedef __attribute__((address_space(3))) char* lds_p;
typedef __amdgpu_buffer_rsrc_t rsrc_t;

#define DI __device__ __forceinline__
#define UNROLL _Pragma("unroll")
#define NOUNROLL _Pragma("unroll 1")
#define MFMA32(a, b, c) __builtin_amdgcn_mfma_f32_32x32x16_bf16((a), (b), (c), 0, 0, 0)
#define MFMA32K8(a, b, c) __builtin_amdgcn_mfma_f32_32x32x8bf16_1k((a), (b), (c), 0, 0, 0)
#define MFMA16(a, b, c) __builtin_amdgcn_mfma_f32_16x16x32_bf16((a), (b), (c), 0, 0, 0)
#define SBAR() __builtin_amdgcn_sched_barrier(0)
#define PIN(x) asm volatile("" : "+v"(x))
#define EX2(x) __builtin_amdgcn_exp2f(x)
#define SC1 16

constexpr unsigned CH = 2048 * 16;
constexpr unsigned WS_QK = 0;
constexpr unsigned WS_O = 160 * CH;
constexpr unsigned WS_Y = WS_O + 80 * CH;
constexpr unsigned ABROW = 640 * 16;
constexpr unsigned WS_AB = WS_Y + 320 * CH;
constexpr unsigned VROW = 10240;
constexpr unsigned WS_VT = WS_AB + 512 * ABROW;
constexpr unsigned WS_WOT = WS_VT + 256 * VROW;
constexpr unsigned WS_G = WS_WOT + 80 * ABROW;
constexpr unsigned FS = 128;
constexpr unsigned WS_ACK = WS_G + 8192;
constexpr unsigned WS_EP = WS_ACK + 8192;
constexpr unsigned WS_F1 = WS_EP + 4096;
constexpr unsigned WS_F2 = WS_F1 + 1024 * FS;
constexpr unsigned WS_F3 = WS_F2 + 1024 * FS;
constexpr unsigned WS_F4 = WS_F3 + 1024 * FS;
constexpr unsigned WS_ONE = WS_F4 + 1024 * FS;
constexpr unsigned WS_END = WS_ONE + 2048 * 128;
constexpr unsigned OOB = 0x80000000u;
#define WSTAIL 1
#define WS_BASE(n) (WSTAIL ? (((n) - WS_END) & ~(size_t)0x1FFFFF) : (size_t)0)

constexpr int P1_STAGE = 32768;
constexpr int SQ_STAGE = 36864;
constexpr int GW_LDS = 15360;
constexpr int EXB = 1536;
constexpr int LDS_EX = 4 * P1_STAGE;
constexpr int LDS_X9 = 4 * P1_STAGE;
constexpr int LDS_GW = LDS_X9 + 20480;
constexpr int LDS_WT = LDS_GW + 2048;
constexpr int LDS_FL = LDS_WT + 4096;
constexpr int LDS_SINK = LDS_FL + 256;
constexpr int LDS_BYTES = LDS_SINK + 256;
static_assert(LDS_BYTES <= 163840 && LDS_EX >= 3 * SQ_STAGE && LDS_FL >= LDS_EX + 8 * EXB, "lds");
constexpr int FL_FULL = LDS_FL, FL_GO = LDS_FL + 32, FL_EX = LDS_FL + 48, FL_SFULL = LDS_FL + 80,
              FL_SFREE = LDS_FL + 96, FL_FIN = LDS_FL + 112, FL_RED = LDS_FL + 128, FL_FREE = LDS_FL + 144;
#define GPRIO 0
#define FWARM 1
#define SL1 4
#define SL3 4
#define SJ 4
#define PUBZ 0
#define AABL 0
#define SABL 0
#define PFX 1
#define CPF 0
#define CODE_LINES 300

#define STAMP(i) do { } while (0)
#define TACC(a) do { } while (0)
#define TDECL() do { } while (0)
#define TOUT(i) do { } while (0)
#define STAMP2(i) do { } while (0)

DI unsigned pack2(float a, float b) {
    bf16x2_t v = {(__bf16)a, (__bf16)b};
    return __builtin_bit_cast(unsigned, v);
}
DI float bflo(unsigned u) { return __uint_as_float(u << 16); }
DI float bfhi(unsigned u) { return __uint_as_float(u & 0xFFFF0000u); }
DI s16x8 as8(u32x4 v) { return __builtin_bit_cast(s16x8, v); }
DI s16x4 as4(u32x2 v) { return __builtin_bit_cast(s16x4, v); }

DI u32x4 mkdesc(const void* p, unsigned bytes) {
    const unsigned long long a = (unsigned long long)p;
    return u32x4{(unsigned)a, (unsigned)(a >> 32) & 0xFFFFu, bytes, 0x00020000u};
}
DI void dma16(unsigned voff, u32x4 desc, unsigned soff, unsigned ldsaddr) {
    unsigned keep;
    asm volatile("s_mov_b32 %0, m0\n\ts_mov_b32 m0, %4\n\ts_nop 0\n\tbuffer_load_dwordx4 %1, %2, %3 offen lds\n\ts_mov_b32 m0, %0"
                 : "=&s"(keep) : "v"(voff), "s"(desc), "s"(soff), "s"(ldsaddr) : "memory");
}
DI void dma16s(unsigned voff, u32x4 desc, unsigned soff, unsigned ldsaddr) {
    unsigned keep;
    asm volatile("s_mov_b32 %0, m0\n\ts_mov_b32 m0, %4\n\ts_nop 0\n\tbuffer_load_dwordx4 %1, %2, %3 offen sc1 lds\n\ts_mov_b32 m0, %0"
                 : "=&s"(keep) : "v"(voff), "s"(desc), "s"(soff), "s"(ldsaddr) : "memory");
}
DI void dma4(unsigned voff, u32x4 desc, unsigned soff, unsigned ldsaddr) {
    unsigned keep;
    asm volatile("s_mov_b32 %0, m0\n\ts_mov_b32 m0, %4\n\ts_nop 0\n\tbuffer_load_dword %1, %2, %3 offen sc1 lds\n\ts_mov_b32 m0, %0"
                 : "=&s"(keep) : "v"(voff), "s"(desc), "s"(soff), "s"(ldsaddr) : "memory");
}
#define WAITV(n) asm volatile("s_waitcnt vmcnt(" #n ")" ::: "memory")
#define WAITL() asm volatile("s_waitcnt lgkmcnt(0)" ::: "memory")
#define BARRIER() asm volatile("s_waitcnt lgkmcnt(0)\n\ts_barrier" ::: "memory")
#define BARRIER_NOWAIT() asm volatile("s_barrier" ::: "memory")

DI s16x8 lds128(lds_cp p) { return *(const __attribute__((address_space(3))) s16x8*)p; }
DI f32x4 ldsf4(lds_cp p) { return *(const __attribute__((address_space(3))) f32x4*)p; }
DI void stsf4(lds_p p, f32x4 v) { *(__attribute__((address_space(3))) f32x4*)p = v; }
DI s16x4 ldstr(lds_cp p) {
    return __builtin_bit_cast(s16x4, __builtin_amdgcn_ds_read_tr16_b64_v4i16((__attribute__((address_space(3))) s16x4*)p));
}
DI s16x8 cat8(s16x4 a, s16x4 b) { return __builtin_shufflevector(a, b, 0, 1, 2, 3, 4, 5, 6, 7); }
DI u32x4 ld128(rsrc_t r, unsigned voff, unsigned soff) { return __builtin_amdgcn_raw_buffer_load_b128(r, voff, soff, SC1); }
DI u32x2 ld64(rsrc_t r, unsigned voff, unsigned soff) { return __builtin_amdgcn_raw_buffer_load_b64(r, voff, soff, SC1); }
DI unsigned ld32(rsrc_t r, unsigned voff, unsigned soff) { return __builtin_amdgcn_raw_buffer_load_b32(r, voff, soff, SC1); }
DI void st128(rsrc_t r, u32x4 v, unsigned voff, unsigned soff) { __builtin_amdgcn_raw_buffer_store_b128(v, r, voff + soff, 0, SC1); }
DI void st64(rsrc_t r, u32x2 v, unsigned voff, unsigned soff) { __builtin_amdgcn_raw_buffer_store_b64(v, r, voff, soff, SC1); }
DI void st32(rsrc_t r, unsigned v, unsigned voff, unsigned soff) { __builtin_amdgcn_raw_buffer_store_b32(v, r, voff, soff, SC1); }

constexpr unsigned SPIN_CAP = 200000;
DI void publish(rsrc_t w, unsigned flag_off, unsigned epoch, int lane) {
    WAITV(0);
    if (lane == 0) st32(w, epoch, flag_off, 0);
}
template <int SL>
DI void wait_flags4(rsrc_t w, unsigned o0, unsigned o1, unsigned o2, unsigned o3, unsigned epoch) {
    NOUNROLL for (unsigned spins = 0; spins < SPIN_CAP; ++spins) {
        asm volatile("" ::: "memory");
        const unsigned a = ld32(w, o0, 0), b = ld32(w, o1, 0), c = ld32(w, o2, 0), d = ld32(w, o3, 0);
        if (__all(a == epoch && b == epoch && c == epoch && d == epoch)) break;
        __builtin_amdgcn_s_sleep(SL);
    }
    asm volatile("" ::: "memory");
}
template <int SL>
DI void wait_flags1(rsrc_t w, unsigned o0, unsigned epoch) {
    NOUNROLL for (unsigned spins = 0; spins < SPIN_CAP; ++spins) {
        asm volatile("" ::: "memory");
        const unsigned a = ld32(w, o0, 0);
        if (__all(a == epoch)) break;
        __builtin_amdgcn_s_sleep(SL);
    }
    asm volatile("" ::: "memory");
}
DI void lds_signal(lds_p p, unsigned val = 1u) {
    WAITL();
    *(volatile __attribute__((address_space(3))) unsigned*)p = val;
}
DI void lds_post(lds_p p, unsigned val) { *(volatile __attribute__((address_space(3))) unsigned*)p = val; }
DI unsigned lds_peek(lds_cp p) { return *(const volatile __attribute__((address_space(3))) unsigned*)p; }
DI void lds_wait(lds_cp p, unsigned val = 1u) {
    NOUNROLL for (unsigned spins = 0; spins < 4 * SPIN_CAP; ++spins) {
        const unsigned v = *(const volatile __attribute__((address_space(3))) unsigned*)p;
        if (__all(v >= val)) break;
        __builtin_amdgcn_s_sleep(1);
    }
    asm volatile("" ::: "memory");
}

__global__ __launch_bounds__(512, 2) void fused(const char* __restrict__ x, const char* __restrict__ wqkv,
                                                 const char* __restrict__ wo, const char* __restrict__ cre,
                                                 const char* __restrict__ cim, const char* __restrict__ wr,
                                                 const char* __restrict__ wi, const char* __restrict__ gw,
                                                 char* ws, char* __restrict__ out) {
    extern __shared__ __attribute__((aligned(16))) char smem[];
    unsigned long long pc; asm volatile("s_getpc_b64 %0" : "=s"(pc));
    const int tid = threadIdx.x;
    const int wid = __builtin_amdgcn_readfirstlane(tid >> 6);
    const int gwv = wid & 3;
    const int xcd = blockIdx.x & 7, idx = blockIdx.x >> 3;
    const unsigned lds0 = (unsigned)(uintptr_t)smem;
    const lds_cp L = (lds_cp)smem;
    const lds_p LW = (lds_p)smem;
    const u32x4 dx = mkdesc(x, 2048 * 1280);
    const int mt = (xcd & 1) * 8 + (idx & 7), nt = (xcd >> 1) * 4 + (idx >> 3);
    const u32x4 dw = mkdesc(wqkv, 640 * 3840);
    const rsrc_t W = __builtin_amdgcn_make_buffer_rsrc(ws, 0, WS_END, 0x00020000);
    const unsigned oneoff = WS_ONE + (blockIdx.x * 8 + wid) * 128;
    unsigned epoch;
    {
    const int lane = tid & 63;
    unsigned roff[2];
    UNROLL for (int i = 0; i < 2; ++i) roff[i] = (lane >> 3) * 1280 + (((lane & 7) ^ ((4 * i + (lane >> 4)) & 7)) << 4);
    unsigned woff[2];
    UNROLL for (int i = 0; i < 2; ++i) woff[i] = nt == 15 ? OOB : (lane >> 3) * 3840 + (((lane & 7) ^ (((lane >> 4) & 1) << 2) ^ (i << 1)) << 4);
#define P1_DX(st, p, par) dma16(roff[par], dx, (mt * 128 + (p) * 8) * 1280 + (st) * 128, lds0 + ((st) & 3) * P1_STAGE + (p) * 1024)
#define P1_DW(st, p, par) dma16(woff[par], dw, ((st) * 64 + ((p) & 7) * 8) * 3840 + nt * 256 + ((p) >> 3) * 128, lds0 + ((st) & 3) * P1_STAGE + 16384 + (p) * 1024)
    P1_DX(0, 2 * wid, 0); P1_DW(0, 2 * wid, 0); P1_DX(0, 2 * wid + 1, 1); P1_DW(0, 2 * wid + 1, 1);
    STAMP(0);
    const int hh = lane >> 5;
    const int l15 = lane & 15, kg = lane >> 4;
    const unsigned fl4 = (lane & 3) * 4;
    const u32x4 dh = mkdesc(nt < 8 ? wr : wi, 640 * 1280);
#define GET_EPOCH() do { epoch = ld32(W, WS_EP, 0) + 1u; if (lane < 4) st32(W, 0x3F803F80u, oneoff + lane * 4, 0); } while (0)
#define WH_ADDR(q) ((q) < 32 ? 2 * P1_STAGE + (q) * 1024 : (q) < 48 ? 3 * P1_STAGE + 16384 + ((q) - 32) * 1024 : \
                    (q) < 68 ? LDS_X9 + ((q) - 48) * 1024 : (q) < 84 ? 16384 + ((q) - 68) * 1024 : P1_STAGE + 16384 + ((q) - 84) * 1024)

    if (wid >= 4) {
#define P1_ISSUE(st) do { UNROLL for (int i = 0; i < 4; ++i) { P1_DX(st, 4 * gwv + i, i & 1); P1_DW(st, 4 * gwv + i, i & 1); } } while (0)
#define P1_DH(q, par) do { const int q_ = (q), c_ = q_ / 10, p_ = q_ - c_ * 10; \
            dma16(roff[par], dh, ((nt & 7) * 80 + p_ * 8) * 1280 + c_ * 128, lds0 + WH_ADDR(q_)); } while (0)
        P1_ISSUE(1); P1_ISSUE(2); P1_ISSUE(3);
        WAITV(24); BARRIER_NOWAIT();
        NOUNROLL for (int k = 1; k < 7; ++k) { WAITV(16); BARRIER_NOWAIT(); P1_ISSUE(k + 3); }
        WAITV(16); BARRIER_NOWAIT();
        UNROLL for (int i = 0; i < 8; ++i) P1_DH(gwv * 8 + i, i & 1);
        WAITV(16); BARRIER_NOWAIT();
        UNROLL for (int i = 0; i < 8; ++i) P1_DH(32 + gwv * 8 + i, i & 1);
        WAITV(16); BARRIER_NOWAIT();
        UNROLL for (int i = 0; i < 6; ++i) { const int q = 64 + gwv * 6 + i; P1_DH(q < 84 ? q : 82 + (i & 1), i & 1); }
        GET_EPOCH();
        const int wm = gwv & 1, wn = gwv >> 1;
        unsigned sb[2], ss0, ss1;
        if (nt < 10) {
            UNROLL for (int i = 0; i < 2; ++i) sb[i] = WS_QK + (nt * 16 + wn * 8 + (kg >> 1)) * CH + (mt * 128 + wm * 64 + (i ^ wn) * 32 + (kg & 1) * 16 + l15) * 16;
            ss0 = 2 * CH; ss1 = 4 * CH;
        } else {
            UNROLL for (int i = 0; i < 2; ++i) sb[i] = nt == 15 ? OOB : WS_VT + ((mt * 8 + wm * 4 + (i ^ wn) * 2) * 2 + (kg & 1)) * VROW + ((nt - 10) * 128 + wn * 64 + hh * 16 + l15) * 16;
            ss0 = 512; ss1 = 2 * VROW;
        }
        __builtin_amdgcn_s_setprio(3);
        WAITV(0); BARRIER_NOWAIT();
        u32x4 tv[8];
        UNROLL for (int i = 0; i < 8; ++i)
            tv[i] = __builtin_bit_cast(u32x4, lds128(L + (i < 4 ? 3 * P1_STAGE : 0) + wm * 8192 + wn * 4096 + (i & 3) * 1024 + lane * 16));
        UNROLL for (int i = 0; i < 8; ++i) PIN(tv[i]);
        UNROLL for (int i = 0; i < 4; ++i) P1_DH(84 + gwv * 4 + i, i & 1);
        UNROLL for (int i = 0; i < 8; ++i) st128(W, tv[i], sb[i >> 2], (i & 1) * ss0 + ((i >> 1) & 1) * ss1);
        STAMP2(16);
        STAMP(1);
    } else {
        const unsigned sink = lds0 + LDS_SINK;
        const int pi = xcd * 4 + wid, pst = pi / 5, pu = pi - pst * 5;
        if (PFX) {
            const unsigned b = blockIdx.x;
            if (wid == 0) { dma4(lane * 128, dx, b * 10240, sink); dma4(lane < 16 ? lane * 128 : OOB, dx, b * 10240 + 8192, sink); }
            if (wid == 1) { dma4(lane * 128, dw, b * 9600, sink); dma4(lane < 11 ? lane * 128 : OOB, dw, b * 9600 + 8192, sink); }
            if (wid == 2) { dma4(FWARM && lane < 16 ? (b * 16 + lane) * FS : OOB, mkdesc(ws, WS_END), WS_F1, sink); dma4(OOB, dx, 0, sink); }
        } else if (wid < 3) { dma4(OOB, dx, 0, sink); dma4(OOB, dx, 0, sink); }
        if (wid == 3) {
            dma4(lane < 12 ? lane * 128 : OOB, mkdesc((const void*)(pc & ~127ull), CODE_LINES * 128), idx * 1536, sink);
            dma16(nt == 15 ? lane * 16 : OOB, mkdesc(gw, 1280), 0, lds0 + LDS_GW);
        }
        if (tid < 64) *(__attribute__((address_space(3))) unsigned*)(LW + LDS_FL + tid * 4) = 0u;
        const int wm = wid & 1, wn = wid >> 1;
        unsigned ra[2];
        UNROLL for (int ks = 0; ks < 2; ++ks) ra[ks] = l15 * 128 + (((4 * ks + kg) ^ ((l15 >> 1) & 7)) << 4);
        const unsigned xk = wm * 8192 + wn * 4096, xo = wm * 8192 + (wn ^ 1) * 4096;
        s16x8 xs[10][2][2];
#define WT_LOAD() dma16((lane >> 1) * 1280 + (lane & 1) * 16, mkdesc(wo, 640 * 1280), \
                  pi < 25 ? ((idx >> 3) * 160 + pst * 32) * 1280 + (idx & 7) * 160 + pu * 32 : OOB, lds0 + LDS_WT + wid * 1024)
#define WT_STORE() do { const lds_cp tp_ = L + LDS_WT + wid * 1024 + (8 * kg + (l15 >> 2)) * 32 + (lane & 3) * 8; \
            st128(W, __builtin_bit_cast(u32x4, cat8(ldstr(tp_), ldstr(tp_ + 128))), \
                  pi < 25 ? WS_WOT + (((idx & 7) * 4 + (idx >> 3)) * 25 + pi) * 1024 + lane * 16 : OOB, 0); } while (0)
        if (nt == 15) {
            const int m0 = mt * 128 + wm * 64 + wn * 32;
            if (wid == 3) dma16(lane * 16, mkdesc(gw, 1280), 1024, lds0 + LDS_GW + 1024);
            WT_LOAD();
            GET_EPOCH();
            {
                const unsigned o0 = (mt * 4 + wid) * 12800, v1 = lane < 36 ? lane * 128 : OOB;
                dma4(lane * 128, mkdesc(wr, 640 * 1280), o0, sink); dma4(v1, mkdesc(wr, 640 * 1280), o0 + 8192, sink);
                dma4(lane * 128, mkdesc(wi, 640 * 1280), o0, sink); dma4(v1, mkdesc(wi, 640 * 1280), o0 + 8192, sink);
            }
            WAITV(4);
            float sum[2] = {0.f, 0.f};
            UNROLL for (int k = 0; k < 10; ++k) {
                BARRIER();
                UNROLL for (int ks = 0; ks < 2; ++ks) {
                    const u32x4 gv = __builtin_bit_cast(u32x4, lds128(L + LDS_GW + k * 128 + (4 * ks + kg) * 16));
                    UNROLL for (int h = 0; h < 2; ++h) {
                        xs[k][ks][h] = lds128(L + (k & 3) * P1_STAGE + xk + h * 2048 + ra[ks]);
                        const u32x4 xv = __builtin_bit_cast(u32x4, xs[k][ks][h]);
                        UNROLL for (int e = 0; e < 4; ++e)
                            { const unsigned xe = xv[e], ge = gv[e]; sum[h] = __builtin_amdgcn_fdot2_f32_bf16(__builtin_bit_cast(bf16x2_t, xe), __builtin_bit_cast(bf16x2_t, ge), sum[h], false); }
                    }
                }
                SBAR();
            }
            UNROLL for (int h = 0; h < 2; ++h) { sum[h] += __shfl_xor(sum[h], 16); sum[h] += __shfl_xor(sum[h], 32); }
            const float sm = kg & 1 ? sum[1] : sum[0];
            if (lane < 32) st32(W, __float_as_uint(1.0f / (1.0f + __expf(-sm))), WS_G + (m0 + lane) * 4, 0);
        } else {
            const int q = l15 >> 2;
            unsigned trb[4];
            UNROLL for (int j = 0; j < 4; ++j)
                trb[j] = 16384 + wn * 8192 + (8 * kg + q) * 128 + ((j ^ (((q >> 1) & 1) << 1) ^ (kg & 1)) << 5) + 8 * (lane & 3);
            f32x4 acc[4][4];
            UNROLL for (int i = 0; i < 4; ++i) UNROLL for (int j = 0; j < 4; ++j) acc[i][j] = f32x4{};
            s16x8 xt[2][2]; s16x4 wv[4][2];
            WT_LOAD();
            GET_EPOCH();
            WAITV(3);
#define P1_RW(z, j) do { const lds_cp p_ = L + (((z) >> 1) & 3) * P1_STAGE + trb[j] + ((z) & 1) * 4096; wv[j][0] = ldstr(p_); wv[j][1] = ldstr(p_ + 512); } while (0)
#define P1_RK(z, h) xs[(z) >> 1][(z) & 1][h] = lds128(L + (((z) >> 1) & 3) * P1_STAGE + xk + (h) * 2048 + ra[(z) & 1])
#define P1_RO(z, h) xt[(z) & 1][h] = lds128(L + (((z) >> 1) & 3) * P1_STAGE + xo + (h) * 2048 + ra[(z) & 1])
#define P1_XF(z, i) ((i) < 2 ? xs[(z) >> 1][(z) & 1][(i) & 1] : xt[(z) & 1][(i) & 1])
#define P1_LOOP(MF) \
            BARRIER(); \
            STAMP2(11); \
            P1_RK(0, 0); P1_RW(0, 0); P1_RK(0, 1); P1_RO(0, 0); P1_RO(0, 1); P1_RW(0, 1); P1_RW(0, 2); P1_RW(0, 3); SBAR(); \
            UNROLL for (int z = 0; z < 20; ++z) { \
                UNROLL for (int j = 0; j < 4; ++j) { \
                    MF(z, 0, j); SBAR(); \
                    if (j == 0 && (z & 1) && z < 19) { BARRIER(); } \
                    MF(z, 1, j); SBAR(); \
                    if (z < 19) { if (j == 0) P1_RK(z + 1, 0); if (j == 1) P1_RK(z + 1, 1); if (j == 2) P1_RO(z + 1, 0); if (j == 3) P1_RO(z + 1, 1); SBAR(); } \
                    MF(z, 2, j); SBAR(); \
                    MF(z, 3, j); SBAR(); \
                    if (z < 19) { P1_RW(z + 1, j); SBAR(); } \
                } \
            }
#define P1_MFQ(z, i, j) acc[i][j] = MFMA16(cat8(wv[j][0], wv[j][1]), P1_XF(z, i), acc[i][j])
#define P1_MFV(z, i, j) acc[i][j] = MFMA16(P1_XF(z, i), cat8(wv[j][0], wv[j][1]), acc[i][j])
            u32x4 tv[8];
            if (nt < 10) {
                P1_LOOP(P1_MFQ)
                STAMP(1);
                const float sc = nt < 5 ? 0.32259889079f : 1.0f;
                UNROLL for (int i = 0; i < 4; i += 2)
                    UNROLL for (int j = 0; j < 4; ++j) {
                        unsigned a0 = pack2(acc[i][j][0] * sc, acc[i][j][1] * sc), a1 = pack2(acc[i][j][2] * sc, acc[i][j][3] * sc);
                        unsigned b0 = pack2(acc[i + 1][j][0] * sc, acc[i + 1][j][1] * sc), b1 = pack2(acc[i + 1][j][2] * sc, acc[i + 1][j][3] * sc);
                        auto s0 = __builtin_amdgcn_permlane16_swap(a0, b0, false, false);
                        auto s1 = __builtin_amdgcn_permlane16_swap(a1, b1, false, false);
                        tv[i * 2 + j] = u32x4{s0[0], s1[0], s0[1], s1[1]};
                    }
            } else {
                P1_LOOP(P1_MFV)
                STAMP(1);
                UNROLL for (int i = 0; i < 4; ++i)
                    UNROLL for (int j = 0; j < 4; j += 2) {
                        unsigned a0 = pack2(acc[i][j][0], acc[i][j][1]), a1 = pack2(acc[i][j][2], acc[i][j][3]);
                        unsigned b0 = pack2(acc[i][j + 1][0], acc[i][j + 1][1]), b1 = pack2(acc[i][j + 1][2], acc[i][j + 1][3]);
                        auto s0 = __builtin_amdgcn_permlane32_swap(a0, b0, false, false);
                        auto s1 = __builtin_amdgcn_permlane32_swap(a1, b1, false, false);
                        tv[i * 2 + (j >> 1)] = u32x4{s0[0], s1[0], s0[1], s1[1]};
                    }
            }
            UNROLL for (int i = 0; i < 8; ++i) PIN(tv[i]);
            UNROLL for (int z = 14; z < 18; ++z) UNROLL for (int h = 0; h < 2; ++h) P1_RK(z, h);
            WAITL();
            UNROLL for (int i = 0; i < 8; ++i)
                *(__attribute__((address_space(3))) u32x4*)(LW + (i < 4 ? 3 * P1_STAGE : 0) + xk + (i & 3) * 1024 + lane * 16) = tv[i];
        }
        BARRIER();
        __builtin_amdgcn_s_setprio(2);
        STAMP(7);
        f32x4 hc[2][5];
        UNROLL for (int h = 0; h < 2; ++h) UNROLL for (int u = 0; u < 5; ++u) hc[h][u] = f32x4{};
        UNROLL for (int z = 18; z < 20; ++z) UNROLL for (int h = 0; h < 2; ++h) P1_RK(z, h);
        s16x8 wb[2][5];
        UNROLL for (int z = 0; z < 21; ++z) {
            if (z == 16) { STAMP2(17); lds_wait(L + FL_FULL + fl4, 1); STAMP2(18); }
            UNROLL for (int u = 0; u < 5; ++u) {
                if (z < 20) { wb[z & 1][u] = lds128(L + WH_ADDR((z >> 1) * 10 + 2 * u) + ra[z & 1]); SBAR(); }
                if (z > 0) {
                    hc[0][u] = MFMA16(xs[(z - 1) >> 1][(z - 1) & 1][0], wb[(z - 1) & 1][u], hc[0][u]);
                    hc[1][u] = MFMA16(xs[(z - 1) >> 1][(z - 1) & 1][1], wb[(z - 1) & 1][u], hc[1][u]); SBAR();
                }
            }
        }
        STAMP2(19);
        WAITV(0);
        WT_STORE();
        lds_signal(LW + FL_FREE + wid * 4, 1);
        const float sg = nt < 8 ? 1.0f : -1.0f;
        const unsigned ab = WS_AB + (((nt & 7) >> 1) * 512 + (nt >> 3) * 256 + mt * 16 + wm * 8 + wn * 4 + (kg & 1) * 2 + (kg >> 1)) * 2560 + ((nt & 1) * 80 + l15) * 16;
        UNROLL for (int u = 0; u < 5; ++u) {
            unsigned a0 = pack2(hc[0][u][0] * sg, hc[0][u][1] * sg), a1 = pack2(hc[0][u][2] * sg, hc[0][u][3] * sg);
            unsigned b0 = pack2(hc[1][u][0] * sg, hc[1][u][1] * sg), b1 = pack2(hc[1][u][2] * sg, hc[1][u][3] * sg);
            auto s0 = __builtin_amdgcn_permlane16_swap(a0, b0, false, false);
            auto s1 = __builtin_amdgcn_permlane16_swap(a1, b1, false, false);
            st128(W, u32x4{s0[0], s1[0], s0[1], s1[1]}, ab, u * 256);
        }
        publish(W, WS_F2 + (nt * 64 + mt * 4 + wid) * FS, epoch, lane);
        if (lane == 0) st32(W, epoch, WS_ACK + (blockIdx.x * 8 + wid) * 4, 0);
    }
    }
    STAMP(2);
    const int lane = __builtin_amdgcn_mbcnt_hi(~0u, __builtin_amdgcn_mbcnt_lo(~0u, 0u));
    const int r = lane & 31, hh = lane >> 5;
    const int l15 = lane & 15, kg = lane >> 4;
    const unsigned fl4 = (lane & 3) * 4;

    const int tt = xcd * 4 + (idx >> 3), ct = idx & 7;
    const int ksp = xcd >> 1, nt4 = idx >> 3;
    const u32x4 dc = mkdesc(ksp < 2 ? cre : cim, 2048 * 4096), dws = mkdesc(ws, WS_END);
    const unsigned cs0 = mt * 128 * 4096 + (ksp & 1) * 2048;
    const unsigned as0 = WS_AB + (nt4 * 512 + ksp * 128) * 2560;

    if (wid >= 4) {
        const int head = xcd * 4 + (idx >> 3), qt = idx & 7;
        const int qh = wid & 1, kh = (wid >> 1) & 1;
        const int col0 = head * 20, odd = head & 1;
        const int cA = (col0 >> 3) + odd, cH = odd ? (col0 >> 3) : (col0 >> 3) + 2;
        const int q0 = qt * 256 + qh * 128;
        unsigned k1off = WS_QK + (80 + cA + hh) * CH + r * 16;
        unsigned k2off = hh ? OOB : WS_QK + (80 + cH) * CH + r * 16 + odd * 8;
        unsigned q2off = hh ? OOB : k2off - 80 * CH;
        unsigned k2or = hh ? 0x3F80u : 0u;
        const int dv = odd ? (r < 16 ? r + 4 : r - 16) : r;
        const unsigned voff = r < 20 ? WS_VT + hh * VROW + (col0 + dv) * 16 : (r == 20 ? oneoff : OOB);
        unsigned vstep = r < 20 ? 4 * VROW : 0;
        unsigned va = voff + kh * 32 * vstep, vb2 = va + (vstep >> 1);
        unsigned cpf = CPF == 1 ? (lane < 8 ? (lane >> 1) * 4096 + (lane & 1) * 128 : OOB) : CPF == 2 ? (lane < 8 ? lane * 128 : OOB) : (lane < 8 ? (lane >> 2) * 4096 + (lane & 3) * 128 : OOB);
        constexpr int NB = 4;
        u32x4 k1[NB], v0[NB], v1[NB]; u32x2 k2[NB];
        f32x16 o[4], s[2];
        u32x4 pa = u32x4{}, pd = u32x4{};
        f32x8 e8 = f32x8{};
        UNROLL for (int i = 0; i < 4; ++i) { o[i] = f32x16{}; PIN(o[i]); }
        v0[NB - 1] = u32x4{}; v1[NB - 1] = u32x4{};
        PIN(v0[NB - 1]); PIN(v1[NB - 1]); PIN(pa); PIN(pd); PIN(e8);
        PIN(k1off); PIN(k2off); PIN(q2off); PIN(k2or); PIN(va); PIN(vb2); PIN(vstep); PIN(cpf);
        WAITV(0);
        if (nt != 15 && lane == 0) st32(W, epoch, WS_F1 + ((nt * 2 + (gwv >> 1)) * 32 + mt * 2 + (gwv & 1)) * FS, 0);
        lds_post(LW + FL_FULL + gwv * 4, 1);
        const int nsa = (lane & 4) ? (col0 + 19) >> 6 : col0 >> 6;
        unsigned fkv = WS_F1 + ((10 + ((lane >> 3) & 1) * 10 + nsa) * 32 + kh * 16) * FS;
        STAMP2(23);
        wait_flags1<SL1>(W, (lane & 16) ? WS_F1 + (nsa * 32 + (q0 >> 6) + (lane & 1)) * FS : fkv + (lane & 3) * FS, epoch);
        fkv += (lane & 1) * FS;
        STAMP(3);
        u32x4 q1[4]; u32x2 q2[4];
        UNROLL for (int i = 0; i < 4; ++i) {
            const int qr = q0 + ((i + 2 + 2 * kh) & 3) * 32;
            q2[i] = ld64(W, q2off, qr * 16);
            q1[i] = ld128(W, k1off - 80 * CH, qr * 16);
        }
        const int t0 = kh * 32;
        unsigned fnx = epoch;
        SBAR();
        UNROLL for (int b = 0; b < NB; ++b) {
            k2[b] = ld64(W, k2off, (t0 + b) * 512); k1[b] = ld128(W, k1off, (t0 + b) * 512); SBAR();
            if (b < NB - 1) { v0[b] = ld128(W, va, 0); v1[b] = ld128(W, vb2, 0); va += vstep; vb2 += vstep; SBAR(); }
        }
        const f32x16 zero16 = f32x16{};
        {
            f32x16 t[4];
            UNROLL for (int i = 0; i < 4; ++i) { t[i] = MFMA32K8(as4(k2[0]), as4(q2[i]), zero16); t[i] = MFMA32(as8(k1[0]), as8(q1[i]), t[i]); SBAR(); }
            k2[0][0] |= k2or;
            UNROLL for (int i = 0; i < 4; ++i) {
                float m = t[i][0];
                UNROLL for (int e = 1; e < 16; ++e) m = fmaxf(m, t[i][e]);
                auto mm = __builtin_amdgcn_permlane32_swap(__float_as_uint(m), __float_as_uint(m), false, false);
                m = fmaxf(__uint_as_float(mm[0]), __uint_as_float(mm[1]));
                q2[i][0] = hh ? pack2(-m, 0.f) & 0xFFFFu : q2[i][0];
                if (i == 0) {
                    s[0] = MFMA32K8(as4(k2[0]), as4(q2[0]), zero16);
                    s[0] = MFMA32(as8(k1[0]), as8(q1[0]), s[0]);
                }
            }
        }
        dma4(lane < 8 ? lane * 128 : OOB, dws, WS_O + cH * CH + (q0 + kh * 64) * 16, lds0 + LDS_SINK);
        st32(W, epoch, lane == 0 ? WS_ACK + (blockIdx.x * 8 + wid) * 4 : OOB, 0);
        __builtin_amdgcn_s_setprio(1);
        STAMP(4);
        NOUNROLL for (int tb = 0; tb < 32; tb += NB) {
            if (__builtin_expect(!__all(fnx == epoch), 0)) wait_flags1<SL1>(W, fkv + (tb < 28 ? tb + 4 : 0) * (FS / 2), epoch);
            if (tb == 16) lds_wait(L + FL_FREE + fl4, 1);
            const unsigned fso = (tb < 24 ? tb + 8 : 0) * (FS / 2);
            SBAR();
            UNROLL for (int b = 0; b < NB; ++b) {
                const int bn = (b + 1) % NB, bp = (b + NB - 1) % NB;
                const unsigned kso = tb + b + NB < 32 ? (t0 + tb + b + NB) * 512 : OOB / 2;
                const unsigned vso = tb + b + NB - 1 < 32 ? 0 : OOB / 2;
                const unsigned cso = tb >= 16 ? OOB / 2 : CPF == 1 ? cs0 + (nt4 * 32 + gwv * 8 + (b & 1) * 4) * 4096 + ((tb + b) >> 1) * 256 :
                                     CPF == 2 ? cs0 + (nt4 * 32 + gwv * 8 + ((tb + b) & 7)) * 4096 + (tb >> 3) * 1024 :
                                                cs0 + (nt4 * 32 + gwv * 8 + b * 2) * 4096 + (tb >> 2) * 512;
                UNROLL for (int qi = 0; qi < 4; ++qi) {
                    const int c = qi & 1, n = c ^ 1, qn = (qi + 1) & 3, qp = (qi + 3) & 3;
                    const int kb = qi == 3 ? bn : b, vb = qi == 0 ? bp : b;
                    f32x16& SC = s[c]; f32x16& SN = s[n];
                    SN = MFMA32K8(as4(k2[kb]), as4(q2[qn]), zero16); SBAR();
                    SC[0] = EX2(SC[0]); SC[1] = EX2(SC[1]); SC[2] = EX2(SC[2]); SC[3] = EX2(SC[3]); SBAR();
                    pd[0] = pack2(e8[0], e8[1]); pd[1] = pack2(e8[2], e8[3]); PIN(SC); PIN(pd); SBAR();
                    SN = MFMA32(as8(k1[kb]), as8(q1[qn]), SN); SBAR();
                    if (qi == 2) { k2[b] = ld64(W, k2off, kso); k1[b] = ld128(W, k1off, kso); SBAR(); }
                    if (qi == 3 && b == 0) { fnx = ld32(W, fkv, fso); SBAR(); }
                    if (CPF && qi == 1) { dma4(cpf, dc, cso, lds0 + LDS_SINK); SBAR(); }
                    SC[4] = EX2(SC[4]); SC[5] = EX2(SC[5]); SC[6] = EX2(SC[6]); SC[7] = EX2(SC[7]); SBAR();
                    pd[2] = pack2(e8[4], e8[5]); pd[3] = pack2(e8[6], e8[7]); PIN(SC); PIN(pd); SBAR();
                    o[qp] = MFMA32(as8(v0[vb]), as8(pa), o[qp]); SBAR();
                    e8[0] = EX2(SC[8]); e8[1] = EX2(SC[9]); e8[2] = EX2(SC[10]); e8[3] = EX2(SC[11]); SBAR();
                    pa[0] = pack2(SC[0], SC[1]); pa[1] = pack2(SC[2], SC[3]);
                    if (qi == 2) { k2[bn][0] |= k2or; PIN(k2[bn]); }
                    if (qi == 1) { va += vstep; vb2 += vstep; PIN(va); PIN(vb2); }
                    PIN(e8); PIN(pa); SBAR();
                    o[qp] = MFMA32(as8(v1[vb]), as8(pd), o[qp]); SBAR();
                    if (qi == 0) { v0[bp] = ld128(W, va, vso); v1[bp] = ld128(W, vb2, vso); SBAR(); }
                    e8[4] = EX2(SC[12]); e8[5] = EX2(SC[13]); e8[6] = EX2(SC[14]); e8[7] = EX2(SC[15]); SBAR();
                    pa[2] = pack2(SC[4], SC[5]); pa[3] = pack2(SC[6], SC[7]); PIN(e8); PIN(pa); SBAR();
                }
            }
        }
        __builtin_amdgcn_s_setprio(3);
        UNROLL for (int e = 0; e < 4; ++e) pd[e] = pack2(e8[2 * e], e8[2 * e + 1]);
        o[3] = MFMA32(as8(v0[NB - 1]), as8(pa), o[3]);
        o[3] = MFMA32(as8(v1[NB - 1]), as8(pd), o[3]);
        SBAR();
        STAMP(5);
        const int f0 = q0 + kh * 64;
        UNROLL for (int j = 0; j < 2; ++j) {
            const lds_p ex = LW + LDS_EX + (gwv * 2 + j) * EXB;
            *(__attribute__((address_space(3))) u32x4*)(ex + lane * 16) =
                u32x4{pack2(o[j][0], o[j][1]), pack2(o[j][2], o[j][3]), pack2(o[j][4], o[j][5]), pack2(o[j][6], o[j][7])};
            *(__attribute__((address_space(3))) u32x2*)(ex + 1024 + lane * 8) =
                hh ? u32x2{__float_as_uint(o[j][8]), q2[j][0] << 16} : u32x2{pack2(o[j][8], o[j][9]), pack2(o[j][10], o[j][11])};
        }
        lds_signal(LW + FL_EX + gwv * 4);
        float n1[2], l1[2];
        UNROLL for (int j = 0; j < 2; ++j) {
            auto nn = __builtin_amdgcn_permlane32_swap(q2[2 + j][0] << 16, q2[2 + j][0] << 16, false, false);
            auto ll = __builtin_amdgcn_permlane32_swap(__float_as_uint(o[2 + j][8]), __float_as_uint(o[2 + j][8]), false, false);
            n1[j] = __uint_as_float(nn[1]); l1[j] = __uint_as_float(ll[1]);
        }
        const unsigned oa = WS_O + (cA + hh) * CH + (f0 + r) * 16, ob = hh ? OOB : WS_O + cH * CH + (f0 + r) * 16 + odd * 8;
        lds_wait(L + FL_EX + (gwv ^ 2) * 4);
        STAMP2(22);
        bool ok = true;
        u32x4 ra[2]; u32x2 rb[2];
        UNROLL for (int j = 0; j < 2; ++j) {
            const lds_cp rx = L + LDS_EX + ((gwv ^ 2) * 2 + j) * EXB;
            const u32x4 a = *(const __attribute__((address_space(3))) u32x4*)(rx + lane * 16);
            const u32x2 c = *(const __attribute__((address_space(3))) u32x2*)(rx + 1024 + r * 8);
            const u32x2 d = *(const __attribute__((address_space(3))) u32x2*)(rx + 1280 + r * 8);
            const float n2 = __uint_as_float(d[1]), l2 = __uint_as_float(d[0]), nm = fminf(n1[j], n2);
            float w1 = EX2(nm - n1[j]), w2 = EX2(nm - n2);
            const float l = l1[j] * w1 + l2 * w2, rl = __builtin_amdgcn_rcpf(l);
            w1 *= rl; w2 *= rl;
            float f[12];
            UNROLL for (int e = 0; e < 4; ++e) { f[2 * e] = o[2 + j][2 * e] * w1 + bflo(a[e]) * w2; f[2 * e + 1] = o[2 + j][2 * e + 1] * w1 + bfhi(a[e]) * w2; }
            UNROLL for (int e = 0; e < 2; ++e) { f[8 + 2 * e] = o[2 + j][8 + 2 * e] * w1 + bflo(c[e]) * w2; f[9 + 2 * e] = o[2 + j][9 + 2 * e] * w1 + bfhi(c[e]) * w2; }
            float sa = 0.f;
            UNROLL for (int e = 0; e < 12; ++e) sa += fabsf(f[e]);
            ok = ok && l1[j] < 1e30f && l2 < 1e30f && l > 1e-18f && sa < 3e38f;
            auto s0 = __builtin_amdgcn_permlane32_swap(pack2(f[0], f[1]), pack2(f[4], f[5]), false, false);
            auto s1 = __builtin_amdgcn_permlane32_swap(pack2(f[2], f[3]), pack2(f[6], f[7]), false, false);
            ra[j] = u32x4{s0[0], s1[0], s0[1], s1[1]};
            rb[j] = u32x2{pack2(f[8], f[9]), pack2(f[10], f[11])};
        }
        if (__builtin_expect(__all(ok), 1)) {
            UNROLL for (int j = 0; j < 2; ++j) { st128(W, ra[j], oa + j * 512, 0); st64(W, rb[j], ob + j * 512, 0); }
        } else {
            wait_flags1<SL1>(W, WS_F1 + ((10 + hh * 10 + (col0 >> 6)) * 32 + r) * FS, epoch);
            wait_flags1<SL1>(W, WS_F1 + ((10 + hh * 10 + ((col0 + 19) >> 6)) * 32 + r) * FS, epoch);
            NOUNROLL for (int j = 0; j < 2; ++j) {
                const u32x2 z2 = ld64(W, q2off, (f0 + j * 32) * 16);
                const u32x4 z1 = ld128(W, k1off - 80 * CH, (f0 + j * 32) * 16);
                float m = -3e38f;
                f32x16 oo = f32x16{};
                NOUNROLL for (int t = 0; t < 64; ++t) {
                    const u32x4 a1 = ld128(W, k1off, t * 512); const u32x2 a2 = ld64(W, k2off, t * 512);
                    const u32x4 w0 = ld128(W, voff + t * vstep, 0), w1 = ld128(W, voff + t * vstep + (vstep >> 1), 0);
                    f32x16 z = MFMA32K8(as4(a2), as4(z2), zero16);
                    z = MFMA32(as8(a1), as8(z1), z);
                    float mx = z[0];
                    UNROLL for (int e = 1; e < 16; ++e) mx = fmaxf(mx, z[e]);
                    auto mm = __builtin_amdgcn_permlane32_swap(__float_as_uint(mx), __float_as_uint(mx), false, false);
                    const float mn = fmaxf(m, fmaxf(__uint_as_float(mm[0]), __uint_as_float(mm[1])));
                    const float al = EX2(m - mn);
                    m = mn;
                    UNROLL for (int e = 0; e < 16; ++e) oo[e] *= al;
                    u32x4 p0, p1;
                    UNROLL for (int e = 0; e < 4; ++e) { p0[e] = pack2(EX2(z[2 * e] - m), EX2(z[2 * e + 1] - m)); p1[e] = pack2(EX2(z[8 + 2 * e] - m), EX2(z[9 + 2 * e] - m)); }
                    oo = MFMA32(as8(w0), as8(p0), oo);
                    oo = MFMA32(as8(w1), as8(p1), oo);
                }
                auto l2 = __builtin_amdgcn_permlane32_swap(__float_as_uint(oo[8]), __float_as_uint(oo[8]), false, false);
                const float sc = 1.0f / __uint_as_float(l2[1]);
                auto s0 = __builtin_amdgcn_permlane32_swap(pack2(oo[0] * sc, oo[1] * sc), pack2(oo[4] * sc, oo[5] * sc), false, false);
                auto s1 = __builtin_amdgcn_permlane32_swap(pack2(oo[2] * sc, oo[3] * sc), pack2(oo[6] * sc, oo[7] * sc), false, false);
                st128(W, u32x4{s0[0], s1[0], s0[1], s1[1]}, oa + j * 512, 0);
                st64(W, u32x2{pack2(oo[8] * sc, oo[9] * sc), pack2(oo[10] * sc, oo[11] * sc)}, ob + j * 512, 0);
            }
        }
        STAMP(6);
        publish(W, WS_F3 + ((f0 >> 6) * 32 + head) * FS, epoch, lane);
        STAMP(7);
        if (blockIdx.x == 0 && wid == 4) {
            NOUNROLL for (unsigned spins = 0; spins < SPIN_CAP; ++spins) {
                asm volatile("" ::: "memory");
                bool ok = true;
                UNROLL for (int i = 0; i < 8; ++i) {
                    const u32x4 f = ld128(W, WS_ACK + i * 1024 + lane * 16, 0);
                    ok = ok && f[0] == epoch && f[1] == epoch && f[2] == epoch && f[3] == epoch;
                }
                if (__all(ok)) break;
                __builtin_amdgcn_s_sleep(32);
            }
            if (lane == 0) st32(W, epoch, WS_EP, 0);
        }
        return;
    }

    __builtin_amdgcn_s_setprio(2);
    u32x4 wf[5][5];
    {
        unsigned coff[2]; const unsigned lin = lane * 16;
        UNROLL for (int i = 0; i < 2; ++i) coff[i] = (lane >> 3) * 4096 + (((lane & 7) ^ ((4 * i + (lane >> 4)) & 7)) << 4);
#define SQ_DC(n, slot, i) dma16(coff[(i) & 1], dc, cs0 + gwv * 131072 + (i) * 32768 + (n) * 128, lds0 + (slot) * SQ_STAGE + gwv * 4096 + (i) * 1024)
#define SQ_DA(n, slot, i) dma16s(lin, dws, as0 + (n) * 20480 + gwv * 5120 + (i) * 1024, lds0 + (slot) * SQ_STAGE + 16384 + gwv * 5120 + (i) * 1024)
        f32x16 acc[5];
        UNROLL for (int u = 0; u < 5; ++u) { acc[u] = f32x16{}; PIN(acc[u]); }
        unsigned rowa[4];
        UNROLL for (int ks = 0; ks < 4; ++ks) rowa[ks] = gwv * 4096 + r * 128 + (((2 * ks + hh) ^ ((r >> 1) & 7)) << 4);
        const unsigned ard = 16384 + hh * 2560 + r * 16;
        s16x8 fc[2], fa[2][5];
        fc[1] = s16x8{};
        UNROLL for (int u = 0; u < 5; ++u) fa[1][u] = s16x8{};
        if (wid == 0) {
            const unsigned f2 = WS_F2 + (((ksp >> 1) * 8 + nt4 * 2 + hh) * 64 + (ksp & 1) * 32 + r) * FS;
            __builtin_amdgcn_s_sleep(16);
            wait_flags1<12>(W, f2, epoch);
            lds_wait(L + FL_FREE + fl4, 1);
            lds_post(LW + FL_FIN, 1);
        } else {
            lds_wait(L + FL_FIN, 1);
        }
        STAMP(3);
        UNROLL for (int n = 0; n < 2; ++n) { UNROLL for (int i = 0; i < 4; ++i) SQ_DC(n, n, i); UNROLL for (int i = 0; i < 5; ++i) SQ_DA(n, n, i); }
        const lds_p pw = LW + (lane == 0 ? FL_SFULL + gwv * 4 : LDS_SINK + lane * 4);
        const lds_cp pr = L + FL_SFULL + fl4;
        const unsigned wfo = WS_WOT + (ct * 4 + gwv) * 25600 + lane * 16;
        int slot = 0;
#define PACE 0
#define SQ_MF(c, u) do { acc[u] = MFMA32(fa[c][u], fc[c], acc[u]); if (PACE && ((u) & PMASK) == 0) { SBAR(); __builtin_amdgcn_s_sleep(PACE); } SBAR(); } while (0)
#define PMASK 0
#define SQ_RA(n, u, ks) fa[n][u] = lds128(sp + (u) * 512 + (ks) * 5120)
#define SQ_STG(DMA) do { \
            __builtin_amdgcn_s_setprio(3); \
            lds_signal(pw, j + 1); lds_wait(pr, j + 1); \
            lds_cp sp = L + slot * SQ_STAGE + ard; lds_cp sr[4]; \
            UNROLL for (int ks = 0; ks < 4; ++ks) { sr[ks] = L + slot * SQ_STAGE + rowa[ks]; PIN(sr[ks]); } \
            PIN(sp); SBAR(); \
            __builtin_amdgcn_s_setprio(GPRIO); \
            const int sf = slot == 0 ? 2 : slot - 1; \
            UNROLL for (int ks = 0; ks < 4; ++ks) { \
                const int n = ks & 1, c = n ^ 1; \
                fc[n] = lds128(sr[ks]); SQ_RA(n, 0, ks); SBAR(); \
                SQ_MF(c, 0); \
                SQ_RA(n, 1, ks); SQ_RA(n, 2, ks); SBAR(); \
                SQ_MF(c, 1); \
                SQ_RA(n, 3, ks); SQ_RA(n, 4, ks); SBAR(); \
                SQ_MF(c, 2); \
                if (DMA) { SQ_DC(j + 2, sf, ks); SBAR(); } \
                SQ_MF(c, 3); \
                if (DMA) { SQ_DA(j + 2, sf, ks); if (ks == 3) SQ_DA(j + 2, sf, 4); SBAR(); } \
                SQ_MF(c, 4); \
            } \
            slot = slot == 2 ? 0 : slot + 1; } while (0)
        int j = 0;
        NOUNROLL for (; j < 14; ++j) { WAITV(9); SQ_STG(true); }
        __builtin_amdgcn_s_setprio(3);
        if (wid == 0) {
            const int px = r >> 2;
            const unsigned fw = WS_F2 + (((px >> 1) * 4 + hh) * 64 + ((px & 1) * 8 + ct) * 4 + (r & 3)) * FS;
            const unsigned fg = WS_F2 + (15 * 64 + (tt >> 1) * 4 + (r & 3)) * FS;
            wait_flags4<12>(W, fw, fw + 128 * FS, fg, fg, epoch);
            lds_post(LW + FL_FIN, 2);
        } else {
            lds_wait(L + FL_FIN, 2);
        }
        UNROLL for (int st = 0; st < 5; ++st) UNROLL for (int u = 0; u < 5; ++u) wf[st][u] = ld128(W, wfo, (st * 5 + u) * 1024);
        SBAR();
        NOUNROLL for (; j < 16; ++j) { if (j == 14) { WAITV(34); } else { WAITV(25); } SQ_STG(false); }
        UNROLL for (int u = 0; u < 5; ++u) acc[u] = MFMA32(fa[1][u], fc[1], acc[u]);
        __builtin_amdgcn_s_setprio(3);
        lds_signal(pw, 17);
        STAMP(4);
        const unsigned yb = WS_Y + (ksp * 80 + nt4 * 20 + hh) * CH + (mt * 128 + gwv * 32 + r) * 16;
        UNROLL for (int u = 0; u < 5; ++u)
            UNROLL for (int g = 0; g < 4; g += 2) {
                unsigned a0 = pack2(acc[u][4 * g], acc[u][4 * g + 1]), a1 = pack2(acc[u][4 * g + 2], acc[u][4 * g + 3]);
                unsigned b0 = pack2(acc[u][4 * g + 4], acc[u][4 * g + 5]), b1 = pack2(acc[u][4 * g + 6], acc[u][4 * g + 7]);
                auto s0 = __builtin_amdgcn_permlane32_swap(a0, b0, false, false);
                auto s1 = __builtin_amdgcn_permlane32_swap(a1, b1, false, false);
                st128(W, u32x4{s0[0], s1[0], s0[1], s1[1]}, yb, (u * 4 + g) * CH);
            }
        publish(W, WS_F4 + ((((mt * 2 + (gwv >> 1)) * 4 + nt4) * 4 + ksp) * 2 + (gwv & 1)) * FS, epoch, lane);
        lds_wait(pr, 17);
        STAMP(5);
    }
    const int t0 = tt * 64;
    f32x4 ys[5];
    float gt;
    {
        if (wid == 0) { wait_flags1<16>(W, WS_F4 + ((tt * 4 + (ct >> 1)) * 8 + (lane & 7)) * FS, epoch); lds_post(LW + FL_FIN, 3); }
        else lds_wait(L + FL_FIN, 3);
        const unsigned yo = WS_Y + (ct * 10 + (kg >> 1)) * CH + (t0 + gwv * 16 + l15) * 16 + (kg & 1) * 8;
        u32x2 y[5][4];
        UNROLL for (int u = 0; u < 5; ++u) UNROLL for (int p = 0; p < 4; ++p) y[u][p] = ld64(W, yo, (p * 80 + u * 2) * CH);
        gt = __uint_as_float(ld32(W, WS_G + (t0 + gwv * 16 + l15) * 4, 0));
        const float h = 1.0f - gt;
        UNROLL for (int u = 0; u < 5; ++u) {
            ys[u][0] = h * ((bflo(y[u][0][0]) + bflo(y[u][1][0])) + (bflo(y[u][2][0]) + bflo(y[u][3][0])));
            ys[u][1] = h * ((bfhi(y[u][0][0]) + bfhi(y[u][1][0])) + (bfhi(y[u][2][0]) + bfhi(y[u][3][0])));
            ys[u][2] = h * ((bflo(y[u][0][1]) + bflo(y[u][1][1])) + (bflo(y[u][2][1]) + bflo(y[u][3][1])));
            ys[u][3] = h * ((bfhi(y[u][0][1]) + bfhi(y[u][1][1])) + (bfhi(y[u][2][1]) + bfhi(y[u][3][1])));
            PIN(ys[u]);
        }
    }
    STAMP(6);
    lds_wait(L + FL_EX + fl4, 1);
    if (wid == 0) { wait_flags1<SL3>(W, WS_F3 + (tt * 32 + r) * FS, epoch); lds_post(LW + FL_FIN, 4); }
    else lds_wait(L + FL_FIN, 4);
    STAMP(8);
    const unsigned boff = WS_O + (gwv * 20 + kg) * CH + (t0 + l15) * 16;
    u32x4 bf[4][5];
    UNROLL for (int d = 0; d < 4; ++d)
        UNROLL for (int st = 0; st < 5; ++st) bf[d][st] = ld128(W, boff + ((gwv + d + 1) & 3) * 256, st * 4 * CH);
    SBAR();
    f32x4 res[5];
    UNROLL for (int d = 0; d < 4; ++d) {
        UNROLL for (int u = 0; u < 5; ++u) res[u] = f32x4{};
        UNROLL for (int st = 0; st < 5; ++st) UNROLL for (int u = 0; u < 5; ++u) res[u] = MFMA16(as8(wf[st][u]), as8(bf[d][st]), res[u]);
        if (d < 3) { UNROLL for (int u = 0; u < 5; ++u) stsf4(LW + ((gwv + d + 1) & 3) * GW_LDS + (d * 5 + u) * 1024 + lane * 16, res[u]); }
        if (d == 2) lds_signal(LW + FL_RED + gwv * 4);
        SBAR();
    }
    STAMP(9);
    lds_wait(L + FL_RED + fl4);
    UNROLL for (int d = 0; d < 3; ++d) UNROLL for (int u = 0; u < 5; ++u) res[u] += ldsf4(L + gwv * GW_LDS + (d * 5 + u) * 1024 + lane * 16);
    UNROLL for (int u = 0; u < 5; ++u) res[u] = res[u] * gt + ys[u];
    char* dst = out + (size_t)(t0 + gwv * 16 + l15) * 1280 + ct * 160 + kg * 8;
    UNROLL for (int u = 0; u < 5; ++u) *(u32x2*)(dst + u * 32) = u32x2{pack2(res[u][0], res[u][1]), pack2(res[u][2], res[u][3])};
    STAMP(10);
}

extern "C" void kernel_launch(void* const* d_in, const int* in_sizes, int n_in, void* d_out, int out_size,
                              void* d_ws, size_t ws_size, hipStream_t stream) {
    (void)hipFuncSetAttribute((const void*)fused, hipFuncAttributeMaxDynamicSharedMemorySize, LDS_BYTES);
    fused<<<256, 512, LDS_BYTES, stream>>>((const char*)d_in[0], (const char*)d_in[1], (const char*)d_in[2], (const char*)d_in[4],
                                           (const char*)d_in[5], (const char*)d_in[6], (const char*)d_in[7], (const char*)d_in[8],
                                           (char*)d_ws + WS_BASE(ws_size), (char*)d_out);
}
```

```cpp
#include <hip/hip_runtime.h>
#include <hip/hip_bf16.h>
#include <stdint.h>

typedef short s16x8 __attribute__((ext_vector_type(8)));
typedef short s16x4 __attribute__((ext_vector_type(4)));
typedef float f32x16 __attribute__((ext_vector_type(16)));
typedef float f32x8 __attribute__((ext_vector_type(8)));
typedef float f32x4 __attribute__((ext_vector_type(4)));
typedef unsigned u32x4 __attribute__((ext_vector_type(4)));
typedef unsigned u32x2 __attribute__((ext_vector_type(2)));
typedef __bf16 bf16x2_t __attribute__((ext_vector_type(2)));
typedef __attribute__((address_space(3))) const char* lds_cp;
typedef __attribute__((address_space(3))) char* lds_p;
typedef __amdgpu_buffer_rsrc_t rsrc_t;

#define DI __device__ __forceinline__
#define UNROLL _Pragma("unroll")
#define NOUNROLL _Pragma("unroll 1")
#define MFMA32(a, b, c) __builtin_amdgcn_mfma_f32_32x32x16_bf16((a), (b), (c), 0, 0, 0)
#define MFMA32K8(a, b, c) __builtin_amdgcn_mfma_f32_32x32x8bf16_1k((a), (b), (c), 0, 0, 0)
#define MFMA16(a, b, c) __builtin_amdgcn_mfma_f32_16x16x32_bf16((a), (b), (c), 0, 0, 0)
#define SBAR() __builtin_amdgcn_sched_barrier(0)
#define PIN(x) asm volatile("" : "+v"(x))
#define EX2(x) __builtin_amdgcn_exp2f(x)
#define SC1 16

constexpr unsigned CH = 2048 * 16;
constexpr unsigned WS_QK = 0;
constexpr unsigned WS_O = 160 * CH;
constexpr unsigned WS_Y = WS_O + 80 * CH;
constexpr unsigned ABROW = 640 * 16;
constexpr unsigned WS_AB = WS_Y + 320 * CH;
constexpr unsigned VROW = 10240;
constexpr unsigned WS_VT = WS_AB + 512 * ABROW;
constexpr unsigned WS_WOT = WS_VT + 256 * VROW;
constexpr unsigned WS_G = WS_WOT + 80 * ABROW;
constexpr unsigned FS = 128;
constexpr unsigned WS_ACK = WS_G + 8192;
constexpr unsigned WS_EP = WS_ACK + 8192;
constexpr unsigned WS_F1 = WS_EP + 4096;
constexpr unsigned WS_F2 = WS_F1 + 1024 * FS;
constexpr unsigned WS_F3 = WS_F2 + 1024 * FS;
constexpr unsigned WS_F4 = WS_F3 + 1024 * FS;
constexpr unsigned WS_ONE = WS_F4 + 1024 * FS;
constexpr unsigned WS_END = WS_ONE + 2048 * 128;
constexpr unsigned OOB = 0x80000000u;
#define WSTAIL 1
#define WS_BASE(n) (WSTAIL ? (((n) - WS_END) & ~(size_t)0x1FFFFF) : (size_t)0)

constexpr int P1_STAGE = 32768;
constexpr int SQ_STAGE = 36864;
constexpr int GW_LDS = 15360;
constexpr int EXB = 1536;
constexpr int LDS_EX = 4 * P1_STAGE;
constexpr int LDS_X9 = 4 * P1_STAGE;
constexpr int LDS_GW = LDS_X9 + 20480;
constexpr int LDS_WT = LDS_GW + 2048;
constexpr int LDS_FL = LDS_WT + 4096;
constexpr int LDS_SINK = LDS_FL + 256;
constexpr int LDS_BYTES = LDS_SINK + 256;
static_assert(LDS_BYTES <= 163840 && LDS_EX >= 3 * SQ_STAGE && LDS_FL >= LDS_EX + 8 * EXB, "lds");
constexpr int FL_FULL = LDS_FL, FL_GO = LDS_FL + 32, FL_EX = LDS_FL + 48, FL_SFULL = LDS_FL + 80,
              FL_SFREE = LDS_FL + 96, FL_FIN = LDS_FL + 112, FL_RED = LDS_FL + 128, FL_FREE = LDS_FL + 144;
#define GPRIO 0
#define FWARM 1
#define TLBG 2097152
#define TLBW 1
#define SL1 4
#define SL3 4
#define SJ 4
#define PUBZ 0
#define AABL 0
#define SABL 0
#define PFX 1
#define CPF 0
#define CODE_LINES 300

#define STAMP(i) do { } while (0)
#define TACC(a) do { } while (0)
#define TDECL() do { } while (0)
#define TOUT(i) do { } while (0)
#define STAMP2(i) do { } while (0)

DI unsigned pack2(float a, float b) {
    bf16x2_t v = {(__bf16)a, (__bf16)b};
    return __builtin_bit_cast(unsigned, v);
}
DI float bflo(unsigned u) { return __uint_as_float(u << 16); }
DI float bfhi(unsigned u) { return __uint_as_float(u & 0xFFFF0000u); }
DI s16x8 as8(u32x4 v) { return __builtin_bit_cast(s16x8, v); }
DI s16x4 as4(u32x2 v) { return __builtin_bit_cast(s16x4, v); }

DI u32x4 mkdesc(const void* p, unsigned bytes) {
    const unsigned long long a = (unsigned long long)p;
    return u32x4{(unsigned)a, (unsigned)(a >> 32) & 0xFFFFu, bytes, 0x00020000u};
}
DI void dma16(unsigned voff, u32x4 desc, unsigned soff, unsigned ldsaddr) {
    unsigned keep;
    asm volatile("s_mov_b32 %0, m0\n\ts_mov_b32 m0, %4\n\ts_nop 0\n\tbuffer_load_dwordx4 %1, %2, %3 offen lds\n\ts_mov_b32 m0, %0"
                 : "=&s"(keep) : "v"(voff), "s"(desc), "s"(soff), "s"(ldsaddr) : "memory");
}
DI void dma16s(unsigned voff, u32x4 desc, unsigned soff, unsigned ldsaddr) {
    unsigned keep;
    asm volatile("s_mov_b32 %0, m0\n\ts_mov_b32 m0, %4\n\ts_nop 0\n\tbuffer_load_dwordx4 %1, %2, %3 offen sc1 lds\n\ts_mov_b32 m0, %0"
                 : "=&s"(keep) : "v"(voff), "s"(desc), "s"(soff), "s"(ldsaddr) : "memory");
}
DI void dma4(unsigned voff, u32x4 desc, unsigned soff, unsigned ldsaddr) {
    unsigned keep;
    asm volatile("s_mov_b32 %0, m0\n\ts_mov_b32 m0, %4\n\ts_nop 0\n\tbuffer_load_dword %1, %2, %3 offen sc1 lds\n\ts_mov_b32 m0, %0"
                 : "=&s"(keep) : "v"(voff), "s"(desc), "s"(soff), "s"(ldsaddr) : "memory");
}
#define WAITV(n) asm volatile("s_waitcnt vmcnt(" #n ")" ::: "memory")
#define WAITL() asm volatile("s_waitcnt lgkmcnt(0)" ::: "memory")
#define BARRIER() asm volatile("s_waitcnt lgkmcnt(0)\n\ts_barrier" ::: "memory")
#define BARRIER_NOWAIT() asm volatile("s_barrier" ::: "memory")

DI s16x8 lds128(lds_cp p) { return *(const __attribute__((address_space(3))) s16x8*)p; }
DI f32x4 ldsf4(lds_cp p) { return *(const __attribute__((address_space(3))) f32x4*)p; }
DI void stsf4(lds_p p, f32x4 v) { *(__attribute__((address_space(3))) f32x4*)p = v; }
DI s16x4 ldstr(lds_cp p) {
    return __builtin_bit_cast(s16x4, __builtin_amdgcn_ds_read_tr16_b64_v4i16((__attribute__((address_space(3))) s16x4*)p));
}
DI s16x8 cat8(s16x4 a, s16x4 b) { return __builtin_shufflevector(a, b, 0, 1, 2, 3, 4, 5, 6, 7); }
DI u32x4 ld128(rsrc_t r, unsigned voff, unsigned soff) { return __builtin_amdgcn_raw_buffer_load_b128(r, voff, soff, SC1); }
DI u32x2 ld64(rsrc_t r, unsigned voff, unsigned soff) { return __builtin_amdgcn_raw_buffer_load_b64(r, voff, soff, SC1); }
DI unsigned ld32(rsrc_t r, unsigned voff, unsigned soff) { return __builtin_amdgcn_raw_buffer_load_b32(r, voff, soff, SC1); }
DI void st128(rsrc_t r, u32x4 v, unsigned voff, unsigned soff) { __builtin_amdgcn_raw_buffer_store_b128(v, r, voff + soff, 0, SC1); }
DI void st64(rsrc_t r, u32x2 v, unsigned voff, unsigned soff) { __builtin_amdgcn_raw_buffer_store_b64(v, r, voff, soff, SC1); }
DI void st32(rsrc_t r, unsigned v, unsigned voff, unsigned soff) { __builtin_amdgcn_raw_buffer_store_b32(v, r, voff, soff, SC1); }

constexpr unsigned SPIN_CAP = 200000;
DI void publish(rsrc_t w, unsigned flag_off, unsigned epoch, int lane) {
    WAITV(0);
    if (lane == 0) st32(w, epoch, flag_off, 0);
}
template <int SL>
DI void wait_flags4(rsrc_t w, unsigned o0, unsigned o1, unsigned o2, unsigned o3, unsigned epoch) {
    NOUNROLL for (unsigned spins = 0; spins < SPIN_CAP; ++spins) {
        asm volatile("" ::: "memory");
        const unsigned a = ld32(w, o0, 0), b = ld32(w, o1, 0), c = ld32(w, o2, 0), d = ld32(w, o3, 0);
        if (__all(a == epoch && b == epoch && c == epoch && d == epoch)) break;
        __builtin_amdgcn_s_sleep(SL);
    }
    asm volatile("" ::: "memory");
}
template <int SL>
DI void wait_flags1(rsrc_t w, unsigned o0, unsigned epoch) {
    NOUNROLL for (unsigned spins = 0; spins < SPIN_CAP; ++spins) {
        asm volatile("" ::: "memory");
        const unsigned a = ld32(w, o0, 0);
        if (__all(a == epoch)) break;
        __builtin_amdgcn_s_sleep(SL);
    }
    asm volatile("" ::: "memory");
}
DI void lds_signal(lds_p p, unsigned val = 1u) {
    WAITL();
    *(volatile __attribute__((address_space(3))) unsigned*)p = val;
}
DI void lds_post(lds_p p, unsigned val) { *(volatile __attribute__((address_space(3))) unsigned*)p = val; }
DI unsigned lds_peek(lds_cp p) { return *(const volatile __attribute__((address_space(3))) unsigned*)p; }
DI void lds_wait(lds_cp p, unsigned val = 1u) {
    NOUNROLL for (unsigned spins = 0; spins < 4 * SPIN_CAP; ++spins) {
        const unsigned v = *(const volatile __attribute__((address_space(3))) unsigned*)p;
        if (__all(v >= val)) break;
        __builtin_amdgcn_s_sleep(1);
    }
    asm volatile("" ::: "memory");
}

__global__ __launch_bounds__(512, 2) void fused(const char* __restrict__ x, const char* __restrict__ wqkv,
                                                 const char* __restrict__ wo, const char* __restrict__ cre,
                                                 const char* __restrict__ cim, const char* __restrict__ wr,
                                                 const char* __restrict__ wi, const char* __restrict__ gw,
                                                 char* ws, char* __restrict__ out) {
    extern __shared__ __attribute__((aligned(16))) char smem[];
    unsigned long long pc; asm volatile("s_getpc_b64 %0" : "=s"(pc));
    const int tid = threadIdx.x;
    const int wid = __builtin_amdgcn_readfirstlane(tid >> 6);
    const int gwv = wid & 3;
    const int xcd = blockIdx.x & 7, idx = blockIdx.x >> 3;
    const unsigned lds0 = (unsigned)(uintptr_t)smem;
    const lds_cp L = (lds_cp)smem;
    const lds_p LW = (lds_p)smem;
    const u32x4 dx = mkdesc(x, 2048 * 1280);
    const int mt = (xcd & 1) * 8 + (idx & 7), nt = (xcd >> 1) * 4 + (idx >> 3);
    const u32x4 dw = mkdesc(wqkv, 640 * 3840);
    const rsrc_t W = __builtin_amdgcn_make_buffer_rsrc(ws, 0, WS_END, 0x00020000);
    const unsigned oneoff = WS_ONE + (blockIdx.x * 8 + wid) * 128;
    unsigned epoch;
    {
    const int lane = tid & 63;
    unsigned roff[2];
    UNROLL for (int i = 0; i < 2; ++i) roff[i] = (lane >> 3) * 1280 + (((lane & 7) ^ ((4 * i + (lane >> 4)) & 7)) << 4);
    unsigned woff[2];
    UNROLL for (int i = 0; i < 2; ++i) woff[i] = nt == 15 ? OOB : (lane >> 3) * 3840 + (((lane & 7) ^ (((lane >> 4) & 1) << 2) ^ (i << 1)) << 4);
#define P1_DX(st, p, par) dma16(roff[par], dx, (mt * 128 + (p) * 8) * 1280 + (st) * 128, lds0 + ((st) & 3) * P1_STAGE + (p) * 1024)
#define P1_DW(st, p, par) dma16(woff[par], dw, ((st) * 64 + ((p) & 7) * 8) * 3840 + nt * 256 + ((p) >> 3) * 128, lds0 + ((st) & 3) * P1_STAGE + 16384 + (p) * 1024)
    P1_DX(0, 2 * wid, 0); P1_DW(0, 2 * wid, 0); P1_DX(0, 2 * wid + 1, 1); P1_DW(0, 2 * wid + 1, 1);
    STAMP(0);
    const int hh = lane >> 5;
    const int l15 = lane & 15, kg = lane >> 4;
    const unsigned fl4 = (lane & 3) * 4;
    const u32x4 dh = mkdesc(nt < 8 ? wr : wi, 640 * 1280);
#define GET_EPOCH() do { epoch = ld32(W, WS_EP, 0) + 1u; if (lane < 4) st32(W, 0x3F803F80u, oneoff + lane * 4, 0); } while (0)
#define WH_ADDR(q) ((q) < 32 ? 2 * P1_STAGE + (q) * 1024 : (q) < 48 ? 3 * P1_STAGE + 16384 + ((q) - 32) * 1024 : \
                    (q) < 68 ? LDS_X9 + ((q) - 48) * 1024 : (q) < 84 ? 16384 + ((q) - 68) * 1024 : P1_STAGE + 16384 + ((q) - 84) * 1024)

    if (wid >= 4) {
#define P1_ISSUE(st) do { UNROLL for (int i = 0; i < 4; ++i) { P1_DX(st, 4 * gwv + i, i & 1); P1_DW(st, 4 * gwv + i, i & 1); } } while (0)
#define P1_DH(q, par) do { const int q_ = (q), c_ = q_ / 10, p_ = q_ - c_ * 10; \
            dma16(roff[par], dh, ((nt & 7) * 80 + p_ * 8) * 1280 + c_ * 128, lds0 + WH_ADDR(q_)); } while (0)
        P1_ISSUE(1); P1_ISSUE(2); P1_ISSUE(3);
        WAITV(24); BARRIER_NOWAIT();
        NOUNROLL for (int k = 1; k < 7; ++k) { WAITV(16); BARRIER_NOWAIT(); P1_ISSUE(k + 3); }
        WAITV(16); BARRIER_NOWAIT();
        UNROLL for (int i = 0; i < 8; ++i) P1_DH(gwv * 8 + i, i & 1);
        WAITV(16); BARRIER_NOWAIT();
        UNROLL for (int i = 0; i < 8; ++i) P1_DH(32 + gwv * 8 + i, i & 1);
        WAITV(16); BARRIER_NOWAIT();
        UNROLL for (int i = 0; i < 6; ++i) { const int q = 64 + gwv * 6 + i; P1_DH(q < 84 ? q : 82 + (i & 1), i & 1); }
        GET_EPOCH();
        const int wm = gwv & 1, wn = gwv >> 1;
        unsigned sb[2], ss0, ss1;
        if (nt < 10) {
            UNROLL for (int i = 0; i < 2; ++i) sb[i] = WS_QK + (nt * 16 + wn * 8 + (kg >> 1)) * CH + (mt * 128 + wm * 64 + (i ^ wn) * 32 + (kg & 1) * 16 + l15) * 16;
            ss0 = 2 * CH; ss1 = 4 * CH;
        } else {
            UNROLL for (int i = 0; i < 2; ++i) sb[i] = nt == 15 ? OOB : WS_VT + ((mt * 8 + wm * 4 + (i ^ wn) * 2) * 2 + (kg & 1)) * VROW + ((nt - 10) * 128 + wn * 64 + hh * 16 + l15) * 16;
            ss0 = 512; ss1 = 2 * VROW;
        }
        __builtin_amdgcn_s_setprio(3);
        WAITV(0); BARRIER_NOWAIT();
        u32x4 tv[8];
        UNROLL for (int i = 0; i < 8; ++i)
            tv[i] = __builtin_bit_cast(u32x4, lds128(L + (i < 4 ? 3 * P1_STAGE : 0) + wm * 8192 + wn * 4096 + (i & 3) * 1024 + lane * 16));
        UNROLL for (int i = 0; i < 8; ++i) PIN(tv[i]);
        UNROLL for (int i = 0; i < 4; ++i) P1_DH(84 + gwv * 4 + i, i & 1);
        UNROLL for (int i = 0; i < 8; ++i) st128(W, tv[i], sb[i >> 2], (i & 1) * ss0 + ((i >> 1) & 1) * ss1);
        STAMP2(16);
        STAMP(1);
    } else {
        const unsigned sink = lds0 + LDS_SINK;
        const int pi = xcd * 4 + wid, pst = pi / 5, pu = pi - pst * 5;
        if (PFX) {
            const unsigned b = blockIdx.x;
            if (wid == 0) { dma4(lane * 128, dx, b * 10240, sink); dma4(lane < 16 ? lane * 128 : OOB, dx, b * 10240 + 8192, sink); }
            if (wid == 1) { dma4(lane * 128, dw, b * 9600, sink); dma4(lane < 11 ? lane * 128 : OOB, dw, b * 9600 + 8192, sink); }
            if (wid == 2) { dma4(FWARM && lane < 16 ? (b * 16 + lane) * FS : OOB, mkdesc(ws, WS_END), WS_F1, sink);
                dma4(lane * TLBG + (b & 255) * 128, mkdesc(ws, WS_END), 0, sink);
                if (TLBW) {
                    dma4(lane * (TLBG / 4) + b * 128, mkdesc((xcd >> 1) < 2 ? cre : cim, 2048 * 4096), 0, sink);
                    dma4(lane * (TLBG / 8) + b * 128, mkdesc(out, 2048 * 1280), 0, sink);
                    dma4(lane * (TLBG / 8) + b * 128, dh, 0, sink);
                } }
        } else if (wid < 3) { dma4(OOB, dx, 0, sink); dma4(OOB, dx, 0, sink); }
        if (wid == 3) {
            dma4(lane < 12 ? lane * 128 : OOB, mkdesc((const void*)(pc & ~127ull), CODE_LINES * 128), idx * 1536, sink);
            dma16(nt == 15 ? lane * 16 : OOB, mkdesc(gw, 1280), 0, lds0 + LDS_GW);
        }
        if (tid < 64) *(__attribute__((address_space(3))) unsigned*)(LW + LDS_FL + tid * 4) = 0u;
        const int wm = wid & 1, wn = wid >> 1;
        unsigned ra[2];
        UNROLL for (int ks = 0; ks < 2; ++ks) ra[ks] = l15 * 128 + (((4 * ks + kg) ^ ((l15 >> 1) & 7)) << 4);
        const unsigned xk = wm * 8192 + wn * 4096, xo = wm * 8192 + (wn ^ 1) * 4096;
        s16x8 xs[10][2][2];
#define WT_LOAD() dma16((lane >> 1) * 1280 + (lane & 1) * 16, mkdesc(wo, 640 * 1280), \
                  pi < 25 ? ((idx >> 3) * 160 + pst * 32) * 1280 + (idx & 7) * 160 + pu * 32 : OOB, lds0 + LDS_WT + wid * 1024)
#define WT_STORE() do { const lds_cp tp_ = L + LDS_WT + wid * 1024 + (8 * kg + (l15 >> 2)) * 32 + (lane & 3) * 8; \
            st128(W, __builtin_bit_cast(u32x4, cat8(ldstr(tp_), ldstr(tp_ + 128))), \
                  pi < 25 ? WS_WOT + (((idx & 7) * 4 + (idx >> 3)) * 25 + pi) * 1024 + lane * 16 : OOB, 0); } while (0)
        if (nt == 15) {
            const int m0 = mt * 128 + wm * 64 + wn * 32;
            if (wid == 3) dma16(lane * 16, mkdesc(gw, 1280), 1024, lds0 + LDS_GW + 1024);
            WT_LOAD();
            GET_EPOCH();
            {
                const unsigned o0 = (mt * 4 + wid) * 12800, v1 = lane < 36 ? lane * 128 : OOB;
                dma4(lane * 128, mkdesc(wr, 640 * 1280), o0, sink); dma4(v1, mkdesc(wr, 640 * 1280), o0 + 8192, sink);
                dma4(lane * 128, mkdesc(wi, 640 * 1280), o0, sink); dma4(v1, mkdesc(wi, 640 * 1280), o0 + 8192, sink);
            }
            WAITV(4);
            float sum[2] = {0.f, 0.f};
            UNROLL for (int k = 0; k < 10; ++k) {
                BARRIER();
                UNROLL for (int ks = 0; ks < 2; ++ks) {
                    const u32x4 gv = __builtin_bit_cast(u32x4, lds128(L + LDS_GW + k * 128 + (4 * ks + kg) * 16));
                    UNROLL for (int h = 0; h < 2; ++h) {
                        xs[k][ks][h] = lds128(L + (k & 3) * P1_STAGE + xk + h * 2048 + ra[ks]);
                        const u32x4 xv = __builtin_bit_cast(u32x4, xs[k][ks][h]);
                        UNROLL for (int e = 0; e < 4; ++e)
                            { const unsigned xe = xv[e], ge = gv[e]; sum[h] = __builtin_amdgcn_fdot2_f32_bf16(__builtin_bit_cast(bf16x2_t, xe), __builtin_bit_cast(bf16x2_t, ge), sum[h], false); }
                    }
                }
                SBAR();
            }
            UNROLL for (int h = 0; h < 2; ++h) { sum[h] += __shfl_xor(sum[h], 16); sum[h] += __shfl_xor(sum[h], 32); }
            const float sm = kg & 1 ? sum[1] : sum[0];
            if (lane < 32) st32(W, __float_as_uint(1.0f / (1.0f + __expf(-sm))), WS_G + (m0 + lane) * 4, 0);
        } else {
            const int q = l15 >> 2;
            unsigned trb[4];
            UNROLL for (int j = 0; j < 4; ++j)
                trb[j] = 16384 + wn * 8192 + (8 * kg + q) * 128 + ((j ^ (((q >> 1) & 1) << 1) ^ (kg & 1)) << 5) + 8 * (lane & 3);
            f32x4 acc[4][4];
            UNROLL for (int i = 0; i < 4; ++i) UNROLL for (int j = 0; j < 4; ++j) acc[i][j] = f32x4{};
            s16x8 xt[2][2]; s16x4 wv[4][2];
            WT_LOAD();
            GET_EPOCH();
            WAITV(3);
#define P1_RW(z, j) do { const lds_cp p_ = L + (((z) >> 1) & 3) * P1_STAGE + trb[j] + ((z) & 1) * 4096; wv[j][0] = ldstr(p_); wv[j][1] = ldstr(p_ + 512); } while (0)
#define P1_RK(z, h) xs[(z) >> 1][(z) & 1][h] = lds128(L + (((z) >> 1) & 3) * P1_STAGE + xk + (h) * 2048 + ra[(z) & 1])
#define P1_RO(z, h) xt[(z) & 1][h] = lds128(L + (((z) >> 1) & 3) * P1_STAGE + xo + (h) * 2048 + ra[(z) & 1])
#define P1_XF(z, i) ((i) < 2 ? xs[(z) >> 1][(z) & 1][(i) & 1] : xt[(z) & 1][(i) & 1])
#define P1_LOOP(MF) \
            BARRIER(); \
            STAMP2(11); \
            P1_RK(0, 0); P1_RW(0, 0); P1_RK(0, 1); P1_RO(0, 0); P1_RO(0, 1); P1_RW(0, 1); P1_RW(0, 2); P1_RW(0, 3); SBAR(); \
            UNROLL for (int z = 0; z < 20; ++z) { \
                UNROLL for (int j = 0; j < 4; ++j) { \
                    MF(z, 0, j); SBAR(); \
                    if (j == 0 && (z & 1) && z < 19) { BARRIER(); } \
                    MF(z, 1, j); SBAR(); \
                    if (z < 19) { if (j == 0) P1_RK(z + 1, 0); if (j == 1) P1_RK(z + 1, 1); if (j == 2) P1_RO(z + 1, 0); if (j == 3) P1_RO(z + 1, 1); SBAR(); } \
                    MF(z, 2, j); SBAR(); \
                    MF(z, 3, j); SBAR(); \
                    if (z < 19) { P1_RW(z + 1, j); SBAR(); } \
                } \
            }
#define P1_MFQ(z, i, j) acc[i][j] = MFMA16(cat8(wv[j][0], wv[j][1]), P1_XF(z, i), acc[i][j])
#define P1_MFV(z, i, j) acc[i][j] = MFMA16(P1_XF(z, i), cat8(wv[j][0], wv[j][1]), acc[i][j])
            u32x4 tv[8];
            if (nt < 10) {
                P1_LOOP(P1_MFQ)
                STAMP(1);
                const float sc = nt < 5 ? 0.32259889079f : 1.0f;
                UNROLL for (int i = 0; i < 4; i += 2)
                    UNROLL for (int j = 0; j < 4; ++j) {
                        unsigned a0 = pack2(acc[i][j][0] * sc, acc[i][j][1] * sc), a1 = pack2(acc[i][j][2] * sc, acc[i][j][3] * sc);
                        unsigned b0 = pack2(acc[i + 1][j][0] * sc, acc[i + 1][j][1] * sc), b1 = pack2(acc[i + 1][j][2] * sc, acc[i + 1][j][3] * sc);
                        auto s0 = __builtin_amdgcn_permlane16_swap(a0, b0, false, false);
                        auto s1 = __builtin_amdgcn_permlane16_swap(a1, b1, false, false);
                        tv[i * 2 + j] = u32x4{s0[0], s1[0], s0[1], s1[1]};
                    }
            } else {
                P1_LOOP(P1_MFV)
                STAMP(1);
                UNROLL for (int i = 0; i < 4; ++i)
                    UNROLL for (int j = 0; j < 4; j += 2) {
                        unsigned a0 = pack2(acc[i][j][0], acc[i][j][1]), a1 = pack2(acc[i][j][2], acc[i][j][3]);
                        unsigned b0 = pack2(acc[i][j + 1][0], acc[i][j + 1][1]), b1 = pack2(acc[i][j + 1][2], acc[i][j + 1][3]);
                        auto s0 = __builtin_amdgcn_permlane32_swap(a0, b0, false, false);
                        auto s1 = __builtin_amdgcn_permlane32_swap(a1, b1, false, false);
                        tv[i * 2 + (j >> 1)] = u32x4{s0[0], s1[0], s0[1], s1[1]};
                    }
            }
            UNROLL for (int i = 0; i < 8; ++i) PIN(tv[i]);
            UNROLL for (int z = 14; z < 18; ++z) UNROLL for (int h = 0; h < 2; ++h) P1_RK(z, h);
            WAITL();
            UNROLL for (int i = 0; i < 8; ++i)
                *(__attribute__((address_space(3))) u32x4*)(LW + (i < 4 ? 3 * P1_STAGE : 0) + xk + (i & 3) * 1024 + lane * 16) = tv[i];
        }
        BARRIER();
        __builtin_amdgcn_s_setprio(2);
        STAMP(7);
        f32x4 hc[2][5];
        UNROLL for (int h = 0; h < 2; ++h) UNROLL for (int u = 0; u < 5; ++u) hc[h][u] = f32x4{};
        UNROLL for (int z = 18; z < 20; ++z) UNROLL for (int h = 0; h < 2; ++h) P1_RK(z, h);
        s16x8 wb[2][5];
        UNROLL for (int z = 0; z < 21; ++z) {
            if (z == 16) { STAMP2(17); lds_wait(L + FL_FULL + fl4, 1); STAMP2(18); }
            UNROLL for (int u = 0; u < 5; ++u) {
                if (z < 20) { wb[z & 1][u] = lds128(L + WH_ADDR((z >> 1) * 10 + 2 * u) + ra[z & 1]); SBAR(); }
                if (z > 0) {
                    hc[0][u] = MFMA16(xs[(z - 1) >> 1][(z - 1) & 1][0], wb[(z - 1) & 1][u], hc[0][u]);
                    hc[1][u] = MFMA16(xs[(z - 1) >> 1][(z - 1) & 1][1], wb[(z - 1) & 1][u], hc[1][u]); SBAR();
                }
            }
        }
        STAMP2(19);
        WAITV(0);
        WT_STORE();
        lds_signal(LW + FL_FREE + wid * 4, 1);
        const float sg = nt < 8 ? 1.0f : -1.0f;
        const unsigned ab = WS_AB + (((nt & 7) >> 1) * 512 + (nt >> 3) * 256 + mt * 16 + wm * 8 + wn * 4 + (kg & 1) * 2 + (kg >> 1)) * 2560 + ((nt & 1) * 80 + l15) * 16;
        UNROLL for (int u = 0; u < 5; ++u) {
            unsigned a0 = pack2(hc[0][u][0] * sg, hc[0][u][1] * sg), a1 = pack2(hc[0][u][2] * sg, hc[0][u][3] * sg);
            unsigned b0 = pack2(hc[1][u][0] * sg, hc[1][u][1] * sg), b1 = pack2(hc[1][u][2] * sg, hc[1][u][3] * sg);
            auto s0 = __builtin_amdgcn_permlane16_swap(a0, b0, false, false);
            auto s1 = __builtin_amdgcn_permlane16_swap(a1, b1, false, false);
            st128(W, u32x4{s0[0], s1[0], s0[1], s1[1]}, ab, u * 256);
        }
        publish(W, WS_F2 + (nt * 64 + mt * 4 + wid) * FS, epoch, lane);
        if (lane == 0) st32(W, epoch, WS_ACK + (blockIdx.x * 8 + wid) * 4, 0);
    }
    }
    STAMP(2);
    const int lane = __builtin_amdgcn_mbcnt_hi(~0u, __builtin_amdgcn_mbcnt_lo(~0u, 0u));
    const int r = lane & 31, hh = lane >> 5;
    const int l15 = lane & 15, kg = lane >> 4;
    const unsigned fl4 = (lane & 3) * 4;

    const int tt = xcd * 4 + (idx >> 3), ct = idx & 7;
    const int ksp = xcd >> 1, nt4 = idx >> 3;
    const u32x4 dc = mkdesc(ksp < 2 ? cre : cim, 2048 * 4096), dws = mkdesc(ws, WS_END);
    const unsigned cs0 = mt * 128 * 4096 + (ksp & 1) * 2048;
    const unsigned as0 = WS_AB + (nt4 * 512 + ksp * 128) * 2560;

    if (wid >= 4) {
        const int head = xcd * 4 + (idx >> 3), qt = idx & 7;
        const int qh = wid & 1, kh = (wid >> 1) & 1;
        const int col0 = head * 20, odd = head & 1;
        const int cA = (col0 >> 3) + odd, cH = odd ? (col0 >> 3) : (col0 >> 3) + 2;
        const int q0 = qt * 256 + qh * 128;
        unsigned k1off = WS_QK + (80 + cA + hh) * CH + r * 16;
        unsigned k2off = hh ? OOB : WS_QK + (80 + cH) * CH + r * 16 + odd * 8;
        unsigned q2off = hh ? OOB : k2off - 80 * CH;
        unsigned k2or = hh ? 0x3F80u : 0u;
        const int dv = odd ? (r < 16 ? r + 4 : r - 16) : r;
        const unsigned voff = r < 20 ? WS_VT + hh * VROW + (col0 + dv) * 16 : (r == 20 ? oneoff : OOB);
        unsigned vstep = r < 20 ? 4 * VROW : 0;
        unsigned va = voff + kh * 32 * vstep, vb2 = va + (vstep >> 1);
        unsigned cpf = CPF == 1 ? (lane < 8 ? (lane >> 1) * 4096 + (lane & 1) * 128 : OOB) : CPF == 2 ? (lane < 8 ? lane * 128 : OOB) : (lane < 8 ? (lane >> 2) * 4096 + (lane & 3) * 128 : OOB);
        constexpr int NB = 4;
        u32x4 k1[NB], v0[NB], v1[NB]; u32x2 k2[NB];
        f32x16 o[4], s[2];
        u32x4 pa = u32x4{}, pd = u32x4{};
        f32x8 e8 = f32x8{};
        UNROLL for (int i = 0; i < 4; ++i) { o[i] = f32x16{}; PIN(o[i]); }
        v0[NB - 1] = u32x4{}; v1[NB - 1] = u32x4{};
        PIN(v0[NB - 1]); PIN(v1[NB - 1]); PIN(pa); PIN(pd); PIN(e8);
        PIN(k1off); PIN(k2off); PIN(q2off); PIN(k2or); PIN(va); PIN(vb2); PIN(vstep); PIN(cpf);
        WAITV(0);
        if (nt != 15 && lane == 0) st32(W, epoch, WS_F1 + ((nt * 2 + (gwv >> 1)) * 32 + mt * 2 + (gwv & 1)) * FS, 0);
        lds_post(LW + FL_FULL + gwv * 4, 1);
        const int nsa = (lane & 4) ? (col0 + 19) >> 6 : col0 >> 6;
        unsigned fkv = WS_F1 + ((10 + ((lane >> 3) & 1) * 10 + nsa) * 32 + kh * 16) * FS;
        STAMP2(23);
        wait_flags1<SL1>(W, (lane & 16) ? WS_F1 + (nsa * 32 + (q0 >> 6) + (lane & 1)) * FS : fkv + (lane & 3) * FS, epoch);
        fkv += (lane & 1) * FS;
        STAMP(3);
        u32x4 q1[4]; u32x2 q2[4];
        UNROLL for (int i = 0; i < 4; ++i) {
            const int qr = q0 + ((i + 2 + 2 * kh) & 3) * 32;
            q2[i] = ld64(W, q2off, qr * 16);
            q1[i] = ld128(W, k1off - 80 * CH, qr * 16);
        }
        const int t0 = kh * 32;
        unsigned fnx = epoch;
        SBAR();
        UNROLL for (int b = 0; b < NB; ++b) {
            k2[b] = ld64(W, k2off, (t0 + b) * 512); k1[b] = ld128(W, k1off, (t0 + b) * 512); SBAR();
            if (b < NB - 1) { v0[b] = ld128(W, va, 0); v1[b] = ld128(W, vb2, 0); va += vstep; vb2 += vstep; SBAR(); }
        }
        const f32x16 zero16 = f32x16{};
        {
            f32x16 t[4];
            UNROLL for (int i = 0; i < 4; ++i) { t[i] = MFMA32K8(as4(k2[0]), as4(q2[i]), zero16); t[i] = MFMA32(as8(k1[0]), as8(q1[i]), t[i]); SBAR(); }
            k2[0][0] |= k2or;
            UNROLL for (int i = 0; i < 4; ++i) {
                float m = t[i][0];
                UNROLL for (int e = 1; e < 16; ++e) m = fmaxf(m, t[i][e]);
                auto mm = __builtin_amdgcn_permlane32_swap(__float_as_uint(m), __float_as_uint(m), false, false);
                m = fmaxf(__uint_as_float(mm[0]), __uint_as_float(mm[1]));
                q2[i][0] = hh ? pack2(-m, 0.f) & 0xFFFFu : q2[i][0];
                if (i == 0) {
                    s[0] = MFMA32K8(as4(k2[0]), as4(q2[0]), zero16);
                    s[0] = MFMA32(as8(k1[0]), as8(q1[0]), s[0]);
                }
            }
        }
        dma4(lane < 8 ? lane * 128 : OOB, dws, WS_O + cH * CH + (q0 + kh * 64) * 16, lds0 + LDS_SINK);
        st32(W, epoch, lane == 0 ? WS_ACK + (blockIdx.x * 8 + wid) * 4 : OOB, 0);
        __builtin_amdgcn_s_setprio(1);
        STAMP(4);
        NOUNROLL for (int tb = 0; tb < 32; tb += NB) {
            if (__builtin_expect(!__all(fnx == epoch), 0)) wait_flags1<SL1>(W, fkv + (tb < 28 ? tb + 4 : 0) * (FS / 2), epoch);
            if (tb == 16) lds_wait(L + FL_FREE + fl4, 1);
            const unsigned fso = (tb < 24 ? tb + 8 : 0) * (FS / 2);
            SBAR();
            UNROLL for (int b = 0; b < NB; ++b) {
                const int bn = (b + 1) % NB, bp = (b + NB - 1) % NB;
                const unsigned kso = tb + b + NB < 32 ? (t0 + tb + b + NB) * 512 : OOB / 2;
                const unsigned vso = tb + b + NB - 1 < 32 ? 0 : OOB / 2;
                const unsigned cso = tb >= 16 ? OOB / 2 : CPF == 1 ? cs0 + (nt4 * 32 + gwv * 8 + (b & 1) * 4) * 4096 + ((tb + b) >> 1) * 256 :
                                     CPF == 2 ? cs0 + (nt4 * 32 + gwv * 8 + ((tb + b) & 7)) * 4096 + (tb >> 3) * 1024 :
                                                cs0 + (nt4 * 32 + gwv * 8 + b * 2) * 4096 + (tb >> 2) * 512;
                UNROLL for (int qi = 0; qi < 4; ++qi) {
                    const int c = qi & 1, n = c ^ 1, qn = (qi + 1) & 3, qp = (qi + 3) & 3;
                    const int kb = qi == 3 ? bn : b, vb = qi == 0 ? bp : b;
                    f32x16& SC = s[c]; f32x16& SN = s[n];
                    SN = MFMA32K8(as4(k2[kb]), as4(q2[qn]), zero16); SBAR();
                    SC[0] = EX2(SC[0]); SC[1] = EX2(SC[1]); SC[2] = EX2(SC[2]); SC[3] = EX2(SC[3]); SBAR();
                    pd[0] = pack2(e8[0], e8[1]); pd[1] = pack2(e8[2], e8[3]); PIN(SC); PIN(pd); SBAR();
                    SN = MFMA32(as8(k1[kb]), as8(q1[qn]), SN); SBAR();
                    if (qi == 2) { k2[b] = ld64(W, k2off, kso); k1[b] = ld128(W, k1off, kso); SBAR(); }
                    if (qi == 3 && b == 0) { fnx = ld32(W, fkv, fso); SBAR(); }
                    if (CPF && qi == 1) { dma4(cpf, dc, cso, lds0 + LDS_SINK); SBAR(); }
                    SC[4] = EX2(SC[4]); SC[5] = EX2(SC[5]); SC[6] = EX2(SC[6]); SC[7] = EX2(SC[7]); SBAR();
                    pd[2] = pack2(e8[4], e8[5]); pd[3] = pack2(e8[6], e8[7]); PIN(SC); PIN(pd); SBAR();
                    o[qp] = MFMA32(as8(v0[vb]), as8(pa), o[qp]); SBAR();
                    e8[0] = EX2(SC[8]); e8[1] = EX2(SC[9]); e8[2] = EX2(SC[10]); e8[3] = EX2(SC[11]); SBAR();
                    pa[0] = pack2(SC[0], SC[1]); pa[1] = pack2(SC[2], SC[3]);
                    if (qi == 2) { k2[bn][0] |= k2or; PIN(k2[bn]); }
                    if (qi == 1) { va += vstep; vb2 += vstep; PIN(va); PIN(vb2); }
                    PIN(e8); PIN(pa); SBAR();
                    o[qp] = MFMA32(as8(v1[vb]), as8(pd), o[qp]); SBAR();
                    if (qi == 0) { v0[bp] = ld128(W, va, vso); v1[bp] = ld128(W, vb2, vso); SBAR(); }
                    e8[4] = EX2(SC[12]); e8[5] = EX2(SC[13]); e8[6] = EX2(SC[14]); e8[7] = EX2(SC[15]); SBAR();
                    pa[2] = pack2(SC[4], SC[5]); pa[3] = pack2(SC[6], SC[7]); PIN(e8); PIN(pa); SBAR();
                }
            }
        }
        __builtin_amdgcn_s_setprio(3);
        UNROLL for (int e = 0; e < 4; ++e) pd[e] = pack2(e8[2 * e], e8[2 * e + 1]);
        o[3] = MFMA32(as8(v0[NB - 1]), as8(pa), o[3]);
        o[3] = MFMA32(as8(v1[NB - 1]), as8(pd), o[3]);
        SBAR();
        STAMP(5);
        const int f0 = q0 + kh * 64;
        UNROLL for (int j = 0; j < 2; ++j) {
            const lds_p ex = LW + LDS_EX + (gwv * 2 + j) * EXB;
            *(__attribute__((address_space(3))) u32x4*)(ex + lane * 16) =
                u32x4{pack2(o[j][0], o[j][1]), pack2(o[j][2], o[j][3]), pack2(o[j][4], o[j][5]), pack2(o[j][6], o[j][7])};
            *(__attribute__((address_space(3))) u32x2*)(ex + 1024 + lane * 8) =
                hh ? u32x2{__float_as_uint(o[j][8]), q2[j][0] << 16} : u32x2{pack2(o[j][8], o[j][9]), pack2(o[j][10], o[j][11])};
        }
        lds_signal(LW + FL_EX + gwv * 4);
        float n1[2], l1[2];
        UNROLL for (int j = 0; j < 2; ++j) {
            auto nn = __builtin_amdgcn_permlane32_swap(q2[2 + j][0] << 16, q2[2 + j][0] << 16, false, false);
            auto ll = __builtin_amdgcn_permlane32_swap(__float_as_uint(o[2 + j][8]), __float_as_uint(o[2 + j][8]), false, false);
            n1[j] = __uint_as_float(nn[1]); l1[j] = __uint_as_float(ll[1]);
        }
        const unsigned oa = WS_O + (cA + hh) * CH + (f0 + r) * 16, ob = hh ? OOB : WS_O + cH * CH + (f0 + r) * 16 + odd * 8;
        lds_wait(L + FL_EX + (gwv ^ 2) * 4);
        STAMP2(22);
        bool ok = true;
        u32x4 ra[2]; u32x2 rb[2];
        UNROLL for (int j = 0; j < 2; ++j) {
            const lds_cp rx = L + LDS_EX + ((gwv ^ 2) * 2 + j) * EXB;
            const u32x4 a = *(const __attribute__((address_space(3))) u32x4*)(rx + lane * 16);
            const u32x2 c = *(const __attribute__((address_space(3))) u32x2*)(rx + 1024 + r * 8);
            const u32x2 d = *(const __attribute__((address_space(3))) u32x2*)(rx + 1280 + r * 8);
            const float n2 = __uint_as_float(d[1]), l2 = __uint_as_float(d[0]), nm = fminf(n1[j], n2);
            float w1 = EX2(nm - n1[j]), w2 = EX2(nm - n2);
            const float l = l1[j] * w1 + l2 * w2, rl = __builtin_amdgcn_rcpf(l);
            w1 *= rl; w2 *= rl;
            float f[12];
            UNROLL for (int e = 0; e < 4; ++e) { f[2 * e] = o[2 + j][2 * e] * w1 + bflo(a[e]) * w2; f[2 * e + 1] = o[2 + j][2 * e + 1] * w1 + bfhi(a[e]) * w2; }
            UNROLL for (int e = 0; e < 2; ++e) { f[8 + 2 * e] = o[2 + j][8 + 2 * e] * w1 + bflo(c[e]) * w2; f[9 + 2 * e] = o[2 + j][9 + 2 * e] * w1 + bfhi(c[e]) * w2; }
            float sa = 0.f;
            UNROLL for (int e = 0; e < 12; ++e) sa += fabsf(f[e]);
            ok = ok && l1[j] < 1e30f && l2 < 1e30f && l > 1e-18f && sa < 3e38f;
            auto s0 = __builtin_amdgcn_permlane32_swap(pack2(f[0], f[1]), pack2(f[4], f[5]), false, false);
            auto s1 = __builtin_amdgcn_permlane32_swap(pack2(f[2], f[3]), pack2(f[6], f[7]), false, false);
            ra[j] = u32x4{s0[0], s1[0], s0[1], s1[1]};
            rb[j] = u32x2{pack2(f[8], f[9]), pack2(f[10], f[11])};
        }
        if (__builtin_expect(__all(ok), 1)) {
            UNROLL for (int j = 0; j < 2; ++j) { st128(W, ra[j], oa + j * 512, 0); st64(W, rb[j], ob + j * 512, 0); }
        } else {
            wait_flags1<SL1>(W, WS_F1 + ((10 + hh * 10 + (col0 >> 6)) * 32 + r) * FS, epoch);
            wait_flags1<SL1>(W, WS_F1 + ((10 + hh * 10 + ((col0 + 19) >> 6)) * 32 + r) * FS, epoch);
            NOUNROLL for (int j = 0; j < 2; ++j) {
                const u32x2 z2 = ld64(W, q2off, (f0 + j * 32) * 16);
                const u32x4 z1 = ld128(W, k1off - 80 * CH, (f0 + j * 32) * 16);
                float m = -3e38f;
                f32x16 oo = f32x16{};
                NOUNROLL for (int t = 0; t < 64; ++t) {
                    const u32x4 a1 = ld128(W, k1off, t * 512); const u32x2 a2 = ld64(W, k2off, t * 512);
                    const u32x4 w0 = ld128(W, voff + t * vstep, 0), w1 = ld128(W, voff + t * vstep + (vstep >> 1), 0);
                    f32x16 z = MFMA32K8(as4(a2), as4(z2), zero16);
                    z = MFMA32(as8(a1), as8(z1), z);
                    float mx = z[0];
                    UNROLL for (int e = 1; e < 16; ++e) mx = fmaxf(mx, z[e]);
                    auto mm = __builtin_amdgcn_permlane32_swap(__float_as_uint(mx), __float_as_uint(mx), false, false);
                    const float mn = fmaxf(m, fmaxf(__uint_as_float(mm[0]), __uint_as_float(mm[1])));
                    const float al = EX2(m - mn);
                    m = mn;
                    UNROLL for (int e = 0; e < 16; ++e) oo[e] *= al;
                    u32x4 p0, p1;
                    UNROLL for (int e = 0; e < 4; ++e) { p0[e] = pack2(EX2(z[2 * e] - m), EX2(z[2 * e + 1] - m)); p1[e] = pack2(EX2(z[8 + 2 * e] - m), EX2(z[9 + 2 * e] - m)); }
                    oo = MFMA32(as8(w0), as8(p0), oo);
                    oo = MFMA32(as8(w1), as8(p1), oo);
                }
                auto l2 = __builtin_amdgcn_permlane32_swap(__float_as_uint(oo[8]), __float_as_uint(oo[8]), false, false);
                const float sc = 1.0f / __uint_as_float(l2[1]);
                auto s0 = __builtin_amdgcn_permlane32_swap(pack2(oo[0] * sc, oo[1] * sc), pack2(oo[4] * sc, oo[5] * sc), false, false);
                auto s1 = __builtin_amdgcn_permlane32_swap(pack2(oo[2] * sc, oo[3] * sc), pack2(oo[6] * sc, oo[7] * sc), false, false);
                st128(W, u32x4{s0[0], s1[0], s0[1], s1[1]}, oa + j * 512, 0);
                st64(W, u32x2{pack2(oo[8] * sc, oo[9] * sc), pack2(oo[10] * sc, oo[11] * sc)}, ob + j * 512, 0);
            }
        }
        STAMP(6);
        publish(W, WS_F3 + ((f0 >> 6) * 32 + head) * FS, epoch, lane);
        STAMP(7);
        if (blockIdx.x == 0 && wid == 4) {
            NOUNROLL for (unsigned spins = 0; spins < SPIN_CAP; ++spins) {
                asm volatile("" ::: "memory");
                bool ok = true;
                UNROLL for (int i = 0; i < 8; ++i) {
                    const u32x4 f = ld128(W, WS_ACK + i * 1024 + lane * 16, 0);
                    ok = ok && f[0] == epoch && f[1] == epoch && f[2] == epoch && f[3] == epoch;
                }
                if (__all(ok)) break;
                __builtin_amdgcn_s_sleep(32);
            }
            if (lane == 0) st32(W, epoch, WS_EP, 0);
        }
        return;
    }

    __builtin_amdgcn_s_setprio(2);
    u32x4 wf[5][5];
    {
        unsigned coff[2]; const unsigned lin = lane * 16;
        UNROLL for (int i = 0; i < 2; ++i) coff[i] = (lane >> 3) * 4096 + (((lane & 7) ^ ((4 * i + (lane >> 4)) & 7)) << 4);
#define SQ_DC(n, slot, i) dma16(coff[(i) & 1], dc, cs0 + gwv * 131072 + (i) * 32768 + (n) * 128, lds0 + (slot) * SQ_STAGE + gwv * 4096 + (i) * 1024)
#define SQ_DA(n, slot, i) dma16s(lin, dws, as0 + (n) * 20480 + gwv * 5120 + (i) * 1024, lds0 + (slot) * SQ_STAGE + 16384 + gwv * 5120 + (i) * 1024)
        f32x16 acc[5];
        UNROLL for (int u = 0; u < 5; ++u) { acc[u] = f32x16{}; PIN(acc[u]); }
        unsigned rowa[4];
        UNROLL for (int ks = 0; ks < 4; ++ks) rowa[ks] = gwv * 4096 + r * 128 + (((2 * ks + hh) ^ ((r >> 1) & 7)) << 4);
        const unsigned ard = 16384 + hh * 2560 + r * 16;
        s16x8 fc[2], fa[2][5];
        fc[1] = s16x8{};
        UNROLL for (int u = 0; u < 5; ++u) fa[1][u] = s16x8{};
        if (wid == 0) {
            const unsigned f2 = WS_F2 + (((ksp >> 1) * 8 + nt4 * 2 + hh) * 64 + (ksp & 1) * 32 + r) * FS;
            __builtin_amdgcn_s_sleep(16);
            wait_flags1<12>(W, f2, epoch);
            lds_wait(L + FL_FREE + fl4, 1);
            lds_post(LW + FL_FIN, 1);
        } else {
            lds_wait(L + FL_FIN, 1);
        }
        STAMP(3);
        UNROLL for (int n = 0; n < 2; ++n) { UNROLL for (int i = 0; i < 4; ++i) SQ_DC(n, n, i); UNROLL for (int i = 0; i < 5; ++i) SQ_DA(n, n, i); }
        const lds_p pw = LW + (lane == 0 ? FL_SFULL + gwv * 4 : LDS_SINK + lane * 4);
        const lds_cp pr = L + FL_SFULL + fl4;
        const unsigned wfo = WS_WOT + (ct * 4 + gwv) * 25600 + lane * 16;
        int slot = 0;
#define PACE 0
#define SQ_MF(c, u) do { acc[u] = MFMA32(fa[c][u], fc[c], acc[u]); if (PACE && ((u) & PMASK) == 0) { SBAR(); __builtin_amdgcn_s_sleep(PACE); } SBAR(); } while (0)
#define PMASK 0
#define SQ_RA(n, u, ks) fa[n][u] = lds128(sp + (u) * 512 + (ks) * 5120)
#define SQ_STG(DMA) do { \
            __builtin_amdgcn_s_setprio(3); \
            lds_signal(pw, j + 1); lds_wait(pr, j + 1); \
            lds_cp sp = L + slot * SQ_STAGE + ard; lds_cp sr[4]; \
            UNROLL for (int ks = 0; ks < 4; ++ks) { sr[ks] = L + slot * SQ_STAGE + rowa[ks]; PIN(sr[ks]); } \
            PIN(sp); SBAR(); \
            __builtin_amdgcn_s_setprio(GPRIO); \
            const int sf = slot == 0 ? 2 : slot - 1; \
            UNROLL for (int ks = 0; ks < 4; ++ks) { \
                const int n = ks & 1, c = n ^ 1; \
                fc[n] = lds128(sr[ks]); SQ_RA(n, 0, ks); SBAR(); \
                SQ_MF(c, 0); \
                SQ_RA(n, 1, ks); SQ_RA(n, 2, ks); SBAR(); \
                SQ_MF(c, 1); \
                SQ_RA(n, 3, ks); SQ_RA(n, 4, ks); SBAR(); \
                SQ_MF(c, 2); \
                if (DMA) { SQ_DC(j + 2, sf, ks); SBAR(); } \
                SQ_MF(c, 3); \
                if (DMA) { SQ_DA(j + 2, sf, ks); if (ks == 3) SQ_DA(j + 2, sf, 4); SBAR(); } \
                SQ_MF(c, 4); \
            } \
            slot = slot == 2 ? 0 : slot + 1; } while (0)
        int j = 0;
        NOUNROLL for (; j < 14; ++j) { WAITV(9); SQ_STG(true); }
        __builtin_amdgcn_s_setprio(3);
        if (wid == 0) {
            const int px = r >> 2;
            const unsigned fw = WS_F2 + (((px >> 1) * 4 + hh) * 64 + ((px & 1) * 8 + ct) * 4 + (r & 3)) * FS;
            const unsigned fg = WS_F2 + (15 * 64 + (tt >> 1) * 4 + (r & 3)) * FS;
            wait_flags4<12>(W, fw, fw + 128 * FS, fg, fg, epoch);
            lds_post(LW + FL_FIN, 2);
        } else {
            lds_wait(L + FL_FIN, 2);
        }
        UNROLL for (int st = 0; st < 5; ++st) UNROLL for (int u = 0; u < 5; ++u) wf[st][u] = ld128(W, wfo, (st * 5 + u) * 1024);
        SBAR();
        NOUNROLL for (; j < 16; ++j) { if (j == 14) { WAITV(34); } else { WAITV(25); } SQ_STG(false); }
        UNROLL for (int u = 0; u < 5; ++u) acc[u] = MFMA32(fa[1][u], fc[1], acc[u]);
        __builtin_amdgcn_s_setprio(3);
        lds_signal(pw, 17);
        STAMP(4);
        const unsigned yb = WS_Y + (ksp * 80 + nt4 * 20 + hh) * CH + (mt * 128 + gwv * 32 + r) * 16;
        UNROLL for (int u = 0; u < 5; ++u)
            UNROLL for (int g = 0; g < 4; g += 2) {
                unsigned a0 = pack2(acc[u][4 * g], acc[u][4 * g + 1]), a1 = pack2(acc[u][4 * g + 2], acc[u][4 * g + 3]);
                unsigned b0 = pack2(acc[u][4 * g + 4], acc[u][4 * g + 5]), b1 = pack2(acc[u][4 * g + 6], acc[u][4 * g + 7]);
                auto s0 = __builtin_amdgcn_permlane32_swap(a0, b0, false, false);
                auto s1 = __builtin_amdgcn_permlane32_swap(a1, b1, false, false);
                st128(W, u32x4{s0[0], s1[0], s0[1], s1[1]}, yb, (u * 4 + g) * CH);
            }
        publish(W, WS_F4 + ((((mt * 2 + (gwv >> 1)) * 4 + nt4) * 4 + ksp) * 2 + (gwv & 1)) * FS, epoch, lane);
        lds_wait(pr, 17);
        STAMP(5);
    }
    const int t0 = tt * 64;
    f32x4 ys[5];
    float gt;
    {
        if (wid == 0) { wait_flags1<16>(W, WS_F4 + ((tt * 4 + (ct >> 1)) * 8 + (lane & 7)) * FS, epoch); lds_post(LW + FL_FIN, 3); }
        else lds_wait(L + FL_FIN, 3);
        const unsigned yo = WS_Y + (ct * 10 + (kg >> 1)) * CH + (t0 + gwv * 16 + l15) * 16 + (kg & 1) * 8;
        u32x2 y[5][4];
        UNROLL for (int u = 0; u < 5; ++u) UNROLL for (int p = 0; p < 4; ++p) y[u][p] = ld64(W, yo, (p * 80 + u * 2) * CH);
        gt = __uint_as_float(ld32(W, WS_G + (t0 + gwv * 16 + l15) * 4, 0));
        const float h = 1.0f - gt;
        UNROLL for (int u = 0; u < 5; ++u) {
            ys[u][0] = h * ((bflo(y[u][0][0]) + bflo(y[u][1][0])) + (bflo(y[u][2][0]) + bflo(y[u][3][0])));
            ys[u][1] = h * ((bfhi(y[u][0][0]) + bfhi(y[u][1][0])) + (bfhi(y[u][2][0]) + bfhi(y[u][3][0])));
            ys[u][2] = h * ((bflo(y[u][0][1]) + bflo(y[u][1][1])) + (bflo(y[u][2][1]) + bflo(y[u][3][1])));
            ys[u][3] = h * ((bfhi(y[u][0][1]) + bfhi(y[u][1][1])) + (bfhi(y[u][2][1]) + bfhi(y[u][3][1])));
            PIN(ys[u]);
        }
    }
    STAMP(6);
    lds_wait(L + FL_EX + fl4, 1);
    if (wid == 0) { wait_flags1<SL3>(W, WS_F3 + (tt * 32 + r) * FS, epoch); lds_post(LW + FL_FIN, 4); }
    else lds_wait(L + FL_FIN, 4);
    STAMP(8);
    const unsigned boff = WS_O + (gwv * 20 + kg) * CH + (t0 + l15) * 16;
    u32x4 bf[4][5];
    UNROLL for (int d = 0; d < 4; ++d)
        UNROLL for (int st = 0; st < 5; ++st) bf[d][st] = ld128(W, boff + ((gwv + d + 1) & 3) * 256, st * 4 * CH);
    SBAR();
    f32x4 res[5];
    UNROLL for (int d = 0; d < 4; ++d) {
        UNROLL for (int u = 0; u < 5; ++u) res[u] = f32x4{};
        UNROLL for (int st = 0; st < 5; ++st) UNROLL for (int u = 0; u < 5; ++u) res[u] = MFMA16(as8(wf[st][u]), as8(bf[d][st]), res[u]);
        if (d < 3) { UNROLL for (int u = 0; u < 5; ++u) stsf4(LW + ((gwv + d + 1) & 3) * GW_LDS + (d * 5 + u) * 1024 + lane * 16, res[u]); }
        if (d == 2) lds_signal(LW + FL_RED + gwv * 4);
        SBAR();
    }
    STAMP(9);
    lds_wait(L + FL_RED + fl4);
    UNROLL for (int d = 0; d < 3; ++d) UNROLL for (int u = 0; u < 5; ++u) res[u] += ldsf4(L + gwv * GW_LDS + (d * 5 + u) * 1024 + lane * 16);
    UNROLL for (int u = 0; u < 5; ++u) res[u] = res[u] * gt + ys[u];
    char* dst = out + (size_t)(t0 + gwv * 16 + l15) * 1280 + ct * 160 + kg * 8;
    UNROLL for (int u = 0; u < 5; ++u) *(u32x2*)(dst + u * 32) = u32x2{pack2(res[u][0], res[u][1]), pack2(res[u][2], res[u][3])};
    STAMP(10);
}

extern "C" void kernel_launch(void* const* d_in, const int* in_sizes, int n_in, void* d_out, int out_size,
                              void* d_ws, size_t ws_size, hipStream_t stream) {
    (void)hipFuncSetAttribute((const void*)fused, hipFuncAttributeMaxDynamicSharedMemorySize, LDS_BYTES);
    fused<<<256, 512, LDS_BYTES, stream>>>((const char*)d_in[0], (const char*)d_in[1], (const char*)d_in[2], (const char*)d_in[4],
                                           (const char*)d_in[5], (const char*)d_in[6], (const char*)d_in[7], (const char*)d_in[8],
                                           (char*)d_ws + WS_BASE(ws_size), (char*)d_out);
}
```

```cpp
#include <hip/hip_runtime.h>
#include <hip/hip_bf16.h>
#include <stdint.h>

typedef short s16x8 __attribute__((ext_vector_type(8)));
typedef short s16x4 __attribute__((ext_vector_type(4)));
typedef float f32x16 __attribute__((ext_vector_type(16)));
typedef float f32x8 __attribute__((ext_vector_type(8)));
typedef float f32x4 __attribute__((ext_vector_type(4)));
typedef unsigned u32x4 __attribute__((ext_vector_type(4)));
typedef unsigned u32x2 __attribute__((ext_vector_type(2)));
typedef __bf16 bf16x2_t __attribute__((ext_vector_type(2)));
typedef __attribute__((address_space(3))) const char* lds_cp;
typedef __attribute__((address_space(3))) char* lds_p;
typedef __amdgpu_buffer_rsrc_t rsrc_t;

#define DI __device__ __forceinline__
#define UNROLL _Pragma("unroll")
#define NOUNROLL _Pragma("unroll 1")
#define MFMA32(a, b, c) __builtin_amdgcn_mfma_f32_32x32x16_bf16((a), (b), (c), 0, 0, 0)
#define MFMA32K8(a, b, c) __builtin_amdgcn_mfma_f32_32x32x8bf16_1k((a), (b), (c), 0, 0, 0)
#define MFMA16(a, b, c) __builtin_amdgcn_mfma_f32_16x16x32_bf16((a), (b), (c), 0, 0, 0)
#define SBAR() __builtin_amdgcn_sched_barrier(0)
#define PIN(x) asm volatile("" : "+v"(x))
#define EX2(x) __builtin_amdgcn_exp2f(x)
#define SC1 16

constexpr unsigned CH = 2048 * 16;
constexpr unsigned WS_QK = 0;
constexpr unsigned WS_O = 160 * CH;
constexpr unsigned WS_Y = WS_O + 80 * CH;
constexpr unsigned ABROW = 640 * 16;
constexpr unsigned WS_AB = WS_Y + 320 * CH;
constexpr unsigned VROW = 10240;
constexpr unsigned WS_VT = WS_AB + 512 * ABROW;
constexpr unsigned WS_WOT = WS_VT + 256 * VROW;
constexpr unsigned WS_G = WS_WOT + 80 * ABROW;
constexpr unsigned FS = 128;
constexpr unsigned WS_ACK = WS_G + 8192;
constexpr unsigned WS_EP = WS_ACK + 8192;
constexpr unsigned WS_F1 = WS_EP + 4096;
constexpr unsigned WS_F2 = WS_F1 + 1024 * FS;
constexpr unsigned WS_F3 = WS_F2 + 1024 * FS;
constexpr unsigned WS_F4 = WS_F3 + 1024 * FS;
constexpr unsigned WS_ONE = WS_F4 + 1024 * FS;
constexpr unsigned WS_END = WS_ONE + 2048 * 128;
constexpr unsigned OOB = 0x80000000u;
#define WSTAIL 1
#define WS_BASE(n) (WSTAIL ? (((n) - WS_END) & ~(size_t)0x1FFFFF) : (size_t)0)

constexpr int P1_STAGE = 32768;
constexpr int SQ_STAGE = 36864;
constexpr int GW_LDS = 15360;
constexpr int EXB = 1536;
constexpr int LDS_EX = 4 * SQ_STAGE;
constexpr int LDS_X9 = 4 * P1_STAGE;
constexpr int LDS_GW = LDS_X9 + 20480;
constexpr int LDS_WT = LDS_GW + 2048;
constexpr int LDS_FL = LDS_EX + 8 * EXB;
constexpr int LDS_SINK = LDS_FL + 256;
constexpr int LDS_BYTES = LDS_SINK + 256;
static_assert(LDS_BYTES <= 163840 && LDS_FL >= LDS_WT + 4096, "lds");
constexpr int FL_FULL = LDS_FL, FL_GO = LDS_FL + 32, FL_EX = LDS_FL + 48, FL_SFULL = LDS_FL + 80,
              FL_SFREE = LDS_FL + 96, FL_FIN = LDS_FL + 112, FL_RED = LDS_FL + 128, FL_FREE = LDS_FL + 144;
#define GPRIO 0
#define FWARM 1
#define HI 0
#define OUTM 3
#define LSL 8
#define LS2 3
#define SL1 4
#define SL3 4
#define SJ 4
#define PUBZ 0
#define AABL 0
#define SABL 0
#define PFX 1
#define CPF 0
#define CODE_LINES 300

#define STAMP(i) do { } while (0)
#define TACC(a) do { } while (0)
#define TDECL() do { } while (0)
#define TOUT(i) do { } while (0)
#define STAMP2(i) do { } while (0)

DI unsigned pack2(float a, float b) {
    bf16x2_t v = {(__bf16)a, (__bf16)b};
    return __builtin_bit_cast(unsigned, v);
}
DI float bflo(unsigned u) { return __uint_as_float(u << 16); }
DI float bfhi(unsigned u) { return __uint_as_float(u & 0xFFFF0000u); }
DI s16x8 as8(u32x4 v) { return __builtin_bit_cast(s16x8, v); }
DI s16x4 as4(u32x2 v) { return __builtin_bit_cast(s16x4, v); }

DI u32x4 mkdesc(const void* p, unsigned bytes) {
    const unsigned long long a = (unsigned long long)p;
    return u32x4{(unsigned)a, (unsigned)(a >> 32) & 0xFFFFu, bytes, 0x00020000u};
}
DI void dma16(unsigned voff, u32x4 desc, unsigned soff, unsigned ldsaddr) {
    unsigned keep;
    asm volatile("s_mov_b32 %0, m0\n\ts_mov_b32 m0, %4\n\ts_nop 0\n\tbuffer_load_dwordx4 %1, %2, %3 offen lds\n\ts_mov_b32 m0, %0"
                 : "=&s"(keep) : "v"(voff), "s"(desc), "s"(soff), "s"(ldsaddr) : "memory");
}
DI void dma16s(unsigned voff, u32x4 desc, unsigned soff, unsigned ldsaddr) {
    unsigned keep;
    asm volatile("s_mov_b32 %0, m0\n\ts_mov_b32 m0, %4\n\ts_nop 0\n\tbuffer_load_dwordx4 %1, %2, %3 offen sc1 lds\n\ts_mov_b32 m0, %0"
                 : "=&s"(keep) : "v"(voff), "s"(desc), "s"(soff), "s"(ldsaddr) : "memory");
}
DI void dma16x4(unsigned v0, unsigned v1, u32x4 desc, unsigned s0, unsigned s1, unsigned s2, unsigned s3, unsigned ldsaddr) {
    unsigned keep;
    asm volatile("s_mov_b32 %0, m0\n\ts_mov_b32 m0, %8\n\ts_nop 0\n\t"
                 "buffer_load_dwordx4 %1, %3, %4 offen lds\n\tbuffer_load_dwordx4 %2, %3, %5 offen offset:1024 lds\n\t"
                 "buffer_load_dwordx4 %1, %3, %6 offen offset:2048 lds\n\tbuffer_load_dwordx4 %2, %3, %7 offen offset:3072 lds\n\ts_mov_b32 m0, %0"
                 : "=&s"(keep) : "v"(v0), "v"(v1), "s"(desc), "s"(s0), "s"(s1), "s"(s2), "s"(s3), "s"(ldsaddr) : "memory");
}
DI void dma16sx4(unsigned voff, u32x4 desc, unsigned soff, unsigned ldsaddr) {
    unsigned keep;
    asm volatile("s_mov_b32 %0, m0\n\ts_mov_b32 m0, %4\n\ts_nop 0\n\t"
                 "buffer_load_dwordx4 %1, %2, %3 offen sc1 lds\n\tbuffer_load_dwordx4 %1, %2, %3 offen offset:1024 sc1 lds\n\t"
                 "buffer_load_dwordx4 %1, %2, %3 offen offset:2048 sc1 lds\n\tbuffer_load_dwordx4 %1, %2, %3 offen offset:3072 sc1 lds\n\ts_mov_b32 m0, %0"
                 : "=&s"(keep) : "v"(voff), "s"(desc), "s"(soff), "s"(ldsaddr) : "memory");
}
DI void dma4(unsigned voff, u32x4 desc, unsigned soff, unsigned ldsaddr) {
    unsigned keep;
    asm volatile("s_mov_b32 %0, m0\n\ts_mov_b32 m0, %4\n\ts_nop 0\n\tbuffer_load_dword %1, %2, %3 offen sc1 lds\n\ts_mov_b32 m0, %0"
                 : "=&s"(keep) : "v"(voff), "s"(desc), "s"(soff), "s"(ldsaddr) : "memory");
}
#define WAITV(n) asm volatile("s_waitcnt vmcnt(" #n ")" ::: "memory")
#define WAITL() asm volatile("s_waitcnt lgkmcnt(0)" ::: "memory")
#define BARRIER() asm volatile("s_waitcnt lgkmcnt(0)\n\ts_barrier" ::: "memory")
#define BARRIER_NOWAIT() asm volatile("s_barrier" ::: "memory")

DI s16x8 lds128(lds_cp p) { return *(const __attribute__((address_space(3))) s16x8*)p; }
DI f32x4 ldsf4(lds_cp p) { return *(const __attribute__((address_space(3))) f32x4*)p; }
DI void stsf4(lds_p p, f32x4 v) { *(__attribute__((address_space(3))) f32x4*)p = v; }
DI s16x4 ldstr(lds_cp p) {
    return __builtin_bit_cast(s16x4, __builtin_amdgcn_ds_read_tr16_b64_v4i16((__attribute__((address_space(3))) s16x4*)p));
}
DI s16x8 cat8(s16x4 a, s16x4 b) { return __builtin_shufflevector(a, b, 0, 1, 2, 3, 4, 5, 6, 7); }
DI u32x4 ld128(rsrc_t r, unsigned voff, unsigned soff) { return __builtin_amdgcn_raw_buffer_load_b128(r, voff, soff, SC1); }
DI u32x2 ld64(rsrc_t r, unsigned voff, unsigned soff) { return __builtin_amdgcn_raw_buffer_load_b64(r, voff, soff, SC1); }
DI unsigned ld32(rsrc_t r, unsigned voff, unsigned soff) { return __builtin_amdgcn_raw_buffer_load_b32(r, voff, soff, SC1); }
DI void st128(rsrc_t r, u32x4 v, unsigned voff, unsigned soff) { __builtin_amdgcn_raw_buffer_store_b128(v, r, voff + soff, 0, SC1); }
DI void st64(rsrc_t r, u32x2 v, unsigned voff, unsigned soff) { __builtin_amdgcn_raw_buffer_store_b64(v, r, voff, soff, SC1); }
DI void st32(rsrc_t r, unsigned v, unsigned voff, unsigned soff) { __builtin_amdgcn_raw_buffer_store_b32(v, r, voff, soff, SC1); }

constexpr unsigned SPIN_CAP = 200000;
DI void publish(rsrc_t w, unsigned flag_off, unsigned epoch, int lane) {
    WAITV(0);
    if (lane == 0) st32(w, epoch, flag_off, 0);
}
template <int SL>
DI void wait_flags4(rsrc_t w, unsigned o0, unsigned o1, unsigned o2, unsigned o3, unsigned epoch) {
    NOUNROLL for (unsigned spins = 0; spins < SPIN_CAP; ++spins) {
        asm volatile("" ::: "memory");
        const unsigned a = ld32(w, o0, 0), b = ld32(w, o1, 0), c = ld32(w, o2, 0), d = ld32(w, o3, 0);
        if (__all(a == epoch && b == epoch && c == epoch && d == epoch)) break;
        __builtin_amdgcn_s_sleep(SL);
    }
    asm volatile("" ::: "memory");
}
template <int SL>
DI void wait_flags1(rsrc_t w, unsigned o0, unsigned epoch) {
    NOUNROLL for (unsigned spins = 0; spins < SPIN_CAP; ++spins) {
        asm volatile("" ::: "memory");
        const unsigned a = ld32(w, o0, 0);
        if (__all(a == epoch)) break;
        __builtin_amdgcn_s_sleep(SL);
    }
    asm volatile("" ::: "memory");
}
DI void lds_signal(lds_p p, unsigned val = 1u) {
    WAITL();
    *(volatile __attribute__((address_space(3))) unsigned*)p = val;
}
DI void lds_post(lds_p p, unsigned val) { *(volatile __attribute__((address_space(3))) unsigned*)p = val; }
DI unsigned lds_peek(lds_cp p) { return *(const volatile __attribute__((address_space(3))) unsigned*)p; }
template <int SL = 1>
DI void lds_wait(lds_cp p, unsigned val = 1u) {
    NOUNROLL for (unsigned spins = 0; spins < 4 * SPIN_CAP; ++spins) {
        const unsigned v = *(const volatile __attribute__((address_space(3))) unsigned*)p;
        if (__all(v >= val)) break;
        __builtin_amdgcn_s_sleep(SL);
    }
    asm volatile("" ::: "memory");
}

__global__ __launch_bounds__(512, 2) void fused(const char* __restrict__ x, const char* __restrict__ wqkv,
                                                 const char* __restrict__ wo, const char* __restrict__ cre,
                                                 const char* __restrict__ cim, const char* __restrict__ wr,
                                                 const char* __restrict__ wi, const char* __restrict__ gw,
                                                 char* ws, char* __restrict__ out) {
    extern __shared__ __attribute__((aligned(16))) char smem[];
    unsigned long long pc; asm volatile("s_getpc_b64 %0" : "=s"(pc));
    const int tid = threadIdx.x;
    const int wid = __builtin_amdgcn_readfirstlane(tid >> 6);
    const int gwv = wid & 3;
    const int xcd = blockIdx.x & 7, idx = blockIdx.x >> 3;
    const unsigned lds0 = (unsigned)(uintptr_t)smem;
    const lds_cp L = (lds_cp)smem;
    const lds_p LW = (lds_p)smem;
    const u32x4 dx = mkdesc(x, 2048 * 1280);
    const int mt = (xcd & 1) * 8 + (idx & 7), nt = (xcd >> 1) * 4 + (idx >> 3);
    const u32x4 dw = mkdesc(wqkv, 640 * 3840);
    const rsrc_t W = __builtin_amdgcn_make_buffer_rsrc(ws, 0, WS_END, 0x00020000);
    const unsigned oneoff = WS_ONE + (blockIdx.x * 8 + wid) * 128;
    unsigned epoch;
    {
    const int lane = tid & 63;
    unsigned roff[2];
    UNROLL for (int i = 0; i < 2; ++i) roff[i] = (lane >> 3) * 1280 + (((lane & 7) ^ ((4 * i + (lane >> 4)) & 7)) << 4);
    unsigned woff[2];
    UNROLL for (int i = 0; i < 2; ++i) woff[i] = nt == 15 ? OOB : (lane >> 3) * 3840 + (((lane & 7) ^ (((lane >> 4) & 1) << 2) ^ (i << 1)) << 4);
#define P1_DX(st, p, par) dma16(roff[par], dx, (mt * 128 + (p) * 8) * 1280 + (st) * 128, lds0 + ((st) & 3) * P1_STAGE + (p) * 1024)
#define P1_DW(st, p, par) dma16(woff[par], dw, ((st) * 64 + ((p) & 7) * 8) * 3840 + nt * 256 + ((p) >> 3) * 128, lds0 + ((st) & 3) * P1_STAGE + 16384 + (p) * 1024)
    P1_DX(0, 2 * wid, 0); P1_DW(0, 2 * wid, 0); P1_DX(0, 2 * wid + 1, 1); P1_DW(0, 2 * wid + 1, 1);
    STAMP(0);
    const int hh = lane >> 5;
    const int l15 = lane & 15, kg = lane >> 4;
    const unsigned fl4 = (lane & 3) * 4;
    const u32x4 dh = mkdesc(nt < 8 ? wr : wi, 640 * 1280);
#define GET_EPOCH() do { epoch = ld32(W, WS_EP, 0) + 1u; if (lane < 4) st32(W, 0x3F803F80u, oneoff + lane * 4, 0); } while (0)
#define WH_ADDR(q) ((q) < 32 ? 2 * P1_STAGE + (q) * 1024 : (q) < 48 ? 3 * P1_STAGE + 16384 + ((q) - 32) * 1024 : \
                    (q) < 68 ? LDS_X9 + ((q) - 48) * 1024 : (q) < 84 ? 16384 + ((q) - 68) * 1024 : P1_STAGE + 16384 + ((q) - 84) * 1024)

    if (wid >= 4) {
#define P1_ISSUE(st) do { UNROLL for (int i = 0; i < 4; ++i) { P1_DX(st, 4 * gwv + i, i & 1); P1_DW(st, 4 * gwv + i, i & 1); } } while (0)
#define P1_DH(q, par) do { const int q_ = (q), c_ = q_ / 10, p_ = q_ - c_ * 10; \
            dma16(roff[par], dh, ((nt & 7) * 80 + p_ * 8) * 1280 + c_ * 128, lds0 + WH_ADDR(q_)); } while (0)
        P1_ISSUE(1); P1_ISSUE(2); P1_ISSUE(3);
        WAITV(24); BARRIER_NOWAIT();
        NOUNROLL for (int k = 1; k < 7; ++k) { WAITV(16); BARRIER_NOWAIT(); P1_ISSUE(k + 3); }
        WAITV(16); BARRIER_NOWAIT();
        UNROLL for (int i = 0; i < 8; ++i) P1_DH(gwv * 8 + i, i & 1);
        WAITV(16); BARRIER_NOWAIT();
        UNROLL for (int i = 0; i < 8; ++i) P1_DH(32 + gwv * 8 + i, i & 1);
        WAITV(16); BARRIER_NOWAIT();
        UNROLL for (int i = 0; i < 6; ++i) { const int q = 64 + gwv * 6 + i; P1_DH(q < 84 ? q : 82 + (i & 1), i & 1); }
        GET_EPOCH();
        const int wm = gwv & 1, wn = gwv >> 1;
        unsigned sb[2], ss0, ss1;
        if (nt < 10) {
            UNROLL for (int i = 0; i < 2; ++i) sb[i] = WS_QK + (nt * 16 + wn * 8 + (kg >> 1)) * CH + (mt * 128 + wm * 64 + (i ^ wn) * 32 + (kg & 1) * 16 + l15) * 16;
            ss0 = 2 * CH; ss1 = 4 * CH;
        } else {
            UNROLL for (int i = 0; i < 2; ++i) sb[i] = nt == 15 ? OOB : WS_VT + ((mt * 8 + wm * 4 + (i ^ wn) * 2) * 2 + (kg & 1)) * VROW + ((nt - 10) * 128 + wn * 64 + hh * 16 + l15) * 16;
            ss0 = 512; ss1 = 2 * VROW;
        }
        __builtin_amdgcn_s_setprio(3);
        WAITV(0); BARRIER_NOWAIT();
        u32x4 tv[8];
        UNROLL for (int i = 0; i < 8; ++i)
            tv[i] = __builtin_bit_cast(u32x4, lds128(L + (i < 4 ? 3 * P1_STAGE : 0) + wm * 8192 + wn * 4096 + (i & 3) * 1024 + lane * 16));
        UNROLL for (int i = 0; i < 8; ++i) PIN(tv[i]);
        UNROLL for (int i = 0; i < 4; ++i) P1_DH(84 + gwv * 4 + i, i & 1);
        UNROLL for (int i = 0; i < 8; ++i) st128(W, tv[i], sb[i >> 2], (i & 1) * ss0 + ((i >> 1) & 1) * ss1);
        STAMP2(16);
        STAMP(1);
    } else {
        const unsigned sink = lds0 + LDS_SINK;
        const int pi = xcd * 4 + wid, pst = pi / 5, pu = pi - pst * 5;
        if (PFX) {
            const unsigned b = blockIdx.x;
            if (wid == 0) { dma4(lane * 128, dx, b * 10240, sink); dma4(lane < 16 ? lane * 128 : OOB, dx, b * 10240 + 8192, sink); }
            if (wid == 1) { dma4(lane * 128, dw, b * 9600, sink); dma4(lane < 11 ? lane * 128 : OOB, dw, b * 9600 + 8192, sink); }
            if (wid == 2) { dma4(FWARM && lane < 16 ? (b * 16 + lane) * FS : OOB, mkdesc(ws, WS_END), WS_F1, sink); dma4(OOB, dx, 0, sink); }
        } else if (wid < 3) { dma4(OOB, dx, 0, sink); dma4(OOB, dx, 0, sink); }
        if (wid == 3) {
            dma4(lane < 12 ? lane * 128 : OOB, mkdesc((const void*)(pc & ~127ull), CODE_LINES * 128), idx * 1536, sink);
            dma16(nt == 15 ? lane * 16 : OOB, mkdesc(gw, 1280), 0, lds0 + LDS_GW);
        }
        if (tid < 64) *(__attribute__((address_space(3))) unsigned*)(LW + LDS_FL + tid * 4) = 0u;
        const int wm = wid & 1, wn = wid >> 1;
        unsigned ra[2];
        UNROLL for (int ks = 0; ks < 2; ++ks) ra[ks] = l15 * 128 + (((4 * ks + kg) ^ ((l15 >> 1) & 7)) << 4);
        const unsigned xk = wm * 8192 + wn * 4096, xo = wm * 8192 + (wn ^ 1) * 4096;
        s16x8 xs[10][2][2];
#define WT_LOAD() dma16((lane >> 1) * 1280 + (lane & 1) * 16, mkdesc(wo, 640 * 1280), \
                  pi < 25 ? ((idx >> 3) * 160 + pst * 32) * 1280 + (idx & 7) * 160 + pu * 32 : OOB, lds0 + LDS_WT + wid * 1024)
#define WT_STORE() do { const lds_cp tp_ = L + LDS_WT + wid * 1024 + (8 * kg + (l15 >> 2)) * 32 + (lane & 3) * 8; \
            st128(W, __builtin_bit_cast(u32x4, cat8(ldstr(tp_), ldstr(tp_ + 128))), \
                  pi < 25 ? WS_WOT + (((idx & 7) * 4 + (idx >> 3)) * 25 + pi) * 1024 + lane * 16 : OOB, 0); } while (0)
        if (nt == 15) {
            const int m0 = mt * 128 + wm * 64 + wn * 32;
            if (wid == 3) dma16(lane * 16, mkdesc(gw, 1280), 1024, lds0 + LDS_GW + 1024);
            WT_LOAD();
            GET_EPOCH();
            {
                const unsigned o0 = (mt * 4 + wid) * 12800, v1 = lane < 36 ? lane * 128 : OOB;
                dma4(lane * 128, mkdesc(wr, 640 * 1280), o0, sink); dma4(v1, mkdesc(wr, 640 * 1280), o0 + 8192, sink);
                dma4(lane * 128, mkdesc(wi, 640 * 1280), o0, sink); dma4(v1, mkdesc(wi, 640 * 1280), o0 + 8192, sink);
            }
            WAITV(4);
            float sum[2] = {0.f, 0.f};
            UNROLL for (int k = 0; k < 10; ++k) {
                BARRIER();
                UNROLL for (int ks = 0; ks < 2; ++ks) {
                    const u32x4 gv = __builtin_bit_cast(u32x4, lds128(L + LDS_GW + k * 128 + (4 * ks + kg) * 16));
                    UNROLL for (int h = 0; h < 2; ++h) {
                        xs[k][ks][h] = lds128(L + (k & 3) * P1_STAGE + xk + h * 2048 + ra[ks]);
                        const u32x4 xv = __builtin_bit_cast(u32x4, xs[k][ks][h]);
                        UNROLL for (int e = 0; e < 4; ++e)
                            { const unsigned xe = xv[e], ge = gv[e]; sum[h] = __builtin_amdgcn_fdot2_f32_bf16(__builtin_bit_cast(bf16x2_t, xe), __builtin_bit_cast(bf16x2_t, ge), sum[h], false); }
                    }
                }
                SBAR();
            }
            UNROLL for (int h = 0; h < 2; ++h) { sum[h] += __shfl_xor(sum[h], 16); sum[h] += __shfl_xor(sum[h], 32); }
            const float sm = kg & 1 ? sum[1] : sum[0];
            if (lane < 32) st32(W, __float_as_uint(1.0f / (1.0f + __expf(-sm))), WS_G + (m0 + lane) * 4, 0);
        } else {
            const int q = l15 >> 2;
            unsigned trb[4];
            UNROLL for (int j = 0; j < 4; ++j)
                trb[j] = 16384 + wn * 8192 + (8 * kg + q) * 128 + ((j ^ (((q >> 1) & 1) << 1) ^ (kg & 1)) << 5) + 8 * (lane & 3);
            f32x4 acc[4][4];
            UNROLL for (int i = 0; i < 4; ++i) UNROLL for (int j = 0; j < 4; ++j) acc[i][j] = f32x4{};
            s16x8 xt[2][2]; s16x4 wv[4][2];
            WT_LOAD();
            GET_EPOCH();
            WAITV(3);
#define P1_RW(z, j) do { const lds_cp p_ = L + (((z) >> 1) & 3) * P1_STAGE + trb[j] + ((z) & 1) * 4096; wv[j][0] = ldstr(p_); wv[j][1] = ldstr(p_ + 512); } while (0)
#define P1_RK(z, h) xs[(z) >> 1][(z) & 1][h] = lds128(L + (((z) >> 1) & 3) * P1_STAGE + xk + (h) * 2048 + ra[(z) & 1])
#define P1_RO(z, h) xt[(z) & 1][h] = lds128(L + (((z) >> 1) & 3) * P1_STAGE + xo + (h) * 2048 + ra[(z) & 1])
#define P1_XF(z, i) ((i) < 2 ? xs[(z) >> 1][(z) & 1][(i) & 1] : xt[(z) & 1][(i) & 1])
#define P1_LOOP(MF) \
            BARRIER(); \
            STAMP2(11); \
            P1_RK(0, 0); P1_RW(0, 0); P1_RK(0, 1); P1_RO(0, 0); P1_RO(0, 1); P1_RW(0, 1); P1_RW(0, 2); P1_RW(0, 3); SBAR(); \
            UNROLL for (int z = 0; z < 20; ++z) { \
                UNROLL for (int j = 0; j < 4; ++j) { \
                    MF(z, 0, j); SBAR(); \
                    if (j == 0 && (z & 1) && z < 19) { BARRIER(); } \
                    MF(z, 1, j); SBAR(); \
                    if (z < 19) { if (j == 0) P1_RK(z + 1, 0); if (j == 1) P1_RK(z + 1, 1); if (j == 2) P1_RO(z + 1, 0); if (j == 3) P1_RO(z + 1, 1); SBAR(); } \
                    MF(z, 2, j); SBAR(); \
                    MF(z, 3, j); SBAR(); \
                    if (z < 19) { P1_RW(z + 1, j); SBAR(); } \
                } \
            }
#define P1_MFQ(z, i, j) acc[i][j] = MFMA16(cat8(wv[j][0], wv[j][1]), P1_XF(z, i), acc[i][j])
#define P1_MFV(z, i, j) acc[i][j] = MFMA16(P1_XF(z, i), cat8(wv[j][0], wv[j][1]), acc[i][j])
            u32x4 tv[8];
            if (nt < 10) {
                P1_LOOP(P1_MFQ)
                STAMP(1);
                const float sc = nt < 5 ? 0.32259889079f : 1.0f;
                UNROLL for (int i = 0; i < 4; i += 2)
                    UNROLL for (int j = 0; j < 4; ++j) {
                        unsigned a0 = pack2(acc[i][j][0] * sc, acc[i][j][1] * sc), a1 = pack2(acc[i][j][2] * sc, acc[i][j][3] * sc);
                        unsigned b0 = pack2(acc[i + 1][j][0] * sc, acc[i + 1][j][1] * sc), b1 = pack2(acc[i + 1][j][2] * sc, acc[i + 1][j][3] * sc);
                        auto s0 = __builtin_amdgcn_permlane16_swap(a0, b0, false, false);
                        auto s1 = __builtin_amdgcn_permlane16_swap(a1, b1, false, false);
                        tv[i * 2 + j] = u32x4{s0[0], s1[0], s0[1], s1[1]};
                    }
            } else {
                P1_LOOP(P1_MFV)
                STAMP(1);
                UNROLL for (int i = 0; i < 4; ++i)
                    UNROLL for (int j = 0; j < 4; j += 2) {
                        unsigned a0 = pack2(acc[i][j][0], acc[i][j][1]), a1 = pack2(acc[i][j][2], acc[i][j][3]);
                        unsigned b0 = pack2(acc[i][j + 1][0], acc[i][j + 1][1]), b1 = pack2(acc[i][j + 1][2], acc[i][j + 1][3]);
                        auto s0 = __builtin_amdgcn_permlane32_swap(a0, b0, false, false);
                        auto s1 = __builtin_amdgcn_permlane32_swap(a1, b1, false, false);
                        tv[i * 2 + (j >> 1)] = u32x4{s0[0], s1[0], s0[1], s1[1]};
                    }
            }
            UNROLL for (int i = 0; i < 8; ++i) PIN(tv[i]);
            UNROLL for (int z = 14; z < 18; ++z) UNROLL for (int h = 0; h < 2; ++h) P1_RK(z, h);
            WAITL();
            UNROLL for (int i = 0; i < 8; ++i)
                *(__attribute__((address_space(3))) u32x4*)(LW + (i < 4 ? 3 * P1_STAGE : 0) + xk + (i & 3) * 1024 + lane * 16) = tv[i];
        }
        BARRIER();
        __builtin_amdgcn_s_setprio(2);
        STAMP(7);
        f32x4 hc[2][5];
        UNROLL for (int h = 0; h < 2; ++h) UNROLL for (int u = 0; u < 5; ++u) hc[h][u] = f32x4{};
        UNROLL for (int z = 18; z < 20; ++z) UNROLL for (int h = 0; h < 2; ++h) P1_RK(z, h);
        s16x8 wb[2][5];
        UNROLL for (int z = 0; z < 21; ++z) {
            if (z == 16) { STAMP2(17); lds_wait(L + FL_FULL + fl4, 1); STAMP2(18); }
            UNROLL for (int u = 0; u < 5; ++u) {
                if (z < 20) { wb[z & 1][u] = lds128(L + WH_ADDR((z >> 1) * 10 + 2 * u) + ra[z & 1]); SBAR(); }
                if (z > 0) {
                    hc[0][u] = MFMA16(xs[(z - 1) >> 1][(z - 1) & 1][0], wb[(z - 1) & 1][u], hc[0][u]);
                    hc[1][u] = MFMA16(xs[(z - 1) >> 1][(z - 1) & 1][1], wb[(z - 1) & 1][u], hc[1][u]); SBAR();
                }
            }
        }
        STAMP2(19);
        WAITV(0);
        WT_STORE();
        lds_signal(LW + FL_FREE + wid * 4, 1);
        const float sg = nt < 8 ? 1.0f : -1.0f;
        const unsigned ab = WS_AB + (((nt & 7) >> 1) * 512 + (nt >> 3) * 256 + mt * 16 + wm * 8 + wn * 4 + (kg & 1) * 2 + (kg >> 1)) * 2560 + ((nt & 1) * 80 + l15) * 16;
        UNROLL for (int u = 0; u < 5; ++u) {
            unsigned a0 = pack2(hc[0][u][0] * sg, hc[0][u][1] * sg), a1 = pack2(hc[0][u][2] * sg, hc[0][u][3] * sg);
            unsigned b0 = pack2(hc[1][u][0] * sg, hc[1][u][1] * sg), b1 = pack2(hc[1][u][2] * sg, hc[1][u][3] * sg);
            auto s0 = __builtin_amdgcn_permlane16_swap(a0, b0, false, false);
            auto s1 = __builtin_amdgcn_permlane16_swap(a1, b1, false, false);
            st128(W, u32x4{s0[0], s1[0], s0[1], s1[1]}, ab, u * 256);
        }
        publish(W, WS_F2 + (nt * 64 + mt * 4 + wid) * FS, epoch, lane);
        if (lane == 0) st32(W, epoch, WS_ACK + (blockIdx.x * 8 + wid) * 4, 0);
    }
    }
    STAMP(2);
    const int lane = __builtin_amdgcn_mbcnt_hi(~0u, __builtin_amdgcn_mbcnt_lo(~0u, 0u));
    const int r = lane & 31, hh = lane >> 5;
    const int l15 = lane & 15, kg = lane >> 4;
    const unsigned fl4 = (lane & 3) * 4;

    const int ksp = xcd >> 1, nt4 = idx >> 3;
    const int tt = mt * 2 + (ksp & 1), ct = nt4 * 2 + (ksp >> 1);
    const u32x4 dc = mkdesc(ksp < 2 ? cre : cim, 2048 * 4096), dws = mkdesc(ws, WS_END);
    const unsigned cs0 = mt * 128 * 4096 + (ksp & 1) * 2048;
    const unsigned as0 = WS_AB + (nt4 * 512 + ksp * 128) * 2560;

    if (wid >= 4) {
        const int head = xcd * 4 + (idx >> 3), qt = idx & 7;
        const int qh = wid & 1, kh = (wid >> 1) & 1;
        const int col0 = head * 20, odd = head & 1;
        const int cA = (col0 >> 3) + odd, cH = odd ? (col0 >> 3) : (col0 >> 3) + 2;
        const int q0 = qt * 256 + qh * 128;
        unsigned k1off = WS_QK + (80 + cA + hh) * CH + r * 16;
        unsigned k2off = hh ? OOB : WS_QK + (80 + cH) * CH + r * 16 + odd * 8;
        unsigned q2off = hh ? OOB : k2off - 80 * CH;
        unsigned k2or = hh ? 0x3F80u : 0u;
        const int dv = odd ? (r < 16 ? r + 4 : r - 16) : r;
        const unsigned voff = r < 20 ? WS_VT + hh * VROW + (col0 + dv) * 16 : (r == 20 ? oneoff : OOB);
        unsigned vstep = r < 20 ? 4 * VROW : 0;
        unsigned va = voff + kh * 32 * vstep, vb2 = va + (vstep >> 1);
        unsigned cpf = CPF == 1 ? (lane < 8 ? (lane >> 1) * 4096 + (lane & 1) * 128 : OOB) : CPF == 2 ? (lane < 8 ? lane * 128 : OOB) : (lane < 8 ? (lane >> 2) * 4096 + (lane & 3) * 128 : OOB);
        constexpr int NB = 4;
        u32x4 k1[NB], v0[NB], v1[NB]; u32x2 k2[NB];
        f32x16 o[4], s[2];
        u32x4 pa = u32x4{}, pd = u32x4{};
        f32x8 e8 = f32x8{};
        UNROLL for (int i = 0; i < 4; ++i) { o[i] = f32x16{}; PIN(o[i]); }
        v0[NB - 1] = u32x4{}; v1[NB - 1] = u32x4{};
        PIN(v0[NB - 1]); PIN(v1[NB - 1]); PIN(pa); PIN(pd); PIN(e8);
        PIN(k1off); PIN(k2off); PIN(q2off); PIN(k2or); PIN(va); PIN(vb2); PIN(vstep); PIN(cpf);
        WAITV(0);
        if (nt != 15 && lane == 0) st32(W, epoch, WS_F1 + ((nt * 2 + (gwv >> 1)) * 32 + mt * 2 + (gwv & 1)) * FS, 0);
        lds_post(LW + FL_FULL + gwv * 4, 1);
        const int nsa = (lane & 4) ? (col0 + 19) >> 6 : col0 >> 6;
        unsigned fkv = WS_F1 + ((10 + ((lane >> 3) & 1) * 10 + nsa) * 32 + kh * 16) * FS;
        STAMP2(23);
        wait_flags1<SL1>(W, (lane & 16) ? WS_F1 + (nsa * 32 + (q0 >> 6) + (lane & 1)) * FS : fkv + (lane & 3) * FS, epoch);
        fkv += (lane & 1) * FS;
        STAMP(3);
        u32x4 q1[4]; u32x2 q2[4];
        UNROLL for (int i = 0; i < 4; ++i) {
            const int qr = q0 + ((i + 2 + 2 * kh) & 3) * 32;
            q2[i] = ld64(W, q2off, qr * 16);
            q1[i] = ld128(W, k1off - 80 * CH, qr * 16);
        }
        const int t0 = kh * 32;
        unsigned fnx = epoch;
        SBAR();
        UNROLL for (int b = 0; b < NB; ++b) {
            k2[b] = ld64(W, k2off, (t0 + b) * 512); k1[b] = ld128(W, k1off, (t0 + b) * 512); SBAR();
            if (b < NB - 1) { v0[b] = ld128(W, va, 0); v1[b] = ld128(W, vb2, 0); va += vstep; vb2 += vstep; SBAR(); }
        }
        const f32x16 zero16 = f32x16{};
        {
            f32x16 t[4];
            UNROLL for (int i = 0; i < 4; ++i) { t[i] = MFMA32K8(as4(k2[0]), as4(q2[i]), zero16); t[i] = MFMA32(as8(k1[0]), as8(q1[i]), t[i]); SBAR(); }
            k2[0][0] |= k2or;
            UNROLL for (int i = 0; i < 4; ++i) {
                float m = t[i][0];
                UNROLL for (int e = 1; e < 16; ++e) m = fmaxf(m, t[i][e]);
                auto mm = __builtin_amdgcn_permlane32_swap(__float_as_uint(m), __float_as_uint(m), false, false);
                m = fmaxf(__uint_as_float(mm[0]), __uint_as_float(mm[1]));
                q2[i][0] = hh ? pack2(-m, 0.f) & 0xFFFFu : q2[i][0];
                if (i == 0) {
                    s[0] = MFMA32K8(as4(k2[0]), as4(q2[0]), zero16);
                    s[0] = MFMA32(as8(k1[0]), as8(q1[0]), s[0]);
                }
            }
        }
        dma4(lane < 8 ? (lane >> 1) * 20480 + (lane & 1) * 128 : OOB, dws, WS_O + ((q0 + kh * 64) >> 4) * 20480 + cH * 256, lds0 + LDS_SINK);
        st32(W, epoch, lane == 0 ? WS_ACK + (blockIdx.x * 8 + wid) * 4 : OOB, 0);
        __builtin_amdgcn_s_setprio(1);
        STAMP(4);
        NOUNROLL for (int tb = 0; tb < 32; tb += NB) {
            if (__builtin_expect(!__all(fnx == epoch), 0)) wait_flags1<SL1>(W, fkv + (tb < 28 ? tb + 4 : 0) * (FS / 2), epoch);
            if (tb == 16) lds_wait(L + FL_FREE + fl4, 1);
            const unsigned fso = (tb < 24 ? tb + 8 : 0) * (FS / 2);
            SBAR();
            UNROLL for (int b = 0; b < NB; ++b) {
                const int bn = (b + 1) % NB, bp = (b + NB - 1) % NB;
                const unsigned kso = tb + b + NB < 32 ? (t0 + tb + b + NB) * 512 : OOB / 2;
                const unsigned vso = tb + b + NB - 1 < 32 ? 0 : OOB / 2;
                const unsigned cso = tb >= 16 ? OOB / 2 : CPF == 1 ? cs0 + (nt4 * 32 + gwv * 8 + (b & 1) * 4) * 4096 + ((tb + b) >> 1) * 256 :
                                     CPF == 2 ? cs0 + (nt4 * 32 + gwv * 8 + ((tb + b) & 7)) * 4096 + (tb >> 3) * 1024 :
                                                cs0 + (nt4 * 32 + gwv * 8 + b * 2) * 4096 + (tb >> 2) * 512;
                UNROLL for (int qi = 0; qi < 4; ++qi) {
                    const int c = qi & 1, n = c ^ 1, qn = (qi + 1) & 3, qp = (qi + 3) & 3;
                    const int kb = qi == 3 ? bn : b, vb = qi == 0 ? bp : b;
                    f32x16& SC = s[c]; f32x16& SN = s[n];
                    SN = MFMA32K8(as4(k2[kb]), as4(q2[qn]), zero16); SBAR();
                    SC[0] = EX2(SC[0]); SC[1] = EX2(SC[1]); SC[2] = EX2(SC[2]); SC[3] = EX2(SC[3]); SBAR();
                    pd[0] = pack2(e8[0], e8[1]); pd[1] = pack2(e8[2], e8[3]); PIN(SC); PIN(pd); SBAR();
                    SN = MFMA32(as8(k1[kb]), as8(q1[qn]), SN); SBAR();
                    if (qi == 2) { k2[b] = ld64(W, k2off, kso); k1[b] = ld128(W, k1off, kso); SBAR(); }
                    if (qi == 3 && b == 0) { fnx = ld32(W, fkv, fso); SBAR(); }
                    if (CPF && qi == 1) { dma4(cpf, dc, cso, lds0 + LDS_SINK); SBAR(); }
                    SC[4] = EX2(SC[4]); SC[5] = EX2(SC[5]); SC[6] = EX2(SC[6]); SC[7] = EX2(SC[7]); SBAR();
                    pd[2] = pack2(e8[4], e8[5]); pd[3] = pack2(e8[6], e8[7]); PIN(SC); PIN(pd); SBAR();
                    o[qp] = MFMA32(as8(v0[vb]), as8(pa), o[qp]); SBAR();
                    e8[0] = EX2(SC[8]); e8[1] = EX2(SC[9]); e8[2] = EX2(SC[10]); e8[3] = EX2(SC[11]); SBAR();
                    pa[0] = pack2(SC[0], SC[1]); pa[1] = pack2(SC[2], SC[3]);
                    if (qi == 2) { k2[bn][0] |= k2or; PIN(k2[bn]); }
                    if (qi == 1) { va += vstep; vb2 += vstep; PIN(va); PIN(vb2); }
                    PIN(e8); PIN(pa); SBAR();
                    o[qp] = MFMA32(as8(v1[vb]), as8(pd), o[qp]); SBAR();
                    if (qi == 0) { v0[bp] = ld128(W, va, vso); v1[bp] = ld128(W, vb2, vso); SBAR(); }
                    e8[4] = EX2(SC[12]); e8[5] = EX2(SC[13]); e8[6] = EX2(SC[14]); e8[7] = EX2(SC[15]); SBAR();
                    pa[2] = pack2(SC[4], SC[5]); pa[3] = pack2(SC[6], SC[7]); PIN(e8); PIN(pa); SBAR();
                }
            }
        }
        __builtin_amdgcn_s_setprio(3);
        UNROLL for (int e = 0; e < 4; ++e) pd[e] = pack2(e8[2 * e], e8[2 * e + 1]);
        o[3] = MFMA32(as8(v0[NB - 1]), as8(pa), o[3]);
        o[3] = MFMA32(as8(v1[NB - 1]), as8(pd), o[3]);
        SBAR();
        STAMP(5);
        const int f0 = q0 + kh * 64;
        UNROLL for (int j = 0; j < 2; ++j) {
            const lds_p ex = LW + LDS_EX + (gwv * 2 + j) * EXB;
            *(__attribute__((address_space(3))) u32x4*)(ex + lane * 16) =
                u32x4{pack2(o[j][0], o[j][1]), pack2(o[j][2], o[j][3]), pack2(o[j][4], o[j][5]), pack2(o[j][6], o[j][7])};
            *(__attribute__((address_space(3))) u32x2*)(ex + 1024 + lane * 8) =
                hh ? u32x2{__float_as_uint(o[j][8]), q2[j][0] << 16} : u32x2{pack2(o[j][8], o[j][9]), pack2(o[j][10], o[j][11])};
        }
        lds_signal(LW + FL_EX + gwv * 4);
        float n1[2], l1[2];
        UNROLL for (int j = 0; j < 2; ++j) {
            auto nn = __builtin_amdgcn_permlane32_swap(q2[2 + j][0] << 16, q2[2 + j][0] << 16, false, false);
            auto ll = __builtin_amdgcn_permlane32_swap(__float_as_uint(o[2 + j][8]), __float_as_uint(o[2 + j][8]), false, false);
            n1[j] = __uint_as_float(nn[1]); l1[j] = __uint_as_float(ll[1]);
        }
        const unsigned orow = WS_O + ((f0 >> 4) + (r >> 4)) * 20480 + (r & 15) * 16;
        const unsigned oa = orow + (cA + hh) * 256, ob = hh ? OOB : orow + cH * 256 + odd * 8;
        lds_wait(L + FL_EX + (gwv ^ 2) * 4);
        STAMP2(22);
        bool ok = true;
        u32x4 ra[2]; u32x2 rb[2];
        UNROLL for (int j = 0; j < 2; ++j) {
            const lds_cp rx = L + LDS_EX + ((gwv ^ 2) * 2 + j) * EXB;
            const u32x4 a = *(const __attribute__((address_space(3))) u32x4*)(rx + lane * 16);
            const u32x2 c = *(const __attribute__((address_space(3))) u32x2*)(rx + 1024 + r * 8);
            const u32x2 d = *(const __attribute__((address_space(3))) u32x2*)(rx + 1280 + r * 8);
            const float n2 = __uint_as_float(d[1]), l2 = __uint_as_float(d[0]), nm = fminf(n1[j], n2);
            float w1 = EX2(nm - n1[j]), w2 = EX2(nm - n2);
            const float l = l1[j] * w1 + l2 * w2, rl = __builtin_amdgcn_rcpf(l);
            w1 *= rl; w2 *= rl;
            float f[12];
            UNROLL for (int e = 0; e < 4; ++e) { f[2 * e] = o[2 + j][2 * e] * w1 + bflo(a[e]) * w2; f[2 * e + 1] = o[2 + j][2 * e + 1] * w1 + bfhi(a[e]) * w2; }
            UNROLL for (int e = 0; e < 2; ++e) { f[8 + 2 * e] = o[2 + j][8 + 2 * e] * w1 + bflo(c[e]) * w2; f[9 + 2 * e] = o[2 + j][9 + 2 * e] * w1 + bfhi(c[e]) * w2; }
            float sa = 0.f;
            UNROLL for (int e = 0; e < 12; ++e) sa += fabsf(f[e]);
            ok = ok && l1[j] < 1e30f && l2 < 1e30f && l > 1e-18f && sa < 3e38f;
            auto s0 = __builtin_amdgcn_permlane32_swap(pack2(f[0], f[1]), pack2(f[4], f[5]), false, false);
            auto s1 = __builtin_amdgcn_permlane32_swap(pack2(f[2], f[3]), pack2(f[6], f[7]), false, false);
            ra[j] = u32x4{s0[0], s1[0], s0[1], s1[1]};
            rb[j] = u32x2{pack2(f[8], f[9]), pack2(f[10], f[11])};
        }
        if (__builtin_expect(__all(ok), 1)) {
            UNROLL for (int j = 0; j < 2; ++j) { st128(W, ra[j], oa + j * 40960, 0); st64(W, rb[j], ob + j * 40960, 0); }
        } else {
            wait_flags1<SL1>(W, WS_F1 + ((10 + hh * 10 + (col0 >> 6)) * 32 + r) * FS, epoch);
            wait_flags1<SL1>(W, WS_F1 + ((10 + hh * 10 + ((col0 + 19) >> 6)) * 32 + r) * FS, epoch);
            NOUNROLL for (int j = 0; j < 2; ++j) {
                const u32x2 z2 = ld64(W, q2off, (f0 + j * 32) * 16);
                const u32x4 z1 = ld128(W, k1off - 80 * CH, (f0 + j * 32) * 16);
                float m = -3e38f;
                f32x16 oo = f32x16{};
                NOUNROLL for (int t = 0; t < 64; ++t) {
                    const u32x4 a1 = ld128(W, k1off, t * 512); const u32x2 a2 = ld64(W, k2off, t * 512);
                    const u32x4 w0 = ld128(W, voff + t * vstep, 0), w1 = ld128(W, voff + t * vstep + (vstep >> 1), 0);
                    f32x16 z = MFMA32K8(as4(a2), as4(z2), zero16);
                    z = MFMA32(as8(a1), as8(z1), z);
                    float mx = z[0];
                    UNROLL for (int e = 1; e < 16; ++e) mx = fmaxf(mx, z[e]);
                    auto mm = __builtin_amdgcn_permlane32_swap(__float_as_uint(mx), __float_as_uint(mx), false, false);
                    const float mn = fmaxf(m, fmaxf(__uint_as_float(mm[0]), __uint_as_float(mm[1])));
                    const float al = EX2(m - mn);
                    m = mn;
                    UNROLL for (int e = 0; e < 16; ++e) oo[e] *= al;
                    u32x4 p0, p1;
                    UNROLL for (int e = 0; e < 4; ++e) { p0[e] = pack2(EX2(z[2 * e] - m), EX2(z[2 * e + 1] - m)); p1[e] = pack2(EX2(z[8 + 2 * e] - m), EX2(z[9 + 2 * e] - m)); }
                    oo = MFMA32(as8(w0), as8(p0), oo);
                    oo = MFMA32(as8(w1), as8(p1), oo);
                }
                auto l2 = __builtin_amdgcn_permlane32_swap(__float_as_uint(oo[8]), __float_as_uint(oo[8]), false, false);
                const float sc = 1.0f / __uint_as_float(l2[1]);
                auto s0 = __builtin_amdgcn_permlane32_swap(pack2(oo[0] * sc, oo[1] * sc), pack2(oo[4] * sc, oo[5] * sc), false, false);
                auto s1 = __builtin_amdgcn_permlane32_swap(pack2(oo[2] * sc, oo[3] * sc), pack2(oo[6] * sc, oo[7] * sc), false, false);
                st128(W, u32x4{s0[0], s1[0], s0[1], s1[1]}, oa + j * 40960, 0);
                st64(W, u32x2{pack2(oo[8] * sc, oo[9] * sc), pack2(oo[10] * sc, oo[11] * sc)}, ob + j * 40960, 0);
            }
        }
        STAMP(6);
        publish(W, WS_F3 + ((f0 >> 6) * 32 + head) * FS, epoch, lane);
        STAMP(7);
        if (blockIdx.x == 0 && wid == 4) {
            NOUNROLL for (unsigned spins = 0; spins < SPIN_CAP; ++spins) {
                asm volatile("" ::: "memory");
                bool ok = true;
                UNROLL for (int i = 0; i < 8; ++i) {
                    const u32x4 f = ld128(W, WS_ACK + i * 1024 + lane * 16, 0);
                    ok = ok && f[0] == epoch && f[1] == epoch && f[2] == epoch && f[3] == epoch;
                }
                if (__all(ok)) break;
                __builtin_amdgcn_s_sleep(32);
            }
            if (lane == 0) st32(W, epoch, WS_EP, 0);
        }
        return;
    }

    __builtin_amdgcn_s_setprio(2);
    u32x4 wf[5][5];
    {
        unsigned coff[2]; const unsigned lin = lane * 16;
        UNROLL for (int i = 0; i < 2; ++i) coff[i] = (lane >> 3) * 4096 + (((lane & 7) ^ ((4 * i + (lane >> 4)) & 7)) << 4);
#define SQ_DC(n, slot, i) dma16(coff[(i) & 1], dc, cs0 + gwv * 131072 + (i) * 32768 + (n) * 128, lds0 + (slot) * SQ_STAGE + gwv * 4096 + (i) * 1024)
#define SQ_DC4(n, slot) do { const unsigned c_ = cs0 + gwv * 131072 + (n) * 128; \
            dma16x4(coff[0], coff[1], dc, c_, c_ + 31744, c_ + 63488, c_ + 95232, lds0 + (slot) * SQ_STAGE + gwv * 4096); } while (0)
#define SQ_DA5(n, slot) do { dma16sx4(lin, dws, as0 + (n) * 20480 + gwv * 5120, lds0 + (slot) * SQ_STAGE + 16384 + gwv * 5120); SQ_DA(n, slot, 4); } while (0)
#define SQ_DA(n, slot, i) dma16s(lin, dws, as0 + (n) * 20480 + gwv * 5120 + (i) * 1024, lds0 + (slot) * SQ_STAGE + 16384 + gwv * 5120 + (i) * 1024)
        const int wm = wid & 1, wn = wid >> 1;
        f32x4 acc[4][5];
        UNROLL for (int i = 0; i < 4; ++i) UNROLL for (int u = 0; u < 5; ++u) { acc[i][u] = f32x4{}; PIN(acc[i][u]); }
        unsigned ra[2];
        UNROLL for (int ks = 0; ks < 2; ++ks) ra[ks] = wm * 8192 + l15 * 128 + (((4 * ks + kg) ^ ((l15 >> 1) & 7)) << 4);
        const unsigned rb = 16384 + kg * 2560 + (wn * 80 + l15) * 16;
        s16x8 fc[2][4], fa[2][5];
        UNROLL for (int i = 0; i < 4; ++i) fc[1][i] = s16x8{};
        UNROLL for (int u = 0; u < 5; ++u) fa[1][u] = s16x8{};
        if (wid == 0) {
            const unsigned f2 = WS_F2 + (((ksp >> 1) * 8 + nt4 * 2 + hh) * 64 + (ksp & 1) * 32 + r) * FS;
            __builtin_amdgcn_s_sleep(16);
            wait_flags1<12>(W, f2, epoch);
            lds_wait(L + FL_FREE + fl4, 1);
            lds_post(LW + FL_FIN, 1);
        } else {
            lds_wait<LSL>(L + FL_FIN, 1);
        }
        STAMP(3);
        UNROLL for (int n = 0; n < 2; ++n) { SQ_DC4(n, n); SQ_DA5(n, n); }
        const lds_p pw = LW + (lane == 0 ? FL_SFULL + gwv * 4 : LDS_SINK + lane * 4);
        const lds_cp pr = L + FL_SFULL + fl4;
        const unsigned wfo = WS_WOT + (ct * 4 + gwv) * 25600 + lane * 16;
#define SQ_MF(u) do { UNROLL for (int i = 0; i < 4; ++i) acc[i][u] = MFMA16(fa[c][u], fc[c][i], acc[i][u]); SBAR(); } while (0)
#define SQ_RC(i) fc[n][i] = lds128(sr[ks] + (i) * 2048)
#define SQ_RA(u) fa[n][u] = lds128(sp + ks * 10240 + (u) * 256)
#define SQ_STG(DMA) do { \
            lds_cp sp = L + (j & 3) * SQ_STAGE + rb; lds_cp sr[2]; \
            UNROLL for (int ks = 0; ks < 2; ++ks) { sr[ks] = L + (j & 3) * SQ_STAGE + ra[ks]; PIN(sr[ks]); } \
            unsigned pv = j + 2, fl = 0; \
            PIN(sp); PIN(pv); SBAR(); \
            __builtin_amdgcn_s_setprio(GPRIO); \
            const int sf = (j + 2) & 3; \
            UNROLL for (int ks = 0; ks < 2; ++ks) { \
                const int n = ks, c = n ^ 1; \
                SQ_RC(0); SQ_RC(1); SQ_RC(2); SBAR(); SQ_MF(0); \
                SQ_RC(3); SQ_RA(0); SQ_RA(1); SBAR(); SQ_MF(1); \
                SQ_RA(2); SQ_RA(3); SQ_RA(4); \
                if (ks == 0) { WAITV(0); lds_post(pw, pv); } \
                SBAR(); SQ_MF(2); \
                if (DMA && ks == 0) { SQ_DC4(j + 2, sf); SBAR(); } \
                if (ks == 1) { fl = lds_peek(pr); SBAR(); } \
                SQ_MF(3); \
                if (DMA && ks == 0) { SQ_DA5(j + 2, sf); SBAR(); } \
                SQ_MF(4); \
            } \
            __builtin_amdgcn_s_setprio(3); \
            if (__builtin_expect(!__all(fl >= pv), 0)) lds_wait<LS2>(pr, pv); } while (0)
        int j = 0;
        WAITV(9); lds_post(pw, 1); lds_wait(pr, 1);
        NOUNROLL for (; j < 14; ++j) SQ_STG(true);
        NOUNROLL for (; j < 16; ++j) SQ_STG(false);
        UNROLL for (int u = 0; u < 5; ++u) UNROLL for (int i = 0; i < 4; ++i) acc[i][u] = MFMA16(fa[1][u], fc[1][i], acc[i][u]);
        __builtin_amdgcn_s_setprio(3);
        STAMP(4);
        if (wid == ksp) {
            UNROLL for (int i = 0; i < 4; ++i) UNROLL for (int u = 0; u < 5; ++u) stsf4(LW + 2 * SQ_STAGE + (i * 5 + u) * 1024 + lane * 16, acc[i][u]);
            const int px = r >> 2;
            const unsigned fw = WS_F2 + (((px >> 1) * 4 + hh) * 64 + ((px & 1) * 8 + ct) * 4 + (r & 3)) * FS;
            const unsigned fg = WS_F2 + (15 * 64 + (tt >> 1) * 4 + (r & 3)) * FS;
            wait_flags4<12>(W, fw, fw + 128 * FS, fg, fg, epoch);
        } else {
            const unsigned yb = WS_Y + ((((mt * 2 + wm) * 8 + nt4 * 2 + wn) * 4 + ksp) * 4) * 2560;
            UNROLL for (int i = 0; i < 4; ++i) {
                UNROLL for (int u = 0; u < 4; u += 2)
                    st128(W, u32x4{pack2(acc[i][u][0], acc[i][u][1]), pack2(acc[i][u][2], acc[i][u][3]), pack2(acc[i][u + 1][0], acc[i][u + 1][1]), pack2(acc[i][u + 1][2], acc[i][u + 1][3])},
                          yb + lane * 16, i * 2560 + u * 512);
                st64(W, u32x2{pack2(acc[i][4][0], acc[i][4][1]), pack2(acc[i][4][2], acc[i][4][3])}, yb + i * 2560 + 2048 + lane * 8, 0);
            }
            publish(W, WS_F4 + (((mt * 2 + wm) * 8 + nt4 * 2 + wn) * 4 + ksp) * FS, epoch, lane);
        }
        lds_signal(pw, 18); lds_wait<LSL>(pr, 18);
        UNROLL for (int st = 0; st < 5; ++st) UNROLL for (int u = 0; u < 5; ++u) wf[st][u] = ld128(W, wfo, (st * 5 + u) * 1024);
        STAMP(5);
    }
    const int t0 = tt * 64;
    const float gt = __uint_as_float(ld32(W, WS_G + (t0 + gwv * 16 + l15) * 4, 0));
    WAITV(0);
    lds_wait<LSL>(L + FL_EX + fl4, 1);
    STAMP(6);
    wait_flags1<SL3>(W, WS_F4 + ((tt * 8 + ct) * 4 + ((ksp + 1 + (r % 3)) & 3)) * FS, epoch);
    u32x4 ya[3][2]; u32x2 yc[3];
    UNROLL for (int p = 0; p < 3; ++p) {
        const unsigned yo = WS_Y + (((tt * 8 + ct) * 4 + ((ksp + 1 + p) & 3)) * 4 + gwv) * 2560;
        ya[p][0] = ld128(W, lane * 16, yo); ya[p][1] = ld128(W, lane * 16 + 1024, yo); yc[p] = ld64(W, lane * 8 + 2048, yo);
    }
    const float h = 1.0f - gt;
    f32x4 ys[5];
    UNROLL for (int u = 0; u < 5; ++u) ys[u] = ldsf4(L + 2 * SQ_STAGE + (gwv * 5 + u) * 1024 + lane * 16);
    SBAR();
    wait_flags1<SL3>(W, WS_F3 + (tt * 32 + gwv * 8 + (lane & 7)) * FS, epoch);
    STAMP(8);
    const unsigned boff = WS_O + tt * 81920 + gwv * 5120 + lane * 16;
    u32x4 bf[4][5];
    UNROLL for (int d = 0; d < 4; ++d)
        UNROLL for (int st = 0; st < 5; ++st) bf[d][st] = ld128(W, boff + st * 1024, ((gwv + d + 1) & 3) * 20480);
    SBAR();
    UNROLL for (int u = 0; u < 5; ++u) {
        UNROLL for (int p = 0; p < 3; ++p) {
            const unsigned w0 = u < 4 ? ya[p][(u >> 1) & 1][(u & 1) * 2] : yc[p][0], w1 = u < 4 ? ya[p][(u >> 1) & 1][(u & 1) * 2 + 1] : yc[p][1];
            ys[u][0] += bflo(w0); ys[u][1] += bfhi(w0); ys[u][2] += bflo(w1); ys[u][3] += bfhi(w1);
        }
        ys[u] *= h; PIN(ys[u]);
    }
    SBAR();
    f32x4 res[5];
    UNROLL for (int d = 0; d < 4; ++d) {
        UNROLL for (int u = 0; u < 5; ++u) res[u] = f32x4{};
        UNROLL for (int st = 0; st < 5; ++st) UNROLL for (int u = 0; u < 5; ++u) res[u] = MFMA16(as8(wf[st][u]), as8(bf[d][st]), res[u]);
        if (d < 3) { UNROLL for (int u = 0; u < 5; ++u) stsf4(LW + ((gwv + d + 1) & 3) * GW_LDS + (d * 5 + u) * 1024 + lane * 16, res[u]); }
        if (d == 2) lds_signal(LW + FL_RED + gwv * 4);
        SBAR();
    }
    STAMP(9);
    lds_wait(L + FL_RED + fl4);
    UNROLL for (int d = 0; d < 3; ++d) UNROLL for (int u = 0; u < 5; ++u) res[u] += ldsf4(L + gwv * GW_LDS + (d * 5 + u) * 1024 + lane * 16);
    UNROLL for (int u = 0; u < 5; ++u) res[u] = res[u] * gt + ys[u];
    char* dst = out + (size_t)(t0 + gwv * 16 + l15) * 1280 + ct * 160 + kg * 8;
    const rsrc_t RO = __builtin_amdgcn_make_buffer_rsrc(out, 0, 2048 * 1280, 0x00020000);
    const unsigned oo = (t0 + gwv * 16 + l15) * 1280 + ct * 160;
    UNROLL for (int u = 0; u < 4; u += 2) {
        auto s0 = __builtin_amdgcn_permlane16_swap(pack2(res[u][0], res[u][1]), pack2(res[u + 1][0], res[u + 1][1]), false, false);
        auto s1 = __builtin_amdgcn_permlane16_swap(pack2(res[u][2], res[u][3]), pack2(res[u + 1][2], res[u + 1][3]), false, false);
        __builtin_amdgcn_raw_buffer_store_b128(u32x4{s0[0], s1[0], s0[1], s1[1]}, RO, oo + (u + (kg & 1)) * 32 + (kg >> 1) * 16, 0, OUTM == 1 ? 0 : OUTM == 2 ? 16 : 2);
    }
    __builtin_amdgcn_raw_buffer_store_b64(u32x2{pack2(res[4][0], res[4][1]), pack2(res[4][2], res[4][3])}, RO, oo + 128 + kg * 8, 0, OUTM == 1 ? 0 : OUTM == 2 ? 16 : 2);
    STAMP(10);
}

extern "C" void kernel_launch(void* const* d_in, const int* in_sizes, int n_in, void* d_out, int out_size,
                              void* d_ws, size_t ws_size, hipStream_t stream) {
    (void)hipFuncSetAttribute((const void*)fused, hipFuncAttributeMaxDynamicSharedMemorySize, LDS_BYTES);
    fused<<<256, 512, LDS_BYTES, stream>>>((const char*)d_in[0], (const char*)d_in[1], (const char*)d_in[2], (const char*)d_in[4],
                                           (const char*)d_in[5], (const char*)d_in[6], (const char*)d_in[7], (const char*)d_in[8],
                                           (char*)d_ws + WS_BASE(ws_size), (char*)d_out);
}
```

```cpp
#include <hip/hip_runtime.h>
#include <hip/hip_bf16.h>
#include <stdint.h>

typedef short s16x8 __attribute__((ext_vector_type(8)));
typedef short s16x4 __attribute__((ext_vector_type(4)));
typedef float f32x16 __attribute__((ext_vector_type(16)));
typedef float f32x8 __attribute__((ext_vector_type(8)));
typedef float f32x4 __attribute__((ext_vector_type(4)));
typedef unsigned u32x4 __attribute__((ext_vector_type(4)));
typedef unsigned u32x2 __attribute__((ext_vector_type(2)));
typedef __bf16 bf16x2_t __attribute__((ext_vector_type(2)));
typedef __attribute__((address_space(3))) const char* lds_cp;
typedef __attribute__((address_space(3))) char* lds_p;
typedef __amdgpu_buffer_rsrc_t rsrc_t;

#define DI __device__ __forceinline__
#define UNROLL _Pragma("unroll")
#define NOUNROLL _Pragma("unroll 1")
#define MFMA32(a, b, c) __builtin_amdgcn_mfma_f32_32x32x16_bf16((a), (b), (c), 0, 0, 0)
#define MFMA32K8(a, b, c) __builtin_amdgcn_mfma_f32_32x32x8bf16_1k((a), (b), (c), 0, 0, 0)
#define MFMA16(a, b, c) __builtin_amdgcn_mfma_f32_16x16x32_bf16((a), (b), (c), 0, 0, 0)
#define SBAR() __builtin_amdgcn_sched_barrier(0)
#define PIN(x) asm volatile("" : "+v"(x))
#define EX2(x) __builtin_amdgcn_exp2f(x)
#define SC1 16

constexpr unsigned CH = 2048 * 16;
constexpr unsigned WS_QK = 0;
constexpr unsigned WS_O = 160 * CH;
constexpr unsigned WS_Y = WS_O + 80 * CH;
constexpr unsigned ABROW = 640 * 16;
constexpr unsigned WS_AB = WS_Y + 320 * CH;
constexpr unsigned VROW = 10240;
constexpr unsigned WS_VT = WS_AB + 512 * ABROW;
constexpr unsigned WS_WOT = WS_VT + 256 * VROW;
constexpr unsigned WS_G = WS_WOT + 80 * ABROW;
constexpr unsigned FS = 128;
constexpr unsigned WS_ACK = WS_G + 8192;
constexpr unsigned WS_EP = WS_ACK + 8192;
constexpr unsigned WS_F1 = WS_EP + 4096;
constexpr unsigned WS_F2 = WS_F1 + 1024 * FS;
constexpr unsigned WS_F3 = WS_F2 + 1024 * FS;
constexpr unsigned WS_F4 = WS_F3 + 1024 * FS;
constexpr unsigned WS_ONE = WS_F4 + 1024 * FS;
constexpr unsigned WS_END = WS_ONE + 2048 * 128;
constexpr unsigned OOB = 0x80000000u;
#define WSTAIL 1
#define WS_BASE(n) (WSTAIL ? (((n) - WS_END) & ~(size_t)0x1FFFFF) : (size_t)0)

constexpr int P1_STAGE = 32768;
constexpr int SQ_STAGE = 36864;
constexpr int GW_LDS = 15360;
constexpr int EXB = 1536;
constexpr int LDS_EX = 4 * SQ_STAGE;
constexpr int LDS_X9 = 4 * P1_STAGE;
constexpr int LDS_GW = LDS_X9 + 20480;
constexpr int LDS_WT = LDS_GW + 2048;
constexpr int LDS_FL = LDS_EX + 8 * EXB;
constexpr int LDS_SINK = LDS_FL + 256;
constexpr int LDS_BYTES = LDS_SINK + 256;
static_assert(LDS_BYTES <= 163840 && LDS_FL >= LDS_WT + 4096, "lds");
constexpr int FL_FULL = LDS_FL, FL_GO = LDS_FL + 32, FL_EX = LDS_FL + 48, FL_SFULL = LDS_FL + 80,
              FL_SFREE = LDS_FL + 96, FL_FIN = LDS_FL + 112, FL_RED = LDS_FL + 128, FL_FREE = LDS_FL + 144;
#define GPRIO 0
#define FWARM 1
#define HI 0
#define OUTM 3
#define LSL 8
#define LS2 3
#define PSL 8
#define SL1 4
#define SL3 4
#define SJ 4
#define PUBZ 3
#define AABL 0
#define SABL 0
#define PFX 1
#define CPF 0
#define CODE_LINES 300

#define STAMP(i) do { } while (0)
#define TACC(a) do { } while (0)
#define TDECL() do { } while (0)
#define TOUT(i) do { } while (0)
#define STAMP2(i) do { } while (0)

DI unsigned pack2(float a, float b) {
    bf16x2_t v = {(__bf16)a, (__bf16)b};
    return __builtin_bit_cast(unsigned, v);
}
DI float bflo(unsigned u) { return __uint_as_float(u << 16); }
DI float bfhi(unsigned u) { return __uint_as_float(u & 0xFFFF0000u); }
DI s16x8 as8(u32x4 v) { return __builtin_bit_cast(s16x8, v); }
DI s16x4 as4(u32x2 v) { return __builtin_bit_cast(s16x4, v); }

DI u32x4 mkdesc(const void* p, unsigned bytes) {
    const unsigned long long a = (unsigned long long)p;
    return u32x4{(unsigned)a, (unsigned)(a >> 32) & 0xFFFFu, bytes, 0x00020000u};
}
DI void dma16(unsigned voff, u32x4 desc, unsigned soff, unsigned ldsaddr) {
    unsigned keep;
    asm volatile("s_mov_b32 %0, m0\n\ts_mov_b32 m0, %4\n\ts_nop 0\n\tbuffer_load_dwordx4 %1, %2, %3 offen lds\n\ts_mov_b32 m0, %0"
                 : "=&s"(keep) : "v"(voff), "s"(desc), "s"(soff), "s"(ldsaddr) : "memory");
}
DI void dma16s(unsigned voff, u32x4 desc, unsigned soff, unsigned ldsaddr) {
    unsigned keep;
    asm volatile("s_mov_b32 %0, m0\n\ts_mov_b32 m0, %4\n\ts_nop 0\n\tbuffer_load_dwordx4 %1, %2, %3 offen sc1 lds\n\ts_mov_b32 m0, %0"
                 : "=&s"(keep) : "v"(voff), "s"(desc), "s"(soff), "s"(ldsaddr) : "memory");
}
DI void dma16x4(unsigned v0, unsigned v1, u32x4 desc, unsigned s0, unsigned s1, unsigned s2, unsigned s3, unsigned ldsaddr) {
    unsigned keep;
    asm volatile("s_mov_b32 %0, m0\n\ts_mov_b32 m0, %8\n\ts_nop 0\n\t"
                 "buffer_load_dwordx4 %1, %3, %4 offen lds\n\tbuffer_load_dwordx4 %2, %3, %5 offen offset:1024 lds\n\t"
                 "buffer_load_dwordx4 %1, %3, %6 offen offset:2048 lds\n\tbuffer_load_dwordx4 %2, %3, %7 offen offset:3072 lds\n\ts_mov_b32 m0, %0"
                 : "=&s"(keep) : "v"(v0), "v"(v1), "s"(desc), "s"(s0), "s"(s1), "s"(s2), "s"(s3), "s"(ldsaddr) : "memory");
}
DI void dma16sx4(unsigned voff, u32x4 desc, unsigned soff, unsigned ldsaddr) {
    unsigned keep;
    asm volatile("s_mov_b32 %0, m0\n\ts_mov_b32 m0, %4\n\ts_nop 0\n\t"
                 "buffer_load_dwordx4 %1, %2, %3 offen sc1 lds\n\tbuffer_load_dwordx4 %1, %2, %3 offen offset:1024 sc1 lds\n\t"
                 "buffer_load_dwordx4 %1, %2, %3 offen offset:2048 sc1 lds\n\tbuffer_load_dwordx4 %1, %2, %3 offen offset:3072 sc1 lds\n\ts_mov_b32 m0, %0"
                 : "=&s"(keep) : "v"(voff), "s"(desc), "s"(soff), "s"(ldsaddr) : "memory");
}
DI void dma4(unsigned voff, u32x4 desc, unsigned soff, unsigned ldsaddr) {
    unsigned keep;
    asm volatile("s_mov_b32 %0, m0\n\ts_mov_b32 m0, %4\n\ts_nop 0\n\tbuffer_load_dword %1, %2, %3 offen sc1 lds\n\ts_mov_b32 m0, %0"
                 : "=&s"(keep) : "v"(voff), "s"(desc), "s"(soff), "s"(ldsaddr) : "memory");
}
#define WAITV(n) asm volatile("s_waitcnt vmcnt(" #n ")" ::: "memory")
#define WAITL() asm volatile("s_waitcnt lgkmcnt(0)" ::: "memory")
#define BARRIER() asm volatile("s_waitcnt lgkmcnt(0)\n\ts_barrier" ::: "memory")
#define BARRIER_NOWAIT() asm volatile("s_barrier" ::: "memory")

DI s16x8 lds128(lds_cp p) { return *(const __attribute__((address_space(3))) s16x8*)p; }
DI f32x4 ldsf4(lds_cp p) { return *(const __attribute__((address_space(3))) f32x4*)p; }
DI void stsf4(lds_p p, f32x4 v) { *(__attribute__((address_space(3))) f32x4*)p = v; }
DI s16x4 ldstr(lds_cp p) {
    return __builtin_bit_cast(s16x4, __builtin_amdgcn_ds_read_tr16_b64_v4i16((__attribute__((address_space(3))) s16x4*)p));
}
DI s16x8 cat8(s16x4 a, s16x4 b) { return __builtin_shufflevector(a, b, 0, 1, 2, 3, 4, 5, 6, 7); }
DI u32x4 ld128(rsrc_t r, unsigned voff, unsigned soff) { return __builtin_amdgcn_raw_buffer_load_b128(r, voff, soff, SC1); }
DI u32x2 ld64(rsrc_t r, unsigned voff, unsigned soff) { return __builtin_amdgcn_raw_buffer_load_b64(r, voff, soff, SC1); }
DI unsigned ld32(rsrc_t r, unsigned voff, unsigned soff) { return __builtin_amdgcn_raw_buffer_load_b32(r, voff, soff, SC1); }
DI void st128(rsrc_t r, u32x4 v, unsigned voff, unsigned soff) { __builtin_amdgcn_raw_buffer_store_b128(v, r, voff + soff, 0, SC1); }
DI void st64(rsrc_t r, u32x2 v, unsigned voff, unsigned soff) { __builtin_amdgcn_raw_buffer_store_b64(v, r, voff, soff, SC1); }
DI void st32(rsrc_t r, unsigned v, unsigned voff, unsigned soff) { __builtin_amdgcn_raw_buffer_store_b32(v, r, voff, soff, SC1); }

constexpr unsigned SPIN_CAP = 200000;
DI void publish(rsrc_t w, unsigned flag_off, unsigned epoch, int lane) {
    WAITV(0);
    if (lane == 0) st32(w, epoch, flag_off, 0);
}
template <int SL>
DI void wait_flags4(rsrc_t w, unsigned o0, unsigned o1, unsigned o2, unsigned o3, unsigned epoch) {
    NOUNROLL for (unsigned spins = 0; spins < SPIN_CAP; ++spins) {
        asm volatile("" ::: "memory");
        const unsigned a = ld32(w, o0, 0), b = ld32(w, o1, 0), c = ld32(w, o2, 0), d = ld32(w, o3, 0);
        if (__all(a == epoch && b == epoch && c == epoch && d == epoch)) break;
        __builtin_amdgcn_s_sleep(SL);
    }
    asm volatile("" ::: "memory");
}
template <int SL>
DI void wait_flags1(rsrc_t w, unsigned o0, unsigned epoch) {
    NOUNROLL for (unsigned spins = 0; spins < SPIN_CAP; ++spins) {
        asm volatile("" ::: "memory");
        const unsigned a = ld32(w, o0, 0);
        if (__all(a == epoch)) break;
        __builtin_amdgcn_s_sleep(SL);
    }
    asm volatile("" ::: "memory");
}
DI void lds_signal(lds_p p, unsigned val = 1u) {
    WAITL();
    *(volatile __attribute__((address_space(3))) unsigned*)p = val;
}
DI void lds_post(lds_p p, unsigned val) { *(volatile __attribute__((address_space(3))) unsigned*)p = val; }
DI unsigned lds_peek(lds_cp p) { return *(const volatile __attribute__((address_space(3))) unsigned*)p; }
template <int SL = 1>
DI void lds_wait(lds_cp p, unsigned val = 1u) {
    NOUNROLL for (unsigned spins = 0; spins < 4 * SPIN_CAP; ++spins) {
        const unsigned v = *(const volatile __attribute__((address_space(3))) unsigned*)p;
        if (__all(v >= val)) break;
        __builtin_amdgcn_s_sleep(SL);
    }
    asm volatile("" ::: "memory");
}

__global__ __launch_bounds__(512, 2) void fused(const char* __restrict__ x, const char* __restrict__ wqkv,
                                                 const char* __restrict__ wo, const char* __restrict__ cre,
                                                 const char* __restrict__ cim, const char* __restrict__ wr,
                                                 const char* __restrict__ wi, const char* __restrict__ gw,
                                                 char* ws, char* __restrict__ out) {
    extern __shared__ __attribute__((aligned(16))) char smem[];
    unsigned long long pc; asm volatile("s_getpc_b64 %0" : "=s"(pc));
    const int tid = threadIdx.x;
    const int wid = __builtin_amdgcn_readfirstlane(tid >> 6);
    const int gwv = wid & 3;
    const int xcd = blockIdx.x & 7, idx = blockIdx.x >> 3;
    const unsigned lds0 = (unsigned)(uintptr_t)smem;
    const lds_cp L = (lds_cp)smem;
    const lds_p LW = (lds_p)smem;
    const u32x4 dx = mkdesc(x, 2048 * 1280);
    const int mt = (xcd & 1) * 8 + (idx & 7), nt = (xcd >> 1) * 4 + (idx >> 3);
    const u32x4 dw = mkdesc(wqkv, 640 * 3840);
    const rsrc_t W = __builtin_amdgcn_make_buffer_rsrc(ws, 0, WS_END, 0x00020000);
    const unsigned oneoff = WS_ONE + (blockIdx.x * 8 + wid) * 128;
    unsigned epoch;
    {
    const int lane = tid & 63;
    unsigned roff[2];
    UNROLL for (int i = 0; i < 2; ++i) roff[i] = (lane >> 3) * 1280 + (((lane & 7) ^ ((4 * i + (lane >> 4)) & 7)) << 4);
    unsigned woff[2];
    UNROLL for (int i = 0; i < 2; ++i) woff[i] = nt == 15 ? OOB : (lane >> 3) * 3840 + (((lane & 7) ^ (((lane >> 4) & 1) << 2) ^ (i << 1)) << 4);
#define P1_DX(st, p, par) dma16(roff[par], dx, (mt * 128 + (p) * 8) * 1280 + (st) * 128, lds0 + ((st) & 3) * P1_STAGE + (p) * 1024)
#define P1_DW(st, p, par) dma16(woff[par], dw, ((st) * 64 + ((p) & 7) * 8) * 3840 + nt * 256 + ((p) >> 3) * 128, lds0 + ((st) & 3) * P1_STAGE + 16384 + (p) * 1024)
    P1_DX(0, 2 * wid, 0); P1_DW(0, 2 * wid, 0); P1_DX(0, 2 * wid + 1, 1); P1_DW(0, 2 * wid + 1, 1);
    STAMP(0);
    const int hh = lane >> 5;
    const int l15 = lane & 15, kg = lane >> 4;
    const unsigned fl4 = (lane & 3) * 4;
    const u32x4 dh = mkdesc(nt < 8 ? wr : wi, 640 * 1280);
#define GET_EPOCH() do { epoch = ld32(W, WS_EP, 0) + 1u; if (lane < 4) st32(W, 0x3F803F80u, oneoff + lane * 4, 0); } while (0)
#define WH_ADDR(q) ((q) < 32 ? 2 * P1_STAGE + (q) * 1024 : (q) < 48 ? 3 * P1_STAGE + 16384 + ((q) - 32) * 1024 : \
                    (q) < 68 ? LDS_X9 + ((q) - 48) * 1024 : (q) < 84 ? 16384 + ((q) - 68) * 1024 : P1_STAGE + 16384 + ((q) - 84) * 1024)

    if (wid >= 4) {
#define P1_ISSUE(st) do { \
            const unsigned x_ = (mt * 128 + gwv * 32) * 1280 + (st) * 128, w_ = ((st) * 64 + (gwv & 1) * 32) * 3840 + nt * 256 + (gwv >> 1) * 128; \
            dma16x4(roff[0], roff[1], dx, x_, x_ + 9216, x_ + 18432, x_ + 27648, lds0 + ((st) & 3) * P1_STAGE + gwv * 4096); \
            dma16x4(woff[0], woff[1], dw, w_, w_ + 29696, w_ + 59392, w_ + 89088, lds0 + ((st) & 3) * P1_STAGE + 16384 + gwv * 4096); } while (0)
#define P1_DH(q, par) do { const int q_ = (q), c_ = q_ / 10, p_ = q_ - c_ * 10; \
            dma16(roff[par], dh, ((nt & 7) * 80 + p_ * 8) * 1280 + c_ * 128, lds0 + WH_ADDR(q_)); } while (0)
        P1_ISSUE(1); P1_ISSUE(2); P1_ISSUE(3);
        WAITV(24); BARRIER_NOWAIT();
        NOUNROLL for (int k = 1; k < 7; ++k) { WAITV(16); BARRIER_NOWAIT(); P1_ISSUE(k + 3); }
        WAITV(16); BARRIER_NOWAIT();
        UNROLL for (int i = 0; i < 8; ++i) P1_DH(gwv * 8 + i, i & 1);
        WAITV(16); BARRIER_NOWAIT();
        UNROLL for (int i = 0; i < 8; ++i) P1_DH(32 + gwv * 8 + i, i & 1);
        WAITV(16); BARRIER_NOWAIT();
        UNROLL for (int i = 0; i < 6; ++i) { const int q = 64 + gwv * 6 + i; P1_DH(q < 84 ? q : 82 + (i & 1), i & 1); }
        GET_EPOCH();
        __builtin_amdgcn_s_setprio(3);
        WAITV(0); BARRIER_NOWAIT();
        UNROLL for (int i = 0; i < 4; ++i) P1_DH(84 + gwv * 4 + i, i & 1);
        STAMP2(16);
        STAMP(1);
    } else {
        const unsigned sink = lds0 + LDS_SINK;
        const int pi = xcd * 4 + wid, pst = pi / 5, pu = pi - pst * 5;
        if (PFX) {
            const unsigned b = blockIdx.x;
            if (wid == 0) { dma4(lane * 128, dx, b * 10240, sink); dma4(lane < 16 ? lane * 128 : OOB, dx, b * 10240 + 8192, sink); }
            if (wid == 1) { dma4(lane * 128, dw, b * 9600, sink); dma4(lane < 11 ? lane * 128 : OOB, dw, b * 9600 + 8192, sink); }
            if (wid == 2) { dma4(FWARM && lane < 16 ? (b * 16 + lane) * FS : OOB, mkdesc(ws, WS_END), WS_F1, sink); dma4(OOB, dx, 0, sink); }
        } else if (wid < 3) { dma4(OOB, dx, 0, sink); dma4(OOB, dx, 0, sink); }
        if (wid == 3) {
            dma4(lane < 12 ? lane * 128 : OOB, mkdesc((const void*)(pc & ~127ull), CODE_LINES * 128), idx * 1536, sink);
            dma16(nt == 15 ? lane * 16 : OOB, mkdesc(gw, 1280), 0, lds0 + LDS_GW);
        }
        if (tid < 64) *(__attribute__((address_space(3))) unsigned*)(LW + LDS_FL + tid * 4) = 0u;
        const int wm = wid & 1, wn = wid >> 1;
        unsigned ra[2];
        UNROLL for (int ks = 0; ks < 2; ++ks) ra[ks] = l15 * 128 + (((4 * ks + kg) ^ ((l15 >> 1) & 7)) << 4);
        const unsigned xk = wm * 8192 + wn * 4096, xo = wm * 8192 + (wn ^ 1) * 4096;
        s16x8 xs[10][2][2];
#define WT_LOAD() dma16((lane >> 1) * 1280 + (lane & 1) * 16, mkdesc(wo, 640 * 1280), \
                  pi < 25 ? ((idx >> 3) * 160 + pst * 32) * 1280 + (idx & 7) * 160 + pu * 32 : OOB, lds0 + LDS_WT + wid * 1024)
#define WT_STORE() do { const lds_cp tp_ = L + LDS_WT + wid * 1024 + (8 * kg + (l15 >> 2)) * 32 + (lane & 3) * 8; \
            st128(W, __builtin_bit_cast(u32x4, cat8(ldstr(tp_), ldstr(tp_ + 128))), \
                  pi < 25 ? WS_WOT + (((idx & 7) * 4 + (idx >> 3)) * 25 + pi) * 1024 + lane * 16 : OOB, 0); } while (0)
        if (nt == 15) {
            const int m0 = mt * 128 + wm * 64 + wn * 32;
            if (wid == 3) dma16(lane * 16, mkdesc(gw, 1280), 1024, lds0 + LDS_GW + 1024);
            WT_LOAD();
            GET_EPOCH();
            {
                const unsigned o0 = (mt * 4 + wid) * 12800, v1 = lane < 36 ? lane * 128 : OOB;
                dma4(lane * 128, mkdesc(wr, 640 * 1280), o0, sink); dma4(v1, mkdesc(wr, 640 * 1280), o0 + 8192, sink);
                dma4(lane * 128, mkdesc(wi, 640 * 1280), o0, sink); dma4(v1, mkdesc(wi, 640 * 1280), o0 + 8192, sink);
            }
            WAITV(4);
            float sum[2] = {0.f, 0.f};
            UNROLL for (int k = 0; k < 10; ++k) {
                BARRIER();
                UNROLL for (int ks = 0; ks < 2; ++ks) {
                    const u32x4 gv = __builtin_bit_cast(u32x4, lds128(L + LDS_GW + k * 128 + (4 * ks + kg) * 16));
                    UNROLL for (int h = 0; h < 2; ++h) {
                        xs[k][ks][h] = lds128(L + (k & 3) * P1_STAGE + xk + h * 2048 + ra[ks]);
                        const u32x4 xv = __builtin_bit_cast(u32x4, xs[k][ks][h]);
                        UNROLL for (int e = 0; e < 4; ++e)
                            { const unsigned xe = xv[e], ge = gv[e]; sum[h] = __builtin_amdgcn_fdot2_f32_bf16(__builtin_bit_cast(bf16x2_t, xe), __builtin_bit_cast(bf16x2_t, ge), sum[h], false); }
                    }
                }
                SBAR();
            }
            UNROLL for (int h = 0; h < 2; ++h) { sum[h] += __shfl_xor(sum[h], 16); sum[h] += __shfl_xor(sum[h], 32); }
            const float sm = kg & 1 ? sum[1] : sum[0];
            if (lane < 32) st32(W, __float_as_uint(1.0f / (1.0f + __expf(-sm))), WS_G + (m0 + lane) * 4, 0);
        } else {
            const int q = l15 >> 2;
            unsigned trb[4];
            UNROLL for (int j = 0; j < 4; ++j)
                trb[j] = 16384 + wn * 8192 + (8 * kg + q) * 128 + ((j ^ (((q >> 1) & 1) << 1) ^ (kg & 1)) << 5) + 8 * (lane & 3);
            f32x4 acc[4][4];
            UNROLL for (int i = 0; i < 4; ++i) UNROLL for (int j = 0; j < 4; ++j) acc[i][j] = f32x4{};
            s16x8 xt[2][2]; s16x4 wv[4][2];
            WT_LOAD();
            GET_EPOCH();
            WAITV(3);
#define P1_RW(z, j) do { const lds_cp p_ = L + (((z) >> 1) & 3) * P1_STAGE + trb[j] + ((z) & 1) * 4096; wv[j][0] = ldstr(p_); wv[j][1] = ldstr(p_ + 512); } while (0)
#define P1_RK(z, h) xs[(z) >> 1][(z) & 1][h] = lds128(L + (((z) >> 1) & 3) * P1_STAGE + xk + (h) * 2048 + ra[(z) & 1])
#define P1_RO(z, h) xt[(z) & 1][h] = lds128(L + (((z) >> 1) & 3) * P1_STAGE + xo + (h) * 2048 + ra[(z) & 1])
#define P1_XF(z, i) ((i) < 2 ? xs[(z) >> 1][(z) & 1][(i) & 1] : xt[(z) & 1][(i) & 1])
#define P1_LOOP(MF) \
            BARRIER(); \
            STAMP2(11); \
            P1_RK(0, 0); P1_RW(0, 0); P1_RK(0, 1); P1_RO(0, 0); P1_RO(0, 1); P1_RW(0, 1); P1_RW(0, 2); P1_RW(0, 3); SBAR(); \
            UNROLL for (int z = 0; z < 20; ++z) { \
                UNROLL for (int j = 0; j < 4; ++j) { \
                    MF(z, 0, j); SBAR(); \
                    if (z < 19 && j == 1) { P1_RK(z + 1, 0); SBAR(); } \
                    if (z < 19 && j == 2) { P1_RO(z + 1, 0); SBAR(); } \
                    MF(z, 1, j); SBAR(); \
                    if (z < 19 && j == 1) { P1_RK(z + 1, 1); SBAR(); } \
                    if (z < 19 && j == 2) { P1_RO(z + 1, 1); SBAR(); } \
                    MF(z, 2, j); SBAR(); \
                    if (j == 0 && (z & 1) && z < 19) { BARRIER(); } \
                    MF(z, 3, j); SBAR(); \
                    if (z < 19) { P1_RW(z + 1, j); SBAR(); } \
                } \
            }
#define P1_MFQ(z, i, j) acc[i][j] = MFMA16(cat8(wv[j][0], wv[j][1]), P1_XF(z, i), acc[i][j])
#define P1_MFV(z, i, j) acc[i][j] = MFMA16(P1_XF(z, i), cat8(wv[j][0], wv[j][1]), acc[i][j])
            if (nt < 10) {
                P1_LOOP(P1_MFQ)
                STAMP(1);
                const float sc = nt < 5 ? 0.32259889079f : 1.0f;
                UNROLL for (int i = 0; i < 4; i += 2)
                    UNROLL for (int j = 0; j < 4; ++j) {
                        unsigned a0 = pack2(acc[i][j][0] * sc, acc[i][j][1] * sc), a1 = pack2(acc[i][j][2] * sc, acc[i][j][3] * sc);
                        unsigned b0 = pack2(acc[i + 1][j][0] * sc, acc[i + 1][j][1] * sc), b1 = pack2(acc[i + 1][j][2] * sc, acc[i + 1][j][3] * sc);
                        auto s0 = __builtin_amdgcn_permlane16_swap(a0, b0, false, false);
                        auto s1 = __builtin_amdgcn_permlane16_swap(a1, b1, false, false);
                        st128(W, u32x4{s0[0], s1[0], s0[1], s1[1]}, WS_QK + (nt * 16 + wn * 8 + (kg >> 1)) * CH + (mt * 128 + wm * 64 + ((i >> 1) ^ wn) * 32 + (kg & 1) * 16 + l15) * 16, j * 2 * CH);
                    }
            } else {
                P1_LOOP(P1_MFV)
                STAMP(1);
                UNROLL for (int i = 0; i < 4; ++i)
                    UNROLL for (int j = 0; j < 4; j += 2) {
                        unsigned a0 = pack2(acc[i][j][0], acc[i][j][1]), a1 = pack2(acc[i][j][2], acc[i][j][3]);
                        unsigned b0 = pack2(acc[i][j + 1][0], acc[i][j + 1][1]), b1 = pack2(acc[i][j + 1][2], acc[i][j + 1][3]);
                        auto s0 = __builtin_amdgcn_permlane32_swap(a0, b0, false, false);
                        auto s1 = __builtin_amdgcn_permlane32_swap(a1, b1, false, false);
                        st128(W, u32x4{s0[0], s1[0], s0[1], s1[1]}, WS_VT + ((mt * 8 + wm * 4 + ((i >> 1) ^ wn) * 2 + (i & 1)) * 2 + (kg & 1)) * VROW + ((nt - 10) * 128 + wn * 64 + hh * 16 + l15) * 16, j * 256);
                    }
            }
        }
        BARRIER();
        __builtin_amdgcn_s_setprio(2);
        STAMP(7);
        f32x4 hc[2][5];
        UNROLL for (int h = 0; h < 2; ++h) UNROLL for (int u = 0; u < 5; ++u) hc[h][u] = f32x4{};
        if (PUBZ == 0 && nt != 15) publish(W, WS_F1 + ((nt * 2 + wn) * 32 + mt * 2 + wm) * FS, epoch, lane);
        UNROLL for (int z = 14; z < 20; ++z) UNROLL for (int h = 0; h < 2; ++h) P1_RK(z, h);
        s16x8 wb[2][5];
        UNROLL for (int z = 0; z < 21; ++z) {
            if (PUBZ && z == PUBZ && nt != 15) publish(W, WS_F1 + ((nt * 2 + wn) * 32 + mt * 2 + wm) * FS, epoch, lane);
            if (z == 16) { STAMP2(17); lds_wait(L + FL_FULL + fl4, 1); STAMP2(18); }
            UNROLL for (int u = 0; u < 5; ++u) {
                if (z < 20) { wb[z & 1][u] = lds128(L + WH_ADDR((z >> 1) * 10 + 2 * u) + ra[z & 1]); SBAR(); }
                if (z > 0) {
                    hc[0][u] = MFMA16(xs[(z - 1) >> 1][(z - 1) & 1][0], wb[(z - 1) & 1][u], hc[0][u]);
                    hc[1][u] = MFMA16(xs[(z - 1) >> 1][(z - 1) & 1][1], wb[(z - 1) & 1][u], hc[1][u]); SBAR();
                }
            }
        }
        STAMP2(19);
        WAITV(0);
        WT_STORE();
        lds_signal(LW + FL_FREE + wid * 4, 1);
        const float sg = nt < 8 ? 1.0f : -1.0f;
        const unsigned ab = WS_AB + (((nt & 7) >> 1) * 512 + (nt >> 3) * 256 + mt * 16 + wm * 8 + wn * 4 + (kg & 1) * 2 + (kg >> 1)) * 2560 + ((nt & 1) * 80 + l15) * 16;
        UNROLL for (int u = 0; u < 5; ++u) {
            unsigned a0 = pack2(hc[0][u][0] * sg, hc[0][u][1] * sg), a1 = pack2(hc[0][u][2] * sg, hc[0][u][3] * sg);
            unsigned b0 = pack2(hc[1][u][0] * sg, hc[1][u][1] * sg), b1 = pack2(hc[1][u][2] * sg, hc[1][u][3] * sg);
            auto s0 = __builtin_amdgcn_permlane16_swap(a0, b0, false, false);
            auto s1 = __builtin_amdgcn_permlane16_swap(a1, b1, false, false);
            st128(W, u32x4{s0[0], s1[0], s0[1], s1[1]}, ab, u * 256);
        }
        publish(W, WS_F2 + (nt * 64 + mt * 4 + wid) * FS, epoch, lane);
        if (lane == 0) st32(W, epoch, WS_ACK + (blockIdx.x * 8 + wid) * 4, 0);
    }
    }
    STAMP(2);
    const int lane = __builtin_amdgcn_mbcnt_hi(~0u, __builtin_amdgcn_mbcnt_lo(~0u, 0u));
    const int r = lane & 31, hh = lane >> 5;
    const int l15 = lane & 15, kg = lane >> 4;
    const unsigned fl4 = (lane & 3) * 4;

    const int ksp = xcd >> 1, nt4 = idx >> 3;
    const int tt = mt * 2 + (ksp & 1), ct = nt4 * 2 + (ksp >> 1);
    const u32x4 dc = mkdesc(ksp < 2 ? cre : cim, 2048 * 4096), dws = mkdesc(ws, WS_END);
    const unsigned cs0 = mt * 128 * 4096 + (ksp & 1) * 2048;
    const unsigned as0 = WS_AB + (nt4 * 512 + ksp * 128) * 2560;

    if (wid >= 4) {
        const int head = xcd * 4 + (idx >> 3), qt = idx & 7;
        const int qh = wid & 1, kh = (wid >> 1) & 1;
        const int col0 = head * 20, odd = head & 1;
        const int cA = (col0 >> 3) + odd, cH = odd ? (col0 >> 3) : (col0 >> 3) + 2;
        const int q0 = qt * 256 + qh * 128;
        unsigned k1off = WS_QK + (80 + cA + hh) * CH + r * 16;
        unsigned k2off = hh ? OOB : WS_QK + (80 + cH) * CH + r * 16 + odd * 8;
        unsigned q2off = hh ? OOB : k2off - 80 * CH;
        unsigned k2or = hh ? 0x3F80u : 0u;
        const int dv = odd ? (r < 16 ? r + 4 : r - 16) : r;
        const unsigned voff = r < 20 ? WS_VT + hh * VROW + (col0 + dv) * 16 : (r == 20 ? oneoff : OOB);
        unsigned vstep = r < 20 ? 4 * VROW : 0;
        unsigned va = voff + kh * 32 * vstep, vb2 = va + (vstep >> 1);
        unsigned cpf = CPF == 1 ? (lane < 8 ? (lane >> 1) * 4096 + (lane & 1) * 128 : OOB) : CPF == 2 ? (lane < 8 ? lane * 128 : OOB) : (lane < 8 ? (lane >> 2) * 4096 + (lane & 3) * 128 : OOB);
        constexpr int NB = 4;
        u32x4 k1[NB], v0[NB], v1[NB]; u32x2 k2[NB];
        f32x16 o[4], s[2];
        u32x4 pa = u32x4{}, pd = u32x4{};
        f32x8 e8 = f32x8{};
        UNROLL for (int i = 0; i < 4; ++i) { o[i] = f32x16{}; PIN(o[i]); }
        v0[NB - 1] = u32x4{}; v1[NB - 1] = u32x4{};
        PIN(v0[NB - 1]); PIN(v1[NB - 1]); PIN(pa); PIN(pd); PIN(e8);
        PIN(k1off); PIN(k2off); PIN(q2off); PIN(k2or); PIN(va); PIN(vb2); PIN(vstep); PIN(cpf);
        WAITV(0);
        lds_post(LW + FL_FULL + gwv * 4, 1);
        const int nsa = (lane & 4) ? (col0 + 19) >> 6 : col0 >> 6;
        unsigned fkv = WS_F1 + ((10 + ((lane >> 3) & 1) * 10 + nsa) * 32 + kh * 16) * FS;
        STAMP2(23);
        __builtin_amdgcn_s_sleep(PSL);
        wait_flags1<SL1>(W, (lane & 16) ? WS_F1 + (nsa * 32 + (q0 >> 6) + (lane & 1)) * FS : fkv + (lane & 3) * FS, epoch);
        fkv += (lane & 1) * FS;
        STAMP(3);
        u32x4 q1[4]; u32x2 q2[4];
        UNROLL for (int i = 0; i < 4; ++i) {
            const int qr = q0 + ((i + 2 + 2 * kh) & 3) * 32;
            q2[i] = ld64(W, q2off, qr * 16);
            q1[i] = ld128(W, k1off - 80 * CH, qr * 16);
        }
        const int t0 = kh * 32;
        unsigned fnx = epoch;
        SBAR();
        UNROLL for (int b = 0; b < NB; ++b) {
            k2[b] = ld64(W, k2off, (t0 + b) * 512); k1[b] = ld128(W, k1off, (t0 + b) * 512); SBAR();
            if (b < NB - 1) { v0[b] = ld128(W, va, 0); v1[b] = ld128(W, vb2, 0); va += vstep; vb2 += vstep; SBAR(); }
        }
        const f32x16 zero16 = f32x16{};
        {
            f32x16 t[4];
            UNROLL for (int i = 0; i < 4; ++i) { t[i] = MFMA32K8(as4(k2[0]), as4(q2[i]), zero16); t[i] = MFMA32(as8(k1[0]), as8(q1[i]), t[i]); SBAR(); }
            k2[0][0] |= k2or;
            UNROLL for (int i = 0; i < 4; ++i) {
                float m = t[i][0];
                UNROLL for (int e = 1; e < 16; ++e) m = fmaxf(m, t[i][e]);
                auto mm = __builtin_amdgcn_permlane32_swap(__float_as_uint(m), __float_as_uint(m), false, false);
                m = fmaxf(__uint_as_float(mm[0]), __uint_as_float(mm[1]));
                q2[i][0] = hh ? pack2(-m, 0.f) & 0xFFFFu : q2[i][0];
                if (i == 0) {
                    s[0] = MFMA32K8(as4(k2[0]), as4(q2[0]), zero16);
                    s[0] = MFMA32(as8(k1[0]), as8(q1[0]), s[0]);
                }
            }
        }
        dma4(lane < 8 ? (lane >> 1) * 20480 + (lane & 1) * 128 : OOB, dws, WS_O + ((q0 + kh * 64) >> 4) * 20480 + cH * 256, lds0 + LDS_SINK);
        st32(W, epoch, lane == 0 ? WS_ACK + (blockIdx.x * 8 + wid) * 4 : OOB, 0);
        __builtin_amdgcn_s_setprio(1);
        STAMP(4);
        NOUNROLL for (int tb = 0; tb < 32; tb += NB) {
            if (__builtin_expect(!__all(fnx == epoch), 0)) wait_flags1<SL1>(W, fkv + (tb < 28 ? tb + 4 : 0) * (FS / 2), epoch);
            if (tb == 16) lds_wait(L + FL_FREE + fl4, 1);
            const unsigned fso = (tb < 24 ? tb + 8 : 0) * (FS / 2);
            SBAR();
            UNROLL for (int b = 0; b < NB; ++b) {
                const int bn = (b + 1) % NB, bp = (b + NB - 1) % NB;
                const unsigned kso = tb + b + NB < 32 ? (t0 + tb + b + NB) * 512 : OOB / 2;
                const unsigned vso = tb + b + NB - 1 < 32 ? 0 : OOB / 2;
                const unsigned cso = tb >= 16 ? OOB / 2 : CPF == 1 ? cs0 + (nt4 * 32 + gwv * 8 + (b & 1) * 4) * 4096 + ((tb + b) >> 1) * 256 :
                                     CPF == 2 ? cs0 + (nt4 * 32 + gwv * 8 + ((tb + b) & 7)) * 4096 + (tb >> 3) * 1024 :
                                                cs0 + (nt4 * 32 + gwv * 8 + b * 2) * 4096 + (tb >> 2) * 512;
                UNROLL for (int qi = 0; qi < 4; ++qi) {
                    const int c = qi & 1, n = c ^ 1, qn = (qi + 1) & 3, qp = (qi + 3) & 3;
                    const int kb = qi == 3 ? bn : b, vb = qi == 0 ? bp : b;
                    f32x16& SC = s[c]; f32x16& SN = s[n];
                    SN = MFMA32K8(as4(k2[kb]), as4(q2[qn]), zero16); SBAR();
                    SC[0] = EX2(SC[0]); SC[1] = EX2(SC[1]); SC[2] = EX2(SC[2]); SC[3] = EX2(SC[3]); SBAR();
                    pd[0] = pack2(e8[0], e8[1]); pd[1] = pack2(e8[2], e8[3]); PIN(SC); PIN(pd); SBAR();
                    SN = MFMA32(as8(k1[kb]), as8(q1[qn]), SN); SBAR();
                    if (qi == 2) { k2[b] = ld64(W, k2off, kso); k1[b] = ld128(W, k1off, kso); SBAR(); }
                    if (qi == 3 && b == 0) { fnx = ld32(W, fkv, fso); SBAR(); }
                    if (CPF && qi == 1) { dma4(cpf, dc, cso, lds0 + LDS_SINK); SBAR(); }
                    SC[4] = EX2(SC[4]); SC[5] = EX2(SC[5]); SC[6] = EX2(SC[6]); SC[7] = EX2(SC[7]); SBAR();
                    pd[2] = pack2(e8[4], e8[5]); pd[3] = pack2(e8[6], e8[7]); PIN(SC); PIN(pd); SBAR();
                    o[qp] = MFMA32(as8(v0[vb]), as8(pa), o[qp]); SBAR();
                    e8[0] = EX2(SC[8]); e8[1] = EX2(SC[9]); e8[2] = EX2(SC[10]); e8[3] = EX2(SC[11]); SBAR();
                    pa[0] = pack2(SC[0], SC[1]); pa[1] = pack2(SC[2], SC[3]);
                    if (qi == 2) { k2[bn][0] |= k2or; PIN(k2[bn]); }
                    if (qi == 1) { va += vstep; vb2 += vstep; PIN(va); PIN(vb2); }
                    PIN(e8); PIN(pa); SBAR();
                    o[qp] = MFMA32(as8(v1[vb]), as8(pd), o[qp]); SBAR();
                    if (qi == 0) { v0[bp] = ld128(W, va, vso); v1[bp] = ld128(W, vb2, vso); SBAR(); }
                    e8[4] = EX2(SC[12]); e8[5] = EX2(SC[13]); e8[6] = EX2(SC[14]); e8[7] = EX2(SC[15]); SBAR();
                    pa[2] = pack2(SC[4], SC[5]); pa[3] = pack2(SC[6], SC[7]); PIN(e8); PIN(pa); SBAR();
                }
            }
        }
        __builtin_amdgcn_s_setprio(3);
        UNROLL for (int e = 0; e < 4; ++e) pd[e] = pack2(e8[2 * e], e8[2 * e + 1]);
        o[3] = MFMA32(as8(v0[NB - 1]), as8(pa), o[3]);
        o[3] = MFMA32(as8(v1[NB - 1]), as8(pd), o[3]);
        SBAR();
        STAMP(5);
        const int f0 = q0 + kh * 64;
        UNROLL for (int j = 0; j < 2; ++j) {
            const lds_p ex = LW + LDS_EX + (gwv * 2 + j) * EXB;
            *(__attribute__((address_space(3))) u32x4*)(ex + lane * 16) =
                u32x4{pack2(o[j][0], o[j][1]), pack2(o[j][2], o[j][3]), pack2(o[j][4], o[j][5]), pack2(o[j][6], o[j][7])};
            *(__attribute__((address_space(3))) u32x2*)(ex + 1024 + lane * 8) =
                hh ? u32x2{__float_as_uint(o[j][8]), q2[j][0] << 16} : u32x2{pack2(o[j][8], o[j][9]), pack2(o[j][10], o[j][11])};
        }
        lds_signal(LW + FL_EX + gwv * 4);
        float n1[2], l1[2];
        UNROLL for (int j = 0; j < 2; ++j) {
            auto nn = __builtin_amdgcn_permlane32_swap(q2[2 + j][0] << 16, q2[2 + j][0] << 16, false, false);
            auto ll = __builtin_amdgcn_permlane32_swap(__float_as_uint(o[2 + j][8]), __float_as_uint(o[2 + j][8]), false, false);
            n1[j] = __uint_as_float(nn[1]); l1[j] = __uint_as_float(ll[1]);
        }
        const unsigned orow = WS_O + ((f0 >> 4) + (r >> 4)) * 20480 + (r & 15) * 16;
        const unsigned oa = orow + (cA + hh) * 256, ob = hh ? OOB : orow + cH * 256 + odd * 8;
        lds_wait(L + FL_EX + (gwv ^ 2) * 4);
        STAMP2(22);
        bool ok = true;
        u32x4 ra[2]; u32x2 rb[2];
        UNROLL for (int j = 0; j < 2; ++j) {
            const lds_cp rx = L + LDS_EX + ((gwv ^ 2) * 2 + j) * EXB;
            const u32x4 a = *(const __attribute__((address_space(3))) u32x4*)(rx + lane * 16);
            const u32x2 c = *(const __attribute__((address_space(3))) u32x2*)(rx + 1024 + r * 8);
            const u32x2 d = *(const __attribute__((address_space(3))) u32x2*)(rx + 1280 + r * 8);
            const float n2 = __uint_as_float(d[1]), l2 = __uint_as_float(d[0]), nm = fminf(n1[j], n2);
            float w1 = EX2(nm - n1[j]), w2 = EX2(nm - n2);
            const float l = l1[j] * w1 + l2 * w2, rl = __builtin_amdgcn_rcpf(l);
            w1 *= rl; w2 *= rl;
            float f[12];
            UNROLL for (int e = 0; e < 4; ++e) { f[2 * e] = o[2 + j][2 * e] * w1 + bflo(a[e]) * w2; f[2 * e + 1] = o[2 + j][2 * e + 1] * w1 + bfhi(a[e]) * w2; }
            UNROLL for (int e = 0; e < 2; ++e) { f[8 + 2 * e] = o[2 + j][8 + 2 * e] * w1 + bflo(c[e]) * w2; f[9 + 2 * e] = o[2 + j][9 + 2 * e] * w1 + bfhi(c[e]) * w2; }
            float sa = 0.f;
            UNROLL for (int e = 0; e < 12; ++e) sa += fabsf(f[e]);
            ok = ok && l1[j] < 1e30f && l2 < 1e30f && l > 1e-18f && sa < 3e38f;
            auto s0 = __builtin_amdgcn_permlane32_swap(pack2(f[0], f[1]), pack2(f[4], f[5]), false, false);
            auto s1 = __builtin_amdgcn_permlane32_swap(pack2(f[2], f[3]), pack2(f[6], f[7]), false, false);
            ra[j] = u32x4{s0[0], s1[0], s0[1], s1[1]};
            rb[j] = u32x2{pack2(f[8], f[9]), pack2(f[10], f[11])};
        }
        if (__builtin_expect(__all(ok), 1)) {
            UNROLL for (int j = 0; j < 2; ++j) { st128(W, ra[j], oa + j * 40960, 0); st64(W, rb[j], ob + j * 40960, 0); }
        } else {
            wait_flags1<SL1>(W, WS_F1 + ((10 + hh * 10 + (col0 >> 6)) * 32 + r) * FS, epoch);
            wait_flags1<SL1>(W, WS_F1 + ((10 + hh * 10 + ((col0 + 19) >> 6)) * 32 + r) * FS, epoch);
            NOUNROLL for (int j = 0; j < 2; ++j) {
                const u32x2 z2 = ld64(W, q2off, (f0 + j * 32) * 16);
                const u32x4 z1 = ld128(W, k1off - 80 * CH, (f0 + j * 32) * 16);
                float m = -3e38f;
                f32x16 oo = f32x16{};
                NOUNROLL for (int t = 0; t < 64; ++t) {
                    const u32x4 a1 = ld128(W, k1off, t * 512); const u32x2 a2 = ld64(W, k2off, t * 512);
                    const u32x4 w0 = ld128(W, voff + t * vstep, 0), w1 = ld128(W, voff + t * vstep + (vstep >> 1), 0);
                    f32x16 z = MFMA32K8(as4(a2), as4(z2), zero16);
                    z = MFMA32(as8(a1), as8(z1), z);
                    float mx = z[0];
                    UNROLL for (int e = 1; e < 16; ++e) mx = fmaxf(mx, z[e]);
                    auto mm = __builtin_amdgcn_permlane32_swap(__float_as_uint(mx), __float_as_uint(mx), false, false);
                    const float mn = fmaxf(m, fmaxf(__uint_as_float(mm[0]), __uint_as_float(mm[1])));
                    const float al = EX2(m - mn);
                    m = mn;
                    UNROLL for (int e = 0; e < 16; ++e) oo[e] *= al;
                    u32x4 p0, p1;
                    UNROLL for (int e = 0; e < 4; ++e) { p0[e] = pack2(EX2(z[2 * e] - m), EX2(z[2 * e + 1] - m)); p1[e] = pack2(EX2(z[8 + 2 * e] - m), EX2(z[9 + 2 * e] - m)); }
                    oo = MFMA32(as8(w0), as8(p0), oo);
                    oo = MFMA32(as8(w1), as8(p1), oo);
                }
                auto l2 = __builtin_amdgcn_permlane32_swap(__float_as_uint(oo[8]), __float_as_uint(oo[8]), false, false);
                const float sc = 1.0f / __uint_as_float(l2[1]);
                auto s0 = __builtin_amdgcn_permlane32_swap(pack2(oo[0] * sc, oo[1] * sc), pack2(oo[4] * sc, oo[5] * sc), false, false);
                auto s1 = __builtin_amdgcn_permlane32_swap(pack2(oo[2] * sc, oo[3] * sc), pack2(oo[6] * sc, oo[7] * sc), false, false);
                st128(W, u32x4{s0[0], s1[0], s0[1], s1[1]}, oa + j * 40960, 0);
                st64(W, u32x2{pack2(oo[8] * sc, oo[9] * sc), pack2(oo[10] * sc, oo[11] * sc)}, ob + j * 40960, 0);
            }
        }
        STAMP(6);
        publish(W, WS_F3 + ((f0 >> 6) * 32 + head) * FS, epoch, lane);
        STAMP(7);
        if (blockIdx.x == 0 && wid == 4) {
            NOUNROLL for (unsigned spins = 0; spins < SPIN_CAP; ++spins) {
                asm volatile("" ::: "memory");
                bool ok = true;
                UNROLL for (int i = 0; i < 8; ++i) {
                    const u32x4 f = ld128(W, WS_ACK + i * 1024 + lane * 16, 0);
                    ok = ok && f[0] == epoch && f[1] == epoch && f[2] == epoch && f[3] == epoch;
                }
                if (__all(ok)) break;
                __builtin_amdgcn_s_sleep(32);
            }
            if (lane == 0) st32(W, epoch, WS_EP, 0);
        }
        return;
    }

    __builtin_amdgcn_s_setprio(2);
    u32x4 wf[5][5];
    {
        unsigned coff[2]; const unsigned lin = lane * 16;
        UNROLL for (int i = 0; i < 2; ++i) coff[i] = (lane >> 3) * 4096 + (((lane & 7) ^ ((4 * i + (lane >> 4)) & 7)) << 4);
#define SQ_DC(n, slot, i) dma16(coff[(i) & 1], dc, cs0 + gwv * 131072 + (i) * 32768 + (n) * 128, lds0 + (slot) * SQ_STAGE + gwv * 4096 + (i) * 1024)
#define SQ_DC4(n, slot) do { const unsigned c_ = cs0 + gwv * 131072 + (n) * 128; \
            dma16x4(coff[0], coff[1], dc, c_, c_ + 31744, c_ + 63488, c_ + 95232, lds0 + (slot) * SQ_STAGE + gwv * 4096); } while (0)
#define SQ_DA5(n, slot) do { dma16sx4(lin, dws, as0 + (n) * 20480 + gwv * 5120, lds0 + (slot) * SQ_STAGE + 16384 + gwv * 5120); SQ_DA(n, slot, 4); } while (0)
#define SQ_DA(n, slot, i) dma16s(lin, dws, as0 + (n) * 20480 + gwv * 5120 + (i) * 1024, lds0 + (slot) * SQ_STAGE + 16384 + gwv * 5120 + (i) * 1024)
        const int wm = wid & 1, wn = wid >> 1;
        f32x4 acc[4][5];
        UNROLL for (int i = 0; i < 4; ++i) UNROLL for (int u = 0; u < 5; ++u) { acc[i][u] = f32x4{}; PIN(acc[i][u]); }
        unsigned ra[2];
        UNROLL for (int ks = 0; ks < 2; ++ks) ra[ks] = wm * 8192 + l15 * 128 + (((4 * ks + kg) ^ ((l15 >> 1) & 7)) << 4);
        const unsigned rb = 16384 + kg * 2560 + (wn * 80 + l15) * 16;
        s16x8 fc[2][4], fa[2][5];
        UNROLL for (int i = 0; i < 4; ++i) fc[1][i] = s16x8{};
        UNROLL for (int u = 0; u < 5; ++u) fa[1][u] = s16x8{};
        if (wid == 0) {
            const unsigned f2 = WS_F2 + (((ksp >> 1) * 8 + nt4 * 2 + hh) * 64 + (ksp & 1) * 32 + r) * FS;
            __builtin_amdgcn_s_sleep(16);
            wait_flags1<12>(W, f2, epoch);
            lds_wait(L + FL_FREE + fl4, 1);
            lds_post(LW + FL_FIN, 1);
        } else {
            lds_wait<LSL>(L + FL_FIN, 1);
        }
        STAMP(3);
        UNROLL for (int n = 0; n < 2; ++n) { SQ_DC4(n, n); SQ_DA5(n, n); }
        const lds_p pw = LW + (lane == 0 ? FL_SFULL + gwv * 4 : LDS_SINK + lane * 4);
        const lds_cp pr = L + FL_SFULL + fl4;
        const unsigned wfo = WS_WOT + (ct * 4 + gwv) * 25600 + lane * 16;
#define SQ_MF(u) do { UNROLL for (int i = 0; i < 4; ++i) acc[i][u] = MFMA16(fa[c][u], fc[c][i], acc[i][u]); SBAR(); } while (0)
#define SQ_RC(i) fc[n][i] = lds128(sr[ks] + (i) * 2048)
#define SQ_RA(u) fa[n][u] = lds128(sp + ks * 10240 + (u) * 256)
#define SQ_STG(DMA) do { \
            lds_cp sp = L + (j & 3) * SQ_STAGE + rb; lds_cp sr[2]; \
            UNROLL for (int ks = 0; ks < 2; ++ks) { sr[ks] = L + (j & 3) * SQ_STAGE + ra[ks]; PIN(sr[ks]); } \
            unsigned pv = j + 2, fl = 0; \
            PIN(sp); PIN(pv); SBAR(); \
            __builtin_amdgcn_s_setprio(GPRIO); \
            const int sf = (j + 2) & 3; \
            UNROLL for (int ks = 0; ks < 2; ++ks) { \
                const int n = ks, c = n ^ 1; \
                SQ_RC(0); SQ_RC(1); SQ_RC(2); SBAR(); SQ_MF(0); \
                SQ_RC(3); SQ_RA(0); SQ_RA(1); SBAR(); SQ_MF(1); \
                SQ_RA(2); SQ_RA(3); SQ_RA(4); \
                if (ks == 0) { WAITV(0); lds_post(pw, pv); } \
                SBAR(); SQ_MF(2); \
                if (DMA && ks == 0) { SQ_DC4(j + 2, sf); SBAR(); } \
                if (ks == 1) { fl = lds_peek(pr); SBAR(); } \
                SQ_MF(3); \
                if (DMA && ks == 0) { SQ_DA5(j + 2, sf); SBAR(); } \
                SQ_MF(4); \
            } \
            __builtin_amdgcn_s_setprio(3); \
            if (__builtin_expect(!__all(fl >= pv), 0)) lds_wait<LS2>(pr, pv); } while (0)
        int j = 0;
        WAITV(9); lds_post(pw, 1); lds_wait(pr, 1);
        NOUNROLL for (; j < 14; ++j) SQ_STG(true);
        NOUNROLL for (; j < 16; ++j) SQ_STG(false);
        UNROLL for (int u = 0; u < 5; ++u) UNROLL for (int i = 0; i < 4; ++i) acc[i][u] = MFMA16(fa[1][u], fc[1][i], acc[i][u]);
        __builtin_amdgcn_s_setprio(3);
        STAMP(4);
        if (wid == ksp) {
            UNROLL for (int i = 0; i < 4; ++i) UNROLL for (int u = 0; u < 5; ++u) stsf4(LW + 2 * SQ_STAGE + (i * 5 + u) * 1024 + lane * 16, acc[i][u]);
            const int px = r >> 2;
            const unsigned fw = WS_F2 + (((px >> 1) * 4 + hh) * 64 + ((px & 1) * 8 + ct) * 4 + (r & 3)) * FS;
            const unsigned fg = WS_F2 + (15 * 64 + (tt >> 1) * 4 + (r & 3)) * FS;
            wait_flags4<12>(W, fw, fw + 128 * FS, fg, fg, epoch);
        } else {
            const unsigned yb = WS_Y + ((((mt * 2 + wm) * 8 + nt4 * 2 + wn) * 4 + ksp) * 4) * 2560;
            UNROLL for (int i = 0; i < 4; ++i) {
                UNROLL for (int u = 0; u < 4; u += 2)
                    st128(W, u32x4{pack2(acc[i][u][0], acc[i][u][1]), pack2(acc[i][u][2], acc[i][u][3]), pack2(acc[i][u + 1][0], acc[i][u + 1][1]), pack2(acc[i][u + 1][2], acc[i][u + 1][3])},
                          yb + lane * 16, i * 2560 + u * 512);
                st64(W, u32x2{pack2(acc[i][4][0], acc[i][4][1]), pack2(acc[i][4][2], acc[i][4][3])}, yb + i * 2560 + 2048 + lane * 8, 0);
            }
            publish(W, WS_F4 + (((mt * 2 + wm) * 8 + nt4 * 2 + wn) * 4 + ksp) * FS, epoch, lane);
        }
        lds_signal(pw, 18); lds_wait<LSL>(pr, 18);
        UNROLL for (int st = 0; st < 5; ++st) UNROLL for (int u = 0; u < 5; ++u) wf[st][u] = ld128(W, wfo, (st * 5 + u) * 1024);
        STAMP(5);
    }
    const int t0 = tt * 64;
    const float gt = __uint_as_float(ld32(W, WS_G + (t0 + gwv * 16 + l15) * 4, 0));
    WAITV(0);
    lds_wait<LSL>(L + FL_EX + fl4, 1);
    STAMP(6);
    wait_flags1<SL3>(W, WS_F4 + ((tt * 8 + ct) * 4 + ((ksp + 1 + (r % 3)) & 3)) * FS, epoch);
    u32x4 ya[3][2]; u32x2 yc[3];
    UNROLL for (int p = 0; p < 3; ++p) {
        const unsigned yo = WS_Y + (((tt * 8 + ct) * 4 + ((ksp + 1 + p) & 3)) * 4 + gwv) * 2560;
        ya[p][0] = ld128(W, lane * 16, yo); ya[p][1] = ld128(W, lane * 16 + 1024, yo); yc[p] = ld64(W, lane * 8 + 2048, yo);
    }
    const float h = 1.0f - gt;
    f32x4 ys[5];
    UNROLL for (int u = 0; u < 5; ++u) ys[u] = ldsf4(L + 2 * SQ_STAGE + (gwv * 5 + u) * 1024 + lane * 16);
    SBAR();
    wait_flags1<SL3>(W, WS_F3 + (tt * 32 + gwv * 8 + (lane & 7)) * FS, epoch);
    STAMP(8);
    const unsigned boff = WS_O + tt * 81920 + gwv * 5120 + lane * 16;
    u32x4 bf[4][5];
    UNROLL for (int d = 0; d < 4; ++d)
        UNROLL for (int st = 0; st < 5; ++st) bf[d][st] = ld128(W, boff + st * 1024, ((gwv + d + 1) & 3) * 20480);
    SBAR();
    UNROLL for (int u = 0; u < 5; ++u) {
        UNROLL for (int p = 0; p < 3; ++p) {
            const unsigned w0 = u < 4 ? ya[p][(u >> 1) & 1][(u & 1) * 2] : yc[p][0], w1 = u < 4 ? ya[p][(u >> 1) & 1][(u & 1) * 2 + 1] : yc[p][1];
            ys[u][0] += bflo(w0); ys[u][1] += bfhi(w0); ys[u][2] += bflo(w1); ys[u][3] += bfhi(w1);
        }
        ys[u] *= h; PIN(ys[u]);
    }
    SBAR();
    f32x4 res[5];
    UNROLL for (int d = 0; d < 4; ++d) {
        UNROLL for (int u = 0; u < 5; ++u) res[u] = f32x4{};
        UNROLL for (int st = 0; st < 5; ++st) UNROLL for (int u = 0; u < 5; ++u) res[u] = MFMA16(as8(wf[st][u]), as8(bf[d][st]), res[u]);
        if (d < 3) { UNROLL for (int u = 0; u < 5; ++u) stsf4(LW + ((gwv + d + 1) & 3) * GW_LDS + (d * 5 + u) * 1024 + lane * 16, res[u]); }
        if (d == 2) lds_signal(LW + FL_RED + gwv * 4);
        SBAR();
    }
    STAMP(9);
    lds_wait(L + FL_RED + fl4);
    UNROLL for (int d = 0; d < 3; ++d) UNROLL for (int u = 0; u < 5; ++u) res[u] += ldsf4(L + gwv * GW_LDS + (d * 5 + u) * 1024 + lane * 16);
    UNROLL for (int u = 0; u < 5; ++u) res[u] = res[u] * gt + ys[u];
    char* dst = out + (size_t)(t0 + gwv * 16 + l15) * 1280 + ct * 160 + kg * 8;
    const rsrc_t RO = __builtin_amdgcn_make_buffer_rsrc(out, 0, 2048 * 1280, 0x00020000);
    const unsigned oo = (t0 + gwv * 16 + l15) * 1280 + ct * 160;
    UNROLL for (int u = 0; u < 4; u += 2) {
        auto s0 = __builtin_amdgcn_permlane16_swap(pack2(res[u][0], res[u][1]), pack2(res[u + 1][0], res[u + 1][1]), false, false);
        auto s1 = __builtin_amdgcn_permlane16_swap(pack2(res[u][2], res[u][3]), pack2(res[u + 1][2], res[u + 1][3]), false, false);
        __builtin_amdgcn_raw_buffer_store_b128(u32x4{s0[0], s1[0], s0[1], s1[1]}, RO, oo + (u + (kg & 1)) * 32 + (kg >> 1) * 16, 0, OUTM == 1 ? 0 : OUTM == 2 ? 16 : 2);
    }
    __builtin_amdgcn_raw_buffer_store_b64(u32x2{pack2(res[4][0], res[4][1]), pack2(res[4][2], res[4][3])}, RO, oo + 128 + kg * 8, 0, OUTM == 1 ? 0 : OUTM == 2 ? 16 : 2);
    STAMP(10);
}

extern "C" void kernel_launch(void* const* d_in, const int* in_sizes, int n_in, void* d_out, int out_size,
                              void* d_ws, size_t ws_size, hipStream_t stream) {
    (void)hipFuncSetAttribute((const void*)fused, hipFuncAttributeMaxDynamicSharedMemorySize, LDS_BYTES);
    fused<<<256, 512, LDS_BYTES, stream>>>((const char*)d_in[0], (const char*)d_in[1], (const char*)d_in[2], (const char*)d_in[4],
                                           (const char*)d_in[5], (const char*)d_in[6], (const char*)d_in[7], (const char*)d_in[8],
                                           (char*)d_ws + WS_BASE(ws_size), (char*)d_out);
}
```

```cpp
#include <hip/hip_runtime.h>
#include <hip/hip_bf16.h>
#include <stdint.h>

typedef short s16x8 __attribute__((ext_vector_type(8)));
typedef short s16x4 __attribute__((ext_vector_type(4)));
typedef float f32x16 __attribute__((ext_vector_type(16)));
typedef float f32x8 __attribute__((ext_vector_type(8)));
typedef float f32x4 __attribute__((ext_vector_type(4)));
typedef unsigned u32x4 __attribute__((ext_vector_type(4)));
typedef unsigned u32x2 __attribute__((ext_vector_type(2)));
typedef __bf16 bf16x2_t __attribute__((ext_vector_type(2)));
typedef __attribute__((address_space(3))) const char* lds_cp;
typedef __attribute__((address_space(3))) char* lds_p;
typedef __amdgpu_buffer_rsrc_t rsrc_t;

#define DI __device__ __forceinline__
#define UNROLL _Pragma("unroll")
#define NOUNROLL _Pragma("unroll 1")
#define MFMA32(a, b, c) __builtin_amdgcn_mfma_f32_32x32x16_bf16((a), (b), (c), 0, 0, 0)
#define MFMA32K8(a, b, c) __builtin_amdgcn_mfma_f32_32x32x8bf16_1k((a), (b), (c), 0, 0, 0)
#define MFMA16(a, b, c) __builtin_amdgcn_mfma_f32_16x16x32_bf16((a), (b), (c), 0, 0, 0)
#define SBAR() __builtin_amdgcn_sched_barrier(0)
#define PIN(x) asm volatile("" : "+v"(x))
#define EX2(x) __builtin_amdgcn_exp2f(x)
#define SC1 16

constexpr unsigned CH = 2048 * 16;
constexpr unsigned WS_QK = 0;
constexpr unsigned WS_O = 160 * CH;
constexpr unsigned WS_Y = WS_O + 80 * CH;
constexpr unsigned ABROW = 640 * 16;
constexpr unsigned WS_AB = WS_Y + 320 * CH;
constexpr unsigned VROW = 10240;
constexpr unsigned WS_VT = WS_AB + 512 * ABROW;
constexpr unsigned WS_WOT = WS_VT + 256 * VROW;
constexpr unsigned WS_G = WS_WOT + 80 * ABROW;
constexpr unsigned FS = 128;
constexpr unsigned WS_ACK = WS_G + 8192;
constexpr unsigned WS_EP = WS_ACK + 8192;
constexpr unsigned WS_F1 = WS_EP + 4096;
constexpr unsigned WS_F2 = WS_F1 + 1024 * FS;
constexpr unsigned WS_F3 = WS_F2 + 1024 * FS;
constexpr unsigned WS_F4 = WS_F3 + 1024 * FS;
constexpr unsigned WS_ONE = WS_F4 + 1024 * FS;
constexpr unsigned WS_END = WS_ONE + 2048 * 128;
constexpr unsigned OOB = 0x80000000u;
#define WSTAIL 0
#define WS_BASE(n) (WSTAIL ? (((n) - WS_END) & ~(size_t)0x1FFFFF) : (size_t)0)

constexpr int P1_STAGE = 32768;
constexpr int SQ_STAGE = 36864;
constexpr int GW_LDS = 15360;
constexpr int EXB = 1536;
constexpr int LDS_EX = 4 * SQ_STAGE;
constexpr int LDS_X9 = 4 * P1_STAGE;
constexpr int LDS_GW = LDS_X9 + 20480;
constexpr int LDS_WT = LDS_GW + 2048;
constexpr int LDS_FL = LDS_EX + 8 * EXB;
constexpr int LDS_SINK = LDS_FL + 256;
constexpr int LDS_BYTES = LDS_SINK + 256;
static_assert(LDS_BYTES <= 163840 && LDS_FL >= LDS_WT + 4096, "lds");
constexpr int FL_FULL = LDS_FL, FL_GO = LDS_FL + 32, FL_EX = LDS_FL + 48, FL_SFULL = LDS_FL + 80,
              FL_SFREE = LDS_FL + 96, FL_FIN = LDS_FL + 112, FL_RED = LDS_FL + 128, FL_FREE = LDS_FL + 144;
#define GPRIO 0
#define FWARM 1
#define HI 0
#define OUTM 3
#define LSL 8
#define LS2 3
#define PSL 8
#define PFD 0
#define INI 2
#define WTL 0
#define ROT 1
#define SROT 0
#define SL1 4
#define SL3 4
#define SJ 4
#define PUBZ 3
#define AABL 0
#define SABL 0
#define PFX 0
#define CPF 0
#define CODE_LINES 300

#define STAMP(i) do { } while (0)
#define TACC(a) do { } while (0)
#define TDECL() do { } while (0)
#define TOUT(i) do { } while (0)
#define STAMP2(i) do { } while (0)

DI unsigned pack2(float a, float b) {
    bf16x2_t v = {(__bf16)a, (__bf16)b};
    return __builtin_bit_cast(unsigned, v);
}
DI float bflo(unsigned u) { return __uint_as_float(u << 16); }
DI float bfhi(unsigned u) { return __uint_as_float(u & 0xFFFF0000u); }
DI s16x8 as8(u32x4 v) { return __builtin_bit_cast(s16x8, v); }
DI s16x4 as4(u32x2 v) { return __builtin_bit_cast(s16x4, v); }

DI u32x4 mkdesc(const void* p, unsigned bytes) {
    const unsigned long long a = (unsigned long long)p;
    return u32x4{(unsigned)a, (unsigned)(a >> 32) & 0xFFFFu, bytes, 0x00020000u};
}
DI void dma16(unsigned voff, u32x4 desc, unsigned soff, unsigned ldsaddr) {
    unsigned keep;
    asm volatile("s_mov_b32 %0, m0\n\ts_mov_b32 m0, %4\n\ts_nop 0\n\tbuffer_load_dwordx4 %1, %2, %3 offen lds\n\ts_mov_b32 m0, %0"
                 : "=&s"(keep) : "v"(voff), "s"(desc), "s"(soff), "s"(ldsaddr) : "memory");
}
DI void dma16s(unsigned voff, u32x4 desc, unsigned soff, unsigned ldsaddr) {
    unsigned keep;
    asm volatile("s_mov_b32 %0, m0\n\ts_mov_b32 m0, %4\n\ts_nop 0\n\tbuffer_load_dwordx4 %1, %2, %3 offen sc1 lds\n\ts_mov_b32 m0, %0"
                 : "=&s"(keep) : "v"(voff), "s"(desc), "s"(soff), "s"(ldsaddr) : "memory");
}
DI void dma16x4(unsigned v0, unsigned v1, u32x4 desc, unsigned s0, unsigned s1, unsigned s2, unsigned s3, unsigned ldsaddr) {
    unsigned keep;
    asm volatile("s_mov_b32 %0, m0\n\ts_mov_b32 m0, %8\n\ts_nop 0\n\t"
                 "buffer_load_dwordx4 %1, %3, %4 offen lds\n\tbuffer_load_dwordx4 %2, %3, %5 offen offset:1024 lds\n\t"
                 "buffer_load_dwordx4 %1, %3, %6 offen offset:2048 lds\n\tbuffer_load_dwordx4 %2, %3, %7 offen offset:3072 lds\n\ts_mov_b32 m0, %0"
                 : "=&s"(keep) : "v"(v0), "v"(v1), "s"(desc), "s"(s0), "s"(s1), "s"(s2), "s"(s3), "s"(ldsaddr) : "memory");
}
DI void dma16sx4(unsigned voff, u32x4 desc, unsigned soff, unsigned ldsaddr) {
    unsigned keep;
    asm volatile("s_mov_b32 %0, m0\n\ts_mov_b32 m0, %4\n\ts_nop 0\n\t"
                 "buffer_load_dwordx4 %1, %2, %3 offen sc1 lds\n\tbuffer_load_dwordx4 %1, %2, %3 offen offset:1024 sc1 lds\n\t"
                 "buffer_load_dwordx4 %1, %2, %3 offen offset:2048 sc1 lds\n\tbuffer_load_dwordx4 %1, %2, %3 offen offset:3072 sc1 lds\n\ts_mov_b32 m0, %0"
                 : "=&s"(keep) : "v"(voff), "s"(desc), "s"(soff), "s"(ldsaddr) : "memory");
}
DI void dma4(unsigned voff, u32x4 desc, unsigned soff, unsigned ldsaddr) {
    unsigned keep;
    asm volatile("s_mov_b32 %0, m0\n\ts_mov_b32 m0, %4\n\ts_nop 0\n\tbuffer_load_dword %1, %2, %3 offen sc1 lds\n\ts_mov_b32 m0, %0"
                 : "=&s"(keep) : "v"(voff), "s"(desc), "s"(soff), "s"(ldsaddr) : "memory");
}
#define WAITV(n) asm volatile("s_waitcnt vmcnt(" #n ")" ::: "memory")
#define WAITL() asm volatile("s_waitcnt lgkmcnt(0)" ::: "memory")
#define BARRIER() asm volatile("s_waitcnt lgkmcnt(0)\n\ts_barrier" ::: "memory")
#define BARRIER_NOWAIT() asm volatile("s_barrier" ::: "memory")

DI s16x8 lds128(lds_cp p) { return *(const __attribute__((address_space(3))) s16x8*)p; }
DI f32x4 ldsf4(lds_cp p) { return *(const __attribute__((address_space(3))) f32x4*)p; }
DI void stsf4(lds_p p, f32x4 v) { *(__attribute__((address_space(3))) f32x4*)p = v; }
DI s16x4 ldstr(lds_cp p) {
    return __builtin_bit_cast(s16x4, __builtin_amdgcn_ds_read_tr16_b64_v4i16((__attribute__((address_space(3))) s16x4*)p));
}
DI s16x8 cat8(s16x4 a, s16x4 b) { return __builtin_shufflevector(a, b, 0, 1, 2, 3, 4, 5, 6, 7); }
DI u32x4 ld128(rsrc_t r, unsigned voff, unsigned soff) { return __builtin_amdgcn_raw_buffer_load_b128(r, voff, soff, SC1); }
DI u32x2 ld64(rsrc_t r, unsigned voff, unsigned soff) { return __builtin_amdgcn_raw_buffer_load_b64(r, voff, soff, SC1); }
DI unsigned ld32(rsrc_t r, unsigned voff, unsigned soff) { return __builtin_amdgcn_raw_buffer_load_b32(r, voff, soff, SC1); }
DI void st128(rsrc_t r, u32x4 v, unsigned voff, unsigned soff) { __builtin_amdgcn_raw_buffer_store_b128(v, r, voff + soff, 0, SC1); }
DI void st64(rsrc_t r, u32x2 v, unsigned voff, unsigned soff) { __builtin_amdgcn_raw_buffer_store_b64(v, r, voff, soff, SC1); }
DI void st32(rsrc_t r, unsigned v, unsigned voff, unsigned soff) { __builtin_amdgcn_raw_buffer_store_b32(v, r, voff, soff, SC1); }

constexpr unsigned SPIN_CAP = 200000;
DI void publish(rsrc_t w, unsigned flag_off, unsigned epoch, int lane) {
    WAITV(0);
    if (lane == 0) st32(w, epoch, flag_off, 0);
}
template <int SL>
DI void wait_flags4(rsrc_t w, unsigned o0, unsigned o1, unsigned o2, unsigned o3, unsigned epoch) {
    NOUNROLL for (unsigned spins = 0; spins < SPIN_CAP; ++spins) {
        asm volatile("" ::: "memory");
        const unsigned a = ld32(w, o0, 0), b = ld32(w, o1, 0), c = ld32(w, o2, 0), d = ld32(w, o3, 0);
        if (__all(a == epoch && b == epoch && c == epoch && d == epoch)) break;
        __builtin_amdgcn_s_sleep(SL);
    }
    asm volatile("" ::: "memory");
}
template <int SL>
DI void wait_flags1(rsrc_t w, unsigned o0, unsigned epoch) {
    NOUNROLL for (unsigned spins = 0; spins < SPIN_CAP; ++spins) {
        asm volatile("" ::: "memory");
        const unsigned a = ld32(w, o0, 0);
        if (__all(a == epoch)) break;
        __builtin_amdgcn_s_sleep(SL);
    }
    asm volatile("" ::: "memory");
}
DI void lds_signal(lds_p p, unsigned val = 1u) {
    WAITL();
    *(volatile __attribute__((address_space(3))) unsigned*)p = val;
}
DI void lds_post(lds_p p, unsigned val) { *(volatile __attribute__((address_space(3))) unsigned*)p = val; }
DI unsigned lds_peek(lds_cp p) { return *(const volatile __attribute__((address_space(3))) unsigned*)p; }
template <int SL = 1>
DI void lds_wait(lds_cp p, unsigned val = 1u) {
    NOUNROLL for (unsigned spins = 0; spins < 4 * SPIN_CAP; ++spins) {
        const unsigned v = *(const volatile __attribute__((address_space(3))) unsigned*)p;
        if (__all(v >= val)) break;
        __builtin_amdgcn_s_sleep(SL);
    }
    asm volatile("" ::: "memory");
}

__global__ __launch_bounds__(512, 2) void fused(const char* __restrict__ x, const char* __restrict__ wqkv,
                                                 const char* __restrict__ wo, const char* __restrict__ cre,
                                                 const char* __restrict__ cim, const char* __restrict__ wr,
                                                 const char* __restrict__ wi, const char* __restrict__ gw,
                                                 char* ws, char* __restrict__ out) {
    extern __shared__ __attribute__((aligned(16))) char smem[];
    unsigned long long pc; asm volatile("s_getpc_b64 %0" : "=s"(pc));
    const int tid = threadIdx.x;
    const int wid = __builtin_amdgcn_readfirstlane(tid >> 6);
    const int gwv = wid & 3;
    const int xcd = blockIdx.x & 7, idx = blockIdx.x >> 3;
    const unsigned lds0 = (unsigned)(uintptr_t)smem;
    const lds_cp L = (lds_cp)smem;
    const lds_p LW = (lds_p)smem;
    const u32x4 dx = mkdesc(x, 2048 * 1280);
    const int mt = (xcd & 1) * 8 + (idx & 7), nt = (xcd >> 1) * 4 + (idx >> 3);
    const u32x4 dw = mkdesc(wqkv, 640 * 3840);
    const int rot = ROT ? (((idx & 7) * 5 + 1) / 4 + ((idx >> 3) * 5 + 1) / 2) % 10 : 0;
#define KST(st) ((st) + rot >= 10 ? (st) + rot - 10 : (st) + rot)
    const rsrc_t W = __builtin_amdgcn_make_buffer_rsrc(ws, 0, WS_END, 0x00020000);
    const unsigned oneoff = WS_ONE + (blockIdx.x * 8 + wid) * 128;
    unsigned epoch;
    {
    const int lane = tid & 63;
    unsigned roff[2];
    UNROLL for (int i = 0; i < 2; ++i) roff[i] = (lane >> 3) * 1280 + (((lane & 7) ^ ((4 * i + (lane >> 4)) & 7)) << 4);
    unsigned woff[2];
    UNROLL for (int i = 0; i < 2; ++i) woff[i] = nt == 15 ? OOB : (lane >> 3) * 3840 + (((lane & 7) ^ (((lane >> 4) & 1) << 2) ^ (i << 1)) << 4);
#define P1_DX(st, p, par) dma16(roff[par], dx, (mt * 128 + (p) * 8) * 1280 + KST(st) * 128, lds0 + ((st) & 3) * P1_STAGE + (p) * 1024)
#define P1_DW(st, p, par) dma16(woff[par], dw, (KST(st) * 64 + ((p) & 7) * 8) * 3840 + nt * 256 + ((p) >> 3) * 128, lds0 + ((st) & 3) * P1_STAGE + 16384 + (p) * 1024)
    P1_DX(0, 2 * wid, 0); P1_DW(0, 2 * wid, 0); P1_DX(0, 2 * wid + 1, 1); P1_DW(0, 2 * wid + 1, 1);
    STAMP(0);
    const int hh = lane >> 5;
    const int l15 = lane & 15, kg = lane >> 4;
    const unsigned fl4 = (lane & 3) * 4;
    const u32x4 dh = mkdesc(nt < 8 ? wr : wi, 640 * 1280);
#define GET_EPOCH() do { epoch = ld32(W, WS_EP, 0) + 1u; if (lane < 4) st32(W, 0x3F803F80u, oneoff + lane * 4, 0); } while (0)
#define WH_ADDR(q) ((q) < 32 ? 2 * P1_STAGE + (q) * 1024 : (q) < 48 ? 3 * P1_STAGE + 16384 + ((q) - 32) * 1024 : \
                    (q) < 68 ? LDS_X9 + ((q) - 48) * 1024 : (q) < 84 ? 16384 + ((q) - 68) * 1024 : P1_STAGE + 16384 + ((q) - 84) * 1024)

    if (wid >= 4) {
#define P1_ISSUE(st) do { \
            const unsigned x_ = (mt * 128 + gwv * 32) * 1280 + KST(st) * 128, w_ = (KST(st) * 64 + (gwv & 1) * 32) * 3840 + nt * 256 + (gwv >> 1) * 128; \
            dma16x4(roff[0], roff[1], dx, x_, x_ + 9216, x_ + 18432, x_ + 27648, lds0 + ((st) & 3) * P1_STAGE + gwv * 4096); \
            dma16x4(woff[0], woff[1], dw, w_, w_ + 29696, w_ + 59392, w_ + 89088, lds0 + ((st) & 3) * P1_STAGE + 16384 + gwv * 4096); } while (0)
#define P1_DH(q, par) do { const int q_ = (q), c_ = q_ / 10, p_ = q_ - c_ * 10; \
            dma16(roff[par], dh, ((nt & 7) * 80 + p_ * 8) * 1280 + KST(c_) * 128, lds0 + WH_ADDR(q_)); } while (0)
        P1_ISSUE(1); P1_ISSUE(2);
        WAITV(16); BARRIER_NOWAIT(); P1_ISSUE(3);
        NOUNROLL for (int k = INI == 1 ? 2 : 1; k < 7; ++k) { WAITV(16); BARRIER_NOWAIT(); P1_ISSUE(k + 3); }
        WAITV(16); BARRIER_NOWAIT();
        UNROLL for (int i = 0; i < 8; ++i) P1_DH(gwv * 8 + i, i & 1);
        WAITV(16); BARRIER_NOWAIT();
        UNROLL for (int i = 0; i < 8; ++i) P1_DH(32 + gwv * 8 + i, i & 1);
        WAITV(16); BARRIER_NOWAIT();
        UNROLL for (int i = 0; i < 6; ++i) { const int q = 64 + gwv * 6 + i; P1_DH(q < 84 ? q : 82 + (i & 1), i & 1); }
        GET_EPOCH();
        __builtin_amdgcn_s_setprio(3);
        WAITV(0); BARRIER_NOWAIT();
        UNROLL for (int i = 0; i < 4; ++i) P1_DH(84 + gwv * 4 + i, i & 1);
        STAMP2(16);
        STAMP(1);
    } else {
        const unsigned sink = lds0 + LDS_SINK;
        const int pi = xcd * 4 + wid, pst = pi / 5, pu = pi - pst * 5;
        if (PFD) __builtin_amdgcn_s_sleep(PFD);
        if (PFX) {
            const unsigned b = blockIdx.x;
            if (wid == 0) { dma4(lane * 128, dx, b * 10240, sink); dma4(lane < 16 ? lane * 128 : OOB, dx, b * 10240 + 8192, sink); }
            if (wid == 1) { dma4(lane * 128, dw, b * 9600, sink); dma4(lane < 11 ? lane * 128 : OOB, dw, b * 9600 + 8192, sink); }
            if (wid == 2) { dma4(FWARM && lane < 16 ? (b * 16 + lane) * FS : OOB, mkdesc(ws, WS_END), WS_F1, sink); dma4(OOB, dx, 0, sink); }
        } else if (wid < 3) { dma4(OOB, dx, 0, sink); dma4(OOB, dx, 0, sink); }
        if (wid == 3) {
            dma4(lane < 12 ? lane * 128 : OOB, mkdesc((const void*)(pc & ~127ull), CODE_LINES * 128), idx * 1536, sink);
            dma16(nt == 15 ? lane * 16 : OOB, mkdesc(gw, 1280), 0, lds0 + LDS_GW);
        }
        if (tid < 64) *(__attribute__((address_space(3))) unsigned*)(LW + LDS_FL + tid * 4) = 0u;
        const int wm = wid & 1, wn = wid >> 1;
        unsigned ra[2];
        UNROLL for (int ks = 0; ks < 2; ++ks) ra[ks] = l15 * 128 + (((4 * ks + kg) ^ ((l15 >> 1) & 7)) << 4);
        const unsigned xk = wm * 8192 + wn * 4096, xo = wm * 8192 + (wn ^ 1) * 4096;
        s16x8 xs[10][2][2];
#define WT_LOAD() dma16((lane >> 1) * 1280 + (lane & 1) * 16, mkdesc(wo, 640 * 1280), \
                  pi < 25 ? ((idx >> 3) * 160 + pst * 32) * 1280 + (idx & 7) * 160 + pu * 32 : OOB, lds0 + LDS_WT + wid * 1024)
#define WT_STORE() do { const lds_cp tp_ = L + LDS_WT + wid * 1024 + (8 * kg + (l15 >> 2)) * 32 + (lane & 3) * 8; \
            st128(W, __builtin_bit_cast(u32x4, cat8(ldstr(tp_), ldstr(tp_ + 128))), \
                  pi < 25 ? WS_WOT + (((idx & 7) * 4 + (idx >> 3)) * 25 + pi) * 1024 + lane * 16 : OOB, 0); } while (0)
        if (nt == 15) {
            const int m0 = mt * 128 + wm * 64 + wn * 32;
            if (wid == 3) dma16(lane * 16, mkdesc(gw, 1280), 1024, lds0 + LDS_GW + 1024);
            WT_LOAD();
            epoch = ld32(W, WS_EP, 0) + 1u;
            {
                const unsigned o0 = (mt * 4 + wid) * 12800, v1 = lane < 36 ? lane * 128 : OOB;
                dma4(lane * 128, mkdesc(wr, 640 * 1280), o0, sink); dma4(v1, mkdesc(wr, 640 * 1280), o0 + 8192, sink);
                dma4(lane * 128, mkdesc(wi, 640 * 1280), o0, sink); dma4(v1, mkdesc(wi, 640 * 1280), o0 + 8192, sink);
            }
            WAITV(4);
            float sum[2] = {0.f, 0.f};
            UNROLL for (int k = 0; k < 10; ++k) {
                BARRIER();
                UNROLL for (int ks = 0; ks < 2; ++ks) {
                    const u32x4 gv = __builtin_bit_cast(u32x4, lds128(L + LDS_GW + KST(k) * 128 + (4 * ks + kg) * 16));
                    UNROLL for (int h = 0; h < 2; ++h) {
                        xs[k][ks][h] = lds128(L + (k & 3) * P1_STAGE + xk + h * 2048 + ra[ks]);
                        const u32x4 xv = __builtin_bit_cast(u32x4, xs[k][ks][h]);
                        UNROLL for (int e = 0; e < 4; ++e)
                            { const unsigned xe = xv[e], ge = gv[e]; sum[h] = __builtin_amdgcn_fdot2_f32_bf16(__builtin_bit_cast(bf16x2_t, xe), __builtin_bit_cast(bf16x2_t, ge), sum[h], false); }
                    }
                }
                SBAR();
            }
            UNROLL for (int h = 0; h < 2; ++h) { sum[h] += __shfl_xor(sum[h], 16); sum[h] += __shfl_xor(sum[h], 32); }
            const float sm = kg & 1 ? sum[1] : sum[0];
            if (lane < 32) st32(W, __float_as_uint(1.0f / (1.0f + __expf(-sm))), WS_G + (m0 + lane) * 4, 0);
        } else {
            const int q = l15 >> 2;
            unsigned trb[4];
            UNROLL for (int j = 0; j < 4; ++j)
                trb[j] = 16384 + wn * 8192 + (8 * kg + q) * 128 + ((j ^ (((q >> 1) & 1) << 1) ^ (kg & 1)) << 5) + 8 * (lane & 3);
            f32x4 acc[4][4];
            UNROLL for (int i = 0; i < 4; ++i) UNROLL for (int j = 0; j < 4; ++j) acc[i][j] = f32x4{};
            s16x8 xt[2][2]; s16x4 wv[4][2];
            if (!WTL) WT_LOAD();
            epoch = ld32(W, WS_EP, 0) + 1u;
            SBAR();
            if (WTL) { WAITV(3); } else { WAITV(4); }
#define P1_RW(z, j) do { const lds_cp p_ = L + (((z) >> 1) & 3) * P1_STAGE + trb[j] + ((z) & 1) * 4096; wv[j][0] = ldstr(p_); wv[j][1] = ldstr(p_ + 512); } while (0)
#define P1_RK(z, h) xs[(z) >> 1][(z) & 1][h] = lds128(L + (((z) >> 1) & 3) * P1_STAGE + xk + (h) * 2048 + ra[(z) & 1])
#define P1_RO(z, h) xt[(z) & 1][h] = lds128(L + (((z) >> 1) & 3) * P1_STAGE + xo + (h) * 2048 + ra[(z) & 1])
#define P1_XF(z, i) ((i) < 2 ? xs[(z) >> 1][(z) & 1][(i) & 1] : xt[(z) & 1][(i) & 1])
#define P1_LOOP(MF) \
            BARRIER(); \
            STAMP2(11); \
            P1_RK(0, 0); P1_RW(0, 0); P1_RK(0, 1); P1_RO(0, 0); P1_RO(0, 1); P1_RW(0, 1); P1_RW(0, 2); P1_RW(0, 3); SBAR(); \
            UNROLL for (int z = 0; z < 20; ++z) { \
                UNROLL for (int j = 0; j < 4; ++j) { \
                    MF(z, 0, j); SBAR(); \
                    if (z < 19 && j == 1) { P1_RK(z + 1, 0); SBAR(); } \
                    if (z < 19 && j == 2) { P1_RO(z + 1, 0); SBAR(); } \
                    MF(z, 1, j); SBAR(); \
                    if (z < 19 && j == 1) { P1_RK(z + 1, 1); SBAR(); } \
                    if (z < 19 && j == 2) { P1_RO(z + 1, 1); SBAR(); } \
                    MF(z, 2, j); SBAR(); \
                    if (j == 0 && (z & 1) && z < 19) { BARRIER(); } \
                    MF(z, 3, j); SBAR(); \
                    if (z < 19) { P1_RW(z + 1, j); SBAR(); } \
                } \
            }
#define P1_MFQ(z, i, j) acc[i][j] = MFMA16(cat8(wv[j][0], wv[j][1]), P1_XF(z, i), acc[i][j])
#define P1_MFV(z, i, j) acc[i][j] = MFMA16(P1_XF(z, i), cat8(wv[j][0], wv[j][1]), acc[i][j])
            if (nt < 10) {
                P1_LOOP(P1_MFQ)
                STAMP(1);
                const float sc = nt < 5 ? 0.32259889079f : 1.0f;
                UNROLL for (int i = 0; i < 4; i += 2)
                    UNROLL for (int j = 0; j < 4; ++j) {
                        unsigned a0 = pack2(acc[i][j][0] * sc, acc[i][j][1] * sc), a1 = pack2(acc[i][j][2] * sc, acc[i][j][3] * sc);
                        unsigned b0 = pack2(acc[i + 1][j][0] * sc, acc[i + 1][j][1] * sc), b1 = pack2(acc[i + 1][j][2] * sc, acc[i + 1][j][3] * sc);
                        auto s0 = __builtin_amdgcn_permlane16_swap(a0, b0, false, false);
                        auto s1 = __builtin_amdgcn_permlane16_swap(a1, b1, false, false);
                        st128(W, u32x4{s0[0], s1[0], s0[1], s1[1]}, WS_QK + (nt * 16 + wn * 8 + (kg >> 1)) * CH + (mt * 128 + wm * 64 + ((i >> 1) ^ wn) * 32 + (kg & 1) * 16 + l15) * 16, j * 2 * CH);
                    }
            } else {
                P1_LOOP(P1_MFV)
                STAMP(1);
                UNROLL for (int i = 0; i < 4; ++i)
                    UNROLL for (int j = 0; j < 4; j += 2) {
                        unsigned a0 = pack2(acc[i][j][0], acc[i][j][1]), a1 = pack2(acc[i][j][2], acc[i][j][3]);
                        unsigned b0 = pack2(acc[i][j + 1][0], acc[i][j + 1][1]), b1 = pack2(acc[i][j + 1][2], acc[i][j + 1][3]);
                        auto s0 = __builtin_amdgcn_permlane32_swap(a0, b0, false, false);
                        auto s1 = __builtin_amdgcn_permlane32_swap(a1, b1, false, false);
                        st128(W, u32x4{s0[0], s1[0], s0[1], s1[1]}, WS_VT + ((mt * 8 + wm * 4 + ((i >> 1) ^ wn) * 2 + (i & 1)) * 2 + (kg & 1)) * VROW + ((nt - 10) * 128 + wn * 64 + hh * 16 + l15) * 16, j * 256);
                    }
            }
        }
        BARRIER();
        __builtin_amdgcn_s_setprio(2);
        STAMP(7);
        f32x4 hc[2][5];
        UNROLL for (int h = 0; h < 2; ++h) UNROLL for (int u = 0; u < 5; ++u) hc[h][u] = f32x4{};
        if (PUBZ == 0 && nt != 15) publish(W, WS_F1 + ((nt * 2 + wn) * 32 + mt * 2 + wm) * FS, epoch, lane);
        UNROLL for (int z = 14; z < 20; ++z) UNROLL for (int h = 0; h < 2; ++h) P1_RK(z, h);
        s16x8 wb[2][5];
        UNROLL for (int z = 0; z < 21; ++z) {
            if (PUBZ && z == PUBZ && nt != 15) publish(W, WS_F1 + ((nt * 2 + wn) * 32 + mt * 2 + wm) * FS, epoch, lane);
            if (WTL && z == PUBZ && nt != 15) WT_LOAD();
            if (z == 16) { STAMP2(17); lds_wait(L + FL_FULL + fl4, 1); STAMP2(18); }
            UNROLL for (int u = 0; u < 5; ++u) {
                if (z < 20) { wb[z & 1][u] = lds128(L + WH_ADDR((z >> 1) * 10 + 2 * u) + ra[z & 1]); SBAR(); }
                if (z > 0) {
                    hc[0][u] = MFMA16(xs[(z - 1) >> 1][(z - 1) & 1][0], wb[(z - 1) & 1][u], hc[0][u]);
                    hc[1][u] = MFMA16(xs[(z - 1) >> 1][(z - 1) & 1][1], wb[(z - 1) & 1][u], hc[1][u]); SBAR();
                }
            }
        }
        STAMP2(19);
        WAITV(0);
        WT_STORE();
        lds_signal(LW + FL_FREE + wid * 4, 1);
        const float sg = nt < 8 ? 1.0f : -1.0f;
        const unsigned ab = WS_AB + (((nt & 7) >> 1) * 512 + (nt >> 3) * 256 + mt * 16 + wm * 8 + wn * 4 + (kg & 1) * 2 + (kg >> 1)) * 2560 + ((nt & 1) * 80 + l15) * 16;
        UNROLL for (int u = 0; u < 5; ++u) {
            unsigned a0 = pack2(hc[0][u][0] * sg, hc[0][u][1] * sg), a1 = pack2(hc[0][u][2] * sg, hc[0][u][3] * sg);
            unsigned b0 = pack2(hc[1][u][0] * sg, hc[1][u][1] * sg), b1 = pack2(hc[1][u][2] * sg, hc[1][u][3] * sg);
            auto s0 = __builtin_amdgcn_permlane16_swap(a0, b0, false, false);
            auto s1 = __builtin_amdgcn_permlane16_swap(a1, b1, false, false);
            st128(W, u32x4{s0[0], s1[0], s0[1], s1[1]}, ab, u * 256);
        }
        publish(W, WS_F2 + (nt * 64 + mt * 4 + wid) * FS, epoch, lane);
        if (lane == 0) st32(W, epoch, WS_ACK + (blockIdx.x * 8 + wid) * 4, 0);
    }
    }
    STAMP(2);
    const int lane = __builtin_amdgcn_mbcnt_hi(~0u, __builtin_amdgcn_mbcnt_lo(~0u, 0u));
    const int r = lane & 31, hh = lane >> 5;
    const int l15 = lane & 15, kg = lane >> 4;
    const unsigned fl4 = (lane & 3) * 4;

    const int ksp = xcd >> 1, nt4 = idx >> 3;
    const int tt = mt * 2 + (ksp & 1), ct = nt4 * 2 + (ksp >> 1);
    const u32x4 dc = mkdesc(ksp < 2 ? cre : cim, 2048 * 4096), dws = mkdesc(ws, WS_END);
    const unsigned cs0 = mt * 128 * 4096 + (ksp & 1) * 2048;
    const unsigned as0 = WS_AB + (nt4 * 512 + ksp * 128) * 2560;

    if (wid >= 4) {
        const int head = xcd * 4 + (idx >> 3), qt = idx & 7;
        const int qh = wid & 1, kh = (wid >> 1) & 1;
        const int col0 = head * 20, odd = head & 1;
        const int cA = (col0 >> 3) + odd, cH = odd ? (col0 >> 3) : (col0 >> 3) + 2;
        const int q0 = qt * 256 + qh * 128;
        unsigned k1off = WS_QK + (80 + cA + hh) * CH + r * 16;
        unsigned k2off = hh ? OOB : WS_QK + (80 + cH) * CH + r * 16 + odd * 8;
        unsigned q2off = hh ? OOB : k2off - 80 * CH;
        unsigned k2or = hh ? 0x3F80u : 0u;
        const int dv = odd ? (r < 16 ? r + 4 : r - 16) : r;
        const unsigned voff = r < 20 ? WS_VT + hh * VROW + (col0 + dv) * 16 : (r == 20 ? oneoff : OOB);
        unsigned vstep = r < 20 ? 4 * VROW : 0;
        unsigned va = voff + kh * 32 * vstep, vb2 = va + (vstep >> 1);
        unsigned cpf = CPF == 1 ? (lane < 8 ? (lane >> 1) * 4096 + (lane & 1) * 128 : OOB) : CPF == 2 ? (lane < 8 ? lane * 128 : OOB) : (lane < 8 ? (lane >> 2) * 4096 + (lane & 3) * 128 : OOB);
        constexpr int NB = 4;
        u32x4 k1[NB], v0[NB], v1[NB]; u32x2 k2[NB];
        f32x16 o[4], s[2];
        u32x4 pa = u32x4{}, pd = u32x4{};
        f32x8 e8 = f32x8{};
        UNROLL for (int i = 0; i < 4; ++i) { o[i] = f32x16{}; PIN(o[i]); }
        v0[NB - 1] = u32x4{}; v1[NB - 1] = u32x4{};
        PIN(v0[NB - 1]); PIN(v1[NB - 1]); PIN(pa); PIN(pd); PIN(e8);
        PIN(k1off); PIN(k2off); PIN(q2off); PIN(k2or); PIN(va); PIN(vb2); PIN(vstep); PIN(cpf);
        WAITV(0);
        lds_post(LW + FL_FULL + gwv * 4, 1);
        const int nsa = (lane & 4) ? (col0 + 19) >> 6 : col0 >> 6;
        unsigned fkv = WS_F1 + ((10 + ((lane >> 3) & 1) * 10 + nsa) * 32 + kh * 16) * FS;
        STAMP2(23);
        __builtin_amdgcn_s_sleep(PSL);
        wait_flags1<SL1>(W, (lane & 16) ? WS_F1 + (nsa * 32 + (q0 >> 6) + (lane & 1)) * FS : fkv + (lane & 3) * FS, epoch);
        fkv += (lane & 1) * FS;
        STAMP(3);
        u32x4 q1[4]; u32x2 q2[4];
        UNROLL for (int i = 0; i < 4; ++i) {
            const int qr = q0 + ((i + 2 + 2 * kh) & 3) * 32;
            q2[i] = ld64(W, q2off, qr * 16);
            q1[i] = ld128(W, k1off - 80 * CH, qr * 16);
        }
        const int t0 = kh * 32;
        unsigned fnx = epoch;
        SBAR();
        UNROLL for (int b = 0; b < NB; ++b) {
            k2[b] = ld64(W, k2off, (t0 + b) * 512); k1[b] = ld128(W, k1off, (t0 + b) * 512); SBAR();
            if (b < NB - 1) { v0[b] = ld128(W, va, 0); v1[b] = ld128(W, vb2, 0); va += vstep; vb2 += vstep; SBAR(); }
        }
        const f32x16 zero16 = f32x16{};
        {
            f32x16 t[4];
            UNROLL for (int i = 0; i < 4; ++i) { t[i] = MFMA32K8(as4(k2[0]), as4(q2[i]), zero16); t[i] = MFMA32(as8(k1[0]), as8(q1[i]), t[i]); SBAR(); }
            k2[0][0] |= k2or;
            UNROLL for (int i = 0; i < 4; ++i) {
                float m = t[i][0];
                UNROLL for (int e = 1; e < 16; ++e) m = fmaxf(m, t[i][e]);
                auto mm = __builtin_amdgcn_permlane32_swap(__float_as_uint(m), __float_as_uint(m), false, false);
                m = fmaxf(__uint_as_float(mm[0]), __uint_as_float(mm[1]));
                q2[i][0] = hh ? pack2(-m, 0.f) & 0xFFFFu : q2[i][0];
                if (i == 0) {
                    s[0] = MFMA32K8(as4(k2[0]), as4(q2[0]), zero16);
                    s[0] = MFMA32(as8(k1[0]), as8(q1[0]), s[0]);
                }
            }
        }
        dma4(lane < 8 ? (lane >> 1) * 20480 + (lane & 1) * 128 : OOB, dws, WS_O + ((q0 + kh * 64) >> 4) * 20480 + cH * 256, lds0 + LDS_SINK);
        st32(W, epoch, lane == 0 ? WS_ACK + (blockIdx.x * 8 + wid) * 4 : OOB, 0);
        __builtin_amdgcn_s_setprio(1);
        STAMP(4);
        NOUNROLL for (int tb = 0; tb < 32; tb += NB) {
            if (__builtin_expect(!__all(fnx == epoch), 0)) wait_flags1<SL1>(W, fkv + (tb < 28 ? tb + 4 : 0) * (FS / 2), epoch);
            if (tb == 16) lds_wait(L + FL_FREE + fl4, 1);
            const unsigned fso = (tb < 24 ? tb + 8 : 0) * (FS / 2);
            SBAR();
            UNROLL for (int b = 0; b < NB; ++b) {
                const int bn = (b + 1) % NB, bp = (b + NB - 1) % NB;
                const unsigned kso = tb + b + NB < 32 ? (t0 + tb + b + NB) * 512 : OOB / 2;
                const unsigned vso = tb + b + NB - 1 < 32 ? 0 : OOB / 2;
                const unsigned cso = tb >= 16 ? OOB / 2 : CPF == 1 ? cs0 + (nt4 * 32 + gwv * 8 + (b & 1) * 4) * 4096 + ((tb + b) >> 1) * 256 :
                                     CPF == 2 ? cs0 + (nt4 * 32 + gwv * 8 + ((tb + b) & 7)) * 4096 + (tb >> 3) * 1024 :
                                                cs0 + (nt4 * 32 + gwv * 8 + b * 2) * 4096 + (tb >> 2) * 512;
                UNROLL for (int qi = 0; qi < 4; ++qi) {
                    const int c = qi & 1, n = c ^ 1, qn = (qi + 1) & 3, qp = (qi + 3) & 3;
                    const int kb = qi == 3 ? bn : b, vb = qi == 0 ? bp : b;
                    f32x16& SC = s[c]; f32x16& SN = s[n];
                    SN = MFMA32K8(as4(k2[kb]), as4(q2[qn]), zero16); SBAR();
                    SC[0] = EX2(SC[0]); SC[1] = EX2(SC[1]); SC[2] = EX2(SC[2]); SC[3] = EX2(SC[3]); SBAR();
                    pd[0] = pack2(e8[0], e8[1]); pd[1] = pack2(e8[2], e8[3]); PIN(SC); PIN(pd); SBAR();
                    SN = MFMA32(as8(k1[kb]), as8(q1[qn]), SN); SBAR();
                    if (qi == 2) { k2[b] = ld64(W, k2off, kso); k1[b] = ld128(W, k1off, kso); SBAR(); }
                    if (qi == 3 && b == 0) { fnx = ld32(W, fkv, fso); SBAR(); }
                    if (CPF && qi == 1) { dma4(cpf, dc, cso, lds0 + LDS_SINK); SBAR(); }
                    SC[4] = EX2(SC[4]); SC[5] = EX2(SC[5]); SC[6] = EX2(SC[6]); SC[7] = EX2(SC[7]); SBAR();
                    pd[2] = pack2(e8[4], e8[5]); pd[3] = pack2(e8[6], e8[7]); PIN(SC); PIN(pd); SBAR();
                    o[qp] = MFMA32(as8(v0[vb]), as8(pa), o[qp]); SBAR();
                    e8[0] = EX2(SC[8]); e8[1] = EX2(SC[9]); e8[2] = EX2(SC[10]); e8[3] = EX2(SC[11]); SBAR();
                    pa[0] = pack2(SC[0], SC[1]); pa[1] = pack2(SC[2], SC[3]);
                    if (qi == 2) { k2[bn][0] |= k2or; PIN(k2[bn]); }
                    if (qi == 1) { va += vstep; vb2 += vstep; PIN(va); PIN(vb2); }
                    PIN(e8); PIN(pa); SBAR();
                    o[qp] = MFMA32(as8(v1[vb]), as8(pd), o[qp]); SBAR();
                    if (qi == 0) { v0[bp] = ld128(W, va, vso); v1[bp] = ld128(W, vb2, vso); SBAR(); }
                    e8[4] = EX2(SC[12]); e8[5] = EX2(SC[13]); e8[6] = EX2(SC[14]); e8[7] = EX2(SC[15]); SBAR();
                    pa[2] = pack2(SC[4], SC[5]); pa[3] = pack2(SC[6], SC[7]); PIN(e8); PIN(pa); SBAR();
                }
            }
        }
        __builtin_amdgcn_s_setprio(3);
        UNROLL for (int e = 0; e < 4; ++e) pd[e] = pack2(e8[2 * e], e8[2 * e + 1]);
        o[3] = MFMA32(as8(v0[NB - 1]), as8(pa), o[3]);
        o[3] = MFMA32(as8(v1[NB - 1]), as8(pd), o[3]);
        SBAR();
        STAMP(5);
        const int f0 = q0 + kh * 64;
        UNROLL for (int j = 0; j < 2; ++j) {
            const lds_p ex = LW + LDS_EX + (gwv * 2 + j) * EXB;
            *(__attribute__((address_space(3))) u32x4*)(ex + lane * 16) =
                u32x4{pack2(o[j][0], o[j][1]), pack2(o[j][2], o[j][3]), pack2(o[j][4], o[j][5]), pack2(o[j][6], o[j][7])};
            *(__attribute__((address_space(3))) u32x2*)(ex + 1024 + lane * 8) =
                hh ? u32x2{__float_as_uint(o[j][8]), q2[j][0] << 16} : u32x2{pack2(o[j][8], o[j][9]), pack2(o[j][10], o[j][11])};
        }
        lds_signal(LW + FL_EX + gwv * 4);
        float n1[2], l1[2];
        UNROLL for (int j = 0; j < 2; ++j) {
            auto nn = __builtin_amdgcn_permlane32_swap(q2[2 + j][0] << 16, q2[2 + j][0] << 16, false, false);
            auto ll = __builtin_amdgcn_permlane32_swap(__float_as_uint(o[2 + j][8]), __float_as_uint(o[2 + j][8]), false, false);
            n1[j] = __uint_as_float(nn[1]); l1[j] = __uint_as_float(ll[1]);
        }
        const unsigned orow = WS_O + ((f0 >> 4) + (r >> 4)) * 20480 + (r & 15) * 16;
        const unsigned oa = orow + (cA + hh) * 256, ob = hh ? OOB : orow + cH * 256 + odd * 8;
        lds_wait(L + FL_EX + (gwv ^ 2) * 4);
        STAMP2(22);
        bool ok = true;
        u32x4 ra[2]; u32x2 rb[2];
        UNROLL for (int j = 0; j < 2; ++j) {
            const lds_cp rx = L + LDS_EX + ((gwv ^ 2) * 2 + j) * EXB;
            const u32x4 a = *(const __attribute__((address_space(3))) u32x4*)(rx + lane * 16);
            const u32x2 c = *(const __attribute__((address_space(3))) u32x2*)(rx + 1024 + r * 8);
            const u32x2 d = *(const __attribute__((address_space(3))) u32x2*)(rx + 1280 + r * 8);
            const float n2 = __uint_as_float(d[1]), l2 = __uint_as_float(d[0]), nm = fminf(n1[j], n2);
            float w1 = EX2(nm - n1[j]), w2 = EX2(nm - n2);
            const float l = l1[j] * w1 + l2 * w2, rl = __builtin_amdgcn_rcpf(l);
            w1 *= rl; w2 *= rl;
            float f[12];
            UNROLL for (int e = 0; e < 4; ++e) { f[2 * e] = o[2 + j][2 * e] * w1 + bflo(a[e]) * w2; f[2 * e + 1] = o[2 + j][2 * e + 1] * w1 + bfhi(a[e]) * w2; }
            UNROLL for (int e = 0; e < 2; ++e) { f[8 + 2 * e] = o[2 + j][8 + 2 * e] * w1 + bflo(c[e]) * w2; f[9 + 2 * e] = o[2 + j][9 + 2 * e] * w1 + bfhi(c[e]) * w2; }
            float sa = 0.f;
            UNROLL for (int e = 0; e < 12; ++e) sa += fabsf(f[e]);
            ok = ok && l1[j] < 1e30f && l2 < 1e30f && l > 1e-18f && sa < 3e38f;
            auto s0 = __builtin_amdgcn_permlane32_swap(pack2(f[0], f[1]), pack2(f[4], f[5]), false, false);
            auto s1 = __builtin_amdgcn_permlane32_swap(pack2(f[2], f[3]), pack2(f[6], f[7]), false, false);
            ra[j] = u32x4{s0[0], s1[0], s0[1], s1[1]};
            rb[j] = u32x2{pack2(f[8], f[9]), pack2(f[10], f[11])};
        }
        if (__builtin_expect(__all(ok), 1)) {
            UNROLL for (int j = 0; j < 2; ++j) { st128(W, ra[j], oa + j * 40960, 0); st64(W, rb[j], ob + j * 40960, 0); }
        } else {
            wait_flags1<SL1>(W, WS_F1 + ((10 + hh * 10 + (col0 >> 6)) * 32 + r) * FS, epoch);
            wait_flags1<SL1>(W, WS_F1 + ((10 + hh * 10 + ((col0 + 19) >> 6)) * 32 + r) * FS, epoch);
            NOUNROLL for (int j = 0; j < 2; ++j) {
                const u32x2 z2 = ld64(W, q2off, (f0 + j * 32) * 16);
                const u32x4 z1 = ld128(W, k1off - 80 * CH, (f0 + j * 32) * 16);
                float m = -3e38f;
                f32x16 oo = f32x16{};
                NOUNROLL for (int t = 0; t < 64; ++t) {
                    const u32x4 a1 = ld128(W, k1off, t * 512); const u32x2 a2 = ld64(W, k2off, t * 512);
                    const u32x4 w0 = ld128(W, voff + t * vstep, 0), w1 = ld128(W, voff + t * vstep + (vstep >> 1), 0);
                    f32x16 z = MFMA32K8(as4(a2), as4(z2), zero16);
                    z = MFMA32(as8(a1), as8(z1), z);
                    float mx = z[0];
                    UNROLL for (int e = 1; e < 16; ++e) mx = fmaxf(mx, z[e]);
                    auto mm = __builtin_amdgcn_permlane32_swap(__float_as_uint(mx), __float_as_uint(mx), false, false);
                    const float mn = fmaxf(m, fmaxf(__uint_as_float(mm[0]), __uint_as_float(mm[1])));
                    const float al = EX2(m - mn);
                    m = mn;
                    UNROLL for (int e = 0; e < 16; ++e) oo[e] *= al;
                    u32x4 p0, p1;
                    UNROLL for (int e = 0; e < 4; ++e) { p0[e] = pack2(EX2(z[2 * e] - m), EX2(z[2 * e + 1] - m)); p1[e] = pack2(EX2(z[8 + 2 * e] - m), EX2(z[9 + 2 * e] - m)); }
                    oo = MFMA32(as8(w0), as8(p0), oo);
                    oo = MFMA32(as8(w1), as8(p1), oo);
                }
                auto l2 = __builtin_amdgcn_permlane32_swap(__float_as_uint(oo[8]), __float_as_uint(oo[8]), false, false);
                const float sc = 1.0f / __uint_as_float(l2[1]);
                auto s0 = __builtin_amdgcn_permlane32_swap(pack2(oo[0] * sc, oo[1] * sc), pack2(oo[4] * sc, oo[5] * sc), false, false);
                auto s1 = __builtin_amdgcn_permlane32_swap(pack2(oo[2] * sc, oo[3] * sc), pack2(oo[6] * sc, oo[7] * sc), false, false);
                st128(W, u32x4{s0[0], s1[0], s0[1], s1[1]}, oa + j * 40960, 0);
                st64(W, u32x2{pack2(oo[8] * sc, oo[9] * sc), pack2(oo[10] * sc, oo[11] * sc)}, ob + j * 40960, 0);
            }
        }
        STAMP(6);
        publish(W, WS_F3 + ((f0 >> 6) * 32 + head) * FS, epoch, lane);
        STAMP(7);
        if (blockIdx.x == 0 && wid == 4) {
            NOUNROLL for (unsigned spins = 0; spins < SPIN_CAP; ++spins) {
                asm volatile("" ::: "memory");
                bool ok = true;
                UNROLL for (int i = 0; i < 8; ++i) {
                    const u32x4 f = ld128(W, WS_ACK + i * 1024 + lane * 16, 0);
                    ok = ok && f[0] == epoch && f[1] == epoch && f[2] == epoch && f[3] == epoch;
                }
                if (__all(ok)) break;
                __builtin_amdgcn_s_sleep(32);
            }
            if (lane == 0) st32(W, epoch, WS_EP, 0);
        }
        return;
    }

    __builtin_amdgcn_s_setprio(2);
    u32x4 wf[5][5];
    {
        unsigned coff[2]; const unsigned lin = lane * 16;
        UNROLL for (int i = 0; i < 2; ++i) coff[i] = (lane >> 3) * 4096 + (((lane & 7) ^ ((4 * i + (lane >> 4)) & 7)) << 4);
#define SQ_DC(n, slot, i) dma16(coff[(i) & 1], dc, cs0 + gwv * 131072 + (i) * 32768 + (n) * 128, lds0 + (slot) * SQ_STAGE + gwv * 4096 + (i) * 1024)
        const int srot = SROT ? (idx & 7) * 2 + (idx >> 3) * 4 + ((idx >> 3) & 1) : 0;
#define SST(n) (((n) + srot) & 15)
#define SQ_DC4(n, slot) do { const unsigned c_ = cs0 + gwv * 131072 + SST(n) * 128; \
            dma16x4(coff[0], coff[1], dc, c_, c_ + 31744, c_ + 63488, c_ + 95232, lds0 + (slot) * SQ_STAGE + gwv * 4096); } while (0)
#define SQ_DA5(n, slot) do { dma16sx4(lin, dws, as0 + SST(n) * 20480 + gwv * 5120, lds0 + (slot) * SQ_STAGE + 16384 + gwv * 5120); SQ_DA(n, slot, 4); } while (0)
#define SQ_DA(n, slot, i) dma16s(lin, dws, as0 + SST(n) * 20480 + gwv * 5120 + (i) * 1024, lds0 + (slot) * SQ_STAGE + 16384 + gwv * 5120 + (i) * 1024)
        const int wm = wid & 1, wn = wid >> 1;
        f32x4 acc[4][5];
        UNROLL for (int i = 0; i < 4; ++i) UNROLL for (int u = 0; u < 5; ++u) { acc[i][u] = f32x4{}; PIN(acc[i][u]); }
        unsigned ra[2];
        UNROLL for (int ks = 0; ks < 2; ++ks) ra[ks] = wm * 8192 + l15 * 128 + (((4 * ks + kg) ^ ((l15 >> 1) & 7)) << 4);
        const unsigned rb = 16384 + kg * 2560 + (wn * 80 + l15) * 16;
        s16x8 fc[2][4], fa[2][5];
        UNROLL for (int i = 0; i < 4; ++i) fc[1][i] = s16x8{};
        UNROLL for (int u = 0; u < 5; ++u) fa[1][u] = s16x8{};
        if (wid == 0) {
            const unsigned f2 = WS_F2 + (((ksp >> 1) * 8 + nt4 * 2 + hh) * 64 + (ksp & 1) * 32 + r) * FS;
            __builtin_amdgcn_s_sleep(16);
            wait_flags1<12>(W, f2, epoch);
            lds_wait(L + FL_FREE + fl4, 1);
            lds_post(LW + FL_FIN, 1);
        } else {
            lds_wait<LSL>(L + FL_FIN, 1);
        }
        STAMP(3);
        UNROLL for (int n = 0; n < 2; ++n) { SQ_DC4(n, n); SQ_DA5(n, n); }
        const lds_p pw = LW + (lane == 0 ? FL_SFULL + gwv * 4 : LDS_SINK + lane * 4);
        const lds_cp pr = L + FL_SFULL + fl4;
        const unsigned wfo = WS_WOT + (ct * 4 + gwv) * 25600 + lane * 16;
#define SQ_MF(u) do { UNROLL for (int i = 0; i < 4; ++i) acc[i][u] = MFMA16(fa[c][u], fc[c][i], acc[i][u]); SBAR(); } while (0)
#define SQ_RC(i) fc[n][i] = lds128(sr[ks] + (i) * 2048)
#define SQ_RA(u) fa[n][u] = lds128(sp + ks * 10240 + (u) * 256)
#define SQ_STG(DMA) do { \
            lds_cp sp = L + (j & 3) * SQ_STAGE + rb; lds_cp sr[2]; \
            UNROLL for (int ks = 0; ks < 2; ++ks) { sr[ks] = L + (j & 3) * SQ_STAGE + ra[ks]; PIN(sr[ks]); } \
            unsigned pv = j + 2, fl = 0; \
            PIN(sp); PIN(pv); SBAR(); \
            __builtin_amdgcn_s_setprio(GPRIO); \
            const int sf = (j + 2) & 3; \
            UNROLL for (int ks = 0; ks < 2; ++ks) { \
                const int n = ks, c = n ^ 1; \
                SQ_RC(0); SQ_RC(1); SQ_RC(2); SBAR(); SQ_MF(0); \
                SQ_RC(3); SQ_RA(0); SQ_RA(1); SBAR(); SQ_MF(1); \
                SQ_RA(2); SQ_RA(3); SQ_RA(4); \
                if (ks == 0) { WAITV(0); lds_post(pw, pv); } \
                SBAR(); SQ_MF(2); \
                if (DMA && ks == 0) { SQ_DC4(j + 2, sf); SBAR(); } \
                if (ks == 1) { fl = lds_peek(pr); SBAR(); } \
                SQ_MF(3); \
                if (DMA && ks == 0) { SQ_DA5(j + 2, sf); SBAR(); } \
                SQ_MF(4); \
            } \
            __builtin_amdgcn_s_setprio(3); \
            if (__builtin_expect(!__all(fl >= pv), 0)) lds_wait<LS2>(pr, pv); } while (0)
        int j = 0;
        WAITV(9); lds_post(pw, 1); lds_wait(pr, 1);
        NOUNROLL for (; j < 14; ++j) SQ_STG(true);
        NOUNROLL for (; j < 16; ++j) SQ_STG(false);
        UNROLL for (int u = 0; u < 5; ++u) UNROLL for (int i = 0; i < 4; ++i) acc[i][u] = MFMA16(fa[1][u], fc[1][i], acc[i][u]);
        __builtin_amdgcn_s_setprio(3);
        STAMP(4);
        if (wid == ksp) {
            UNROLL for (int i = 0; i < 4; ++i) UNROLL for (int u = 0; u < 5; ++u) stsf4(LW + 2 * SQ_STAGE + (i * 5 + u) * 1024 + lane * 16, acc[i][u]);
            const int px = r >> 2;
            const unsigned fw = WS_F2 + (((px >> 1) * 4 + hh) * 64 + ((px & 1) * 8 + ct) * 4 + (r & 3)) * FS;
            const unsigned fg = WS_F2 + (15 * 64 + (tt >> 1) * 4 + (r & 3)) * FS;
            wait_flags4<12>(W, fw, fw + 128 * FS, fg, fg, epoch);
        } else {
            const unsigned yb = WS_Y + ((((mt * 2 + wm) * 8 + nt4 * 2 + wn) * 4 + ksp) * 4) * 2560;
            UNROLL for (int i = 0; i < 4; ++i) {
                UNROLL for (int u = 0; u < 4; u += 2)
                    st128(W, u32x4{pack2(acc[i][u][0], acc[i][u][1]), pack2(acc[i][u][2], acc[i][u][3]), pack2(acc[i][u + 1][0], acc[i][u + 1][1]), pack2(acc[i][u + 1][2], acc[i][u + 1][3])},
                          yb + lane * 16, i * 2560 + u * 512);
                st64(W, u32x2{pack2(acc[i][4][0], acc[i][4][1]), pack2(acc[i][4][2], acc[i][4][3])}, yb + i * 2560 + 2048 + lane * 8, 0);
            }
            publish(W, WS_F4 + (((mt * 2 + wm) * 8 + nt4 * 2 + wn) * 4 + ksp) * FS, epoch, lane);
        }
        lds_signal(pw, 18); lds_wait<LSL>(pr, 18);
        UNROLL for (int st = 0; st < 5; ++st) UNROLL for (int u = 0; u < 5; ++u) wf[st][u] = ld128(W, wfo, (st * 5 + u) * 1024);
        STAMP(5);
    }
    const int t0 = tt * 64;
    const float gt = __uint_as_float(ld32(W, WS_G + (t0 + gwv * 16 + l15) * 4, 0));
    WAITV(0);
    lds_wait<LSL>(L + FL_EX + fl4, 1);
    STAMP(6);
    wait_flags1<SL3>(W, WS_F4 + ((tt * 8 + ct) * 4 + ((ksp + 1 + (r % 3)) & 3)) * FS, epoch);
    u32x4 ya[3][2]; u32x2 yc[3];
    UNROLL for (int p = 0; p < 3; ++p) {
        const unsigned yo = WS_Y + (((tt * 8 + ct) * 4 + ((ksp + 1 + p) & 3)) * 4 + gwv) * 2560;
        ya[p][0] = ld128(W, lane * 16, yo); ya[p][1] = ld128(W, lane * 16 + 1024, yo); yc[p] = ld64(W, lane * 8 + 2048, yo);
    }
    const float h = 1.0f - gt;
    f32x4 ys[5];
    UNROLL for (int u = 0; u < 5; ++u) ys[u] = ldsf4(L + 2 * SQ_STAGE + (gwv * 5 + u) * 1024 + lane * 16);
    SBAR();
    wait_flags1<SL3>(W, WS_F3 + (tt * 32 + gwv * 8 + (lane & 7)) * FS, epoch);
    STAMP(8);
    const unsigned boff = WS_O + tt * 81920 + gwv * 5120 + lane * 16;
    u32x4 bf[4][5];
    UNROLL for (int d = 0; d < 4; ++d)
        UNROLL for (int st = 0; st < 5; ++st) bf[d][st] = ld128(W, boff + st * 1024, ((gwv + d + 1) & 3) * 20480);
    SBAR();
    UNROLL for (int u = 0; u < 5; ++u) {
        UNROLL for (int p = 0; p < 3; ++p) {
            const unsigned w0 = u < 4 ? ya[p][(u >> 1) & 1][(u & 1) * 2] : yc[p][0], w1 = u < 4 ? ya[p][(u >> 1) & 1][(u & 1) * 2 + 1] : yc[p][1];
            ys[u][0] += bflo(w0); ys[u][1] += bfhi(w0); ys[u][2] += bflo(w1); ys[u][3] += bfhi(w1);
        }
        ys[u] *= h; PIN(ys[u]);
    }
    SBAR();
    f32x4 res[5];
    UNROLL for (int d = 0; d < 4; ++d) {
        UNROLL for (int u = 0; u < 5; ++u) res[u] = f32x4{};
        UNROLL for (int st = 0; st < 5; ++st) UNROLL for (int u = 0; u < 5; ++u) res[u] = MFMA16(as8(wf[st][u]), as8(bf[d][st]), res[u]);
        if (d < 3) { UNROLL for (int u = 0; u < 5; ++u) stsf4(LW + ((gwv + d + 1) & 3) * GW_LDS + (d * 5 + u) * 1024 + lane * 16, res[u]); }
        if (d == 2) lds_signal(LW + FL_RED + gwv * 4);
        SBAR();
    }
    STAMP(9);
    lds_wait(L + FL_RED + fl4);
    UNROLL for (int d = 0; d < 3; ++d) UNROLL for (int u = 0; u < 5; ++u) res[u] += ldsf4(L + gwv * GW_LDS + (d * 5 + u) * 1024 + lane * 16);
    UNROLL for (int u = 0; u < 5; ++u) res[u] = res[u] * gt + ys[u];
    char* dst = out + (size_t)(t0 + gwv * 16 + l15) * 1280 + ct * 160 + kg * 8;
    const rsrc_t RO = __builtin_amdgcn_make_buffer_rsrc(out, 0, 2048 * 1280, 0x00020000);
    const unsigned oo = (t0 + gwv * 16 + l15) * 1280 + ct * 160;
    UNROLL for (int u = 0; u < 4; u += 2) {
        auto s0 = __builtin_amdgcn_permlane16_swap(pack2(res[u][0], res[u][1]), pack2(res[u + 1][0], res[u + 1][1]), false, false);
        auto s1 = __builtin_amdgcn_permlane16_swap(pack2(res[u][2], res[u][3]), pack2(res[u + 1][2], res[u + 1][3]), false, false);
        __builtin_amdgcn_raw_buffer_store_b128(u32x4{s0[0], s1[0], s0[1], s1[1]}, RO, oo + (u + (kg & 1)) * 32 + (kg >> 1) * 16, 0, OUTM == 1 ? 0 : OUTM == 2 ? 16 : 2);
    }
    __builtin_amdgcn_raw_buffer_store_b64(u32x2{pack2(res[4][0], res[4][1]), pack2(res[4][2], res[4][3])}, RO, oo + 128 + kg * 8, 0, OUTM == 1 ? 0 : OUTM == 2 ? 16 : 2);
    STAMP(10);
}

extern "C" void kernel_launch(void* const* d_in, const int* in_sizes, int n_in, void* d_out, int out_size,
                              void* d_ws, size_t ws_size, hipStream_t stream) {
    (void)hipFuncSetAttribute((const void*)fused, hipFuncAttributeMaxDynamicSharedMemorySize, LDS_BYTES);
    fused<<<256, 512, LDS_BYTES, stream>>>((const char*)d_in[0], (const char*)d_in[1], (const char*)d_in[2], (const char*)d_in[4],
                                           (const char*)d_in[5], (const char*)d_in[6], (const char*)d_in[7], (const char*)d_in[8],
                                           (char*)d_ws + WS_BASE(ws_size), (char*)d_out);
}
```

```cpp
#include <hip/hip_runtime.h>
#include <hip/hip_bf16.h>
#include <stdint.h>

typedef short s16x8 __attribute__((ext_vector_type(8)));
typedef short s16x4 __attribute__((ext_vector_type(4)));
typedef float f32x16 __attribute__((ext_vector_type(16)));
typedef float f32x8 __attribute__((ext_vector_type(8)));
typedef float f32x4 __attribute__((ext_vector_type(4)));
typedef unsigned u32x4 __attribute__((ext_vector_type(4)));
typedef unsigned u32x2 __attribute__((ext_vector_type(2)));
typedef __bf16 bf16x2_t __attribute__((ext_vector_type(2)));
typedef __attribute__((address_space(3))) const char* lds_cp;
typedef __attribute__((address_space(3))) char* lds_p;
typedef __amdgpu_buffer_rsrc_t rsrc_t;

#define DI __device__ __forceinline__
#define UNROLL _Pragma("unroll")
#define NOUNROLL _Pragma("unroll 1")
#define MFMA32(a, b, c) __builtin_amdgcn_mfma_f32_32x32x16_bf16((a), (b), (c), 0, 0, 0)
#define MFMA32K8(a, b, c) __builtin_amdgcn_mfma_f32_32x32x8bf16_1k((a), (b), (c), 0, 0, 0)
#define MFMA16(a, b, c) __builtin_amdgcn_mfma_f32_16x16x32_bf16((a), (b), (c), 0, 0, 0)
#define SBAR() __builtin_amdgcn_sched_barrier(0)
#define PIN(x) asm volatile("" : "+v"(x))
#define EX2(x) __builtin_amdgcn_exp2f(x)
#define SC1 16

constexpr unsigned CH = 2048 * 16;
constexpr unsigned WS_QK = 0;
constexpr unsigned WS_O = 160 * CH;
constexpr unsigned WS_Y = WS_O + 80 * CH;
constexpr unsigned ABROW = 640 * 16;
constexpr unsigned WS_AB = WS_Y + 320 * CH;
constexpr unsigned VROW = 10240;
constexpr unsigned WS_VT = WS_AB + 512 * ABROW;
constexpr unsigned WS_WOT = WS_VT + 256 * VROW;
constexpr unsigned WS_G = WS_WOT + 80 * ABROW;
constexpr unsigned FS = 128;
constexpr unsigned WS_ACK = WS_G + 8192;
constexpr unsigned WS_EP = WS_ACK + 8192;
constexpr unsigned WS_F1 = WS_EP + 4096;
constexpr unsigned WS_F2 = WS_F1 + 1024 * FS;
constexpr unsigned WS_F3 = WS_F2 + 1024 * FS;
constexpr unsigned WS_F4 = WS_F3 + 1024 * FS;
constexpr unsigned WS_ONE = WS_F4 + 1024 * FS;
constexpr unsigned WS_END = WS_ONE + 2048 * 128;
constexpr unsigned OOB = 0x80000000u;

constexpr int P1_STAGE = 32768;
constexpr int SQ_STAGE = 36864;
constexpr int GW_LDS = 15360;
constexpr int EXB = 1536;
constexpr int LDS_EX = 4 * SQ_STAGE;
constexpr int LDS_X9 = 4 * P1_STAGE;
constexpr int LDS_GW = LDS_X9 + 20480;
constexpr int LDS_WT = LDS_GW + 2048;
constexpr int LDS_FL = LDS_EX + 8 * EXB;
constexpr int LDS_SINK = LDS_FL + 256;
constexpr int LDS_BYTES = LDS_SINK + 256;
static_assert(LDS_BYTES <= 163840 && LDS_FL >= LDS_WT + 4096, "lds");
constexpr int FL_FULL = LDS_FL, FL_EX = LDS_FL + 16, FL_SFULL = LDS_FL + 32, FL_FIN = LDS_FL + 48, FL_RED = LDS_FL + 64,
              FL_FREE = LDS_FL + 80;
constexpr int CODE_LINES = 288;


DI unsigned pack2(float a, float b) {
    bf16x2_t v = {(__bf16)a, (__bf16)b};
    return __builtin_bit_cast(unsigned, v);
}
DI float bflo(unsigned u) { return __uint_as_float(u << 16); }
DI float bfhi(unsigned u) { return __uint_as_float(u & 0xFFFF0000u); }
DI s16x8 as8(u32x4 v) { return __builtin_bit_cast(s16x8, v); }
DI s16x4 as4(u32x2 v) { return __builtin_bit_cast(s16x4, v); }

DI u32x4 mkdesc(const void* p, unsigned bytes) {
    const unsigned long long a = (unsigned long long)p;
    return u32x4{(unsigned)a, (unsigned)(a >> 32) & 0xFFFFu, bytes, 0x00020000u};
}
DI void dma16(unsigned voff, u32x4 desc, unsigned soff, unsigned ldsaddr) {
    unsigned keep;
    asm volatile("s_mov_b32 %0, m0\n\ts_mov_b32 m0, %4\n\ts_nop 0\n\tbuffer_load_dwordx4 %1, %2, %3 offen lds\n\ts_mov_b32 m0, %0"
                 : "=&s"(keep) : "v"(voff), "s"(desc), "s"(soff), "s"(ldsaddr) : "memory");
}
DI void dma16s(unsigned voff, u32x4 desc, unsigned soff, unsigned ldsaddr) {
    unsigned keep;
    asm volatile("s_mov_b32 %0, m0\n\ts_mov_b32 m0, %4\n\ts_nop 0\n\tbuffer_load_dwordx4 %1, %2, %3 offen sc1 lds\n\ts_mov_b32 m0, %0"
                 : "=&s"(keep) : "v"(voff), "s"(desc), "s"(soff), "s"(ldsaddr) : "memory");
}
DI void dma16x4(unsigned v0, unsigned v1, u32x4 desc, unsigned s0, unsigned s1, unsigned s2, unsigned s3, unsigned ldsaddr) {
    unsigned keep;
    asm volatile("s_mov_b32 %0, m0\n\ts_mov_b32 m0, %8\n\ts_nop 0\n\t"
                 "buffer_load_dwordx4 %1, %3, %4 offen lds\n\tbuffer_load_dwordx4 %2, %3, %5 offen offset:1024 lds\n\t"
                 "buffer_load_dwordx4 %1, %3, %6 offen offset:2048 lds\n\tbuffer_load_dwordx4 %2, %3, %7 offen offset:3072 lds\n\ts_mov_b32 m0, %0"
                 : "=&s"(keep) : "v"(v0), "v"(v1), "s"(desc), "s"(s0), "s"(s1), "s"(s2), "s"(s3), "s"(ldsaddr) : "memory");
}
DI void dma16sx4(unsigned voff, u32x4 desc, unsigned soff, unsigned ldsaddr) {
    unsigned keep;
    asm volatile("s_mov_b32 %0, m0\n\ts_mov_b32 m0, %4\n\ts_nop 0\n\t"
                 "buffer_load_dwordx4 %1, %2, %3 offen sc1 lds\n\tbuffer_load_dwordx4 %1, %2, %3 offen offset:1024 sc1 lds\n\t"
                 "buffer_load_dwordx4 %1, %2, %3 offen offset:2048 sc1 lds\n\tbuffer_load_dwordx4 %1, %2, %3 offen offset:3072 sc1 lds\n\ts_mov_b32 m0, %0"
                 : "=&s"(keep) : "v"(voff), "s"(desc), "s"(soff), "s"(ldsaddr) : "memory");
}
DI void dma4(unsigned voff, u32x4 desc, unsigned soff, unsigned ldsaddr) {
    unsigned keep;
    asm volatile("s_mov_b32 %0, m0\n\ts_mov_b32 m0, %4\n\ts_nop 0\n\tbuffer_load_dword %1, %2, %3 offen sc1 lds\n\ts_mov_b32 m0, %0"
                 : "=&s"(keep) : "v"(voff), "s"(desc), "s"(soff), "s"(ldsaddr) : "memory");
}
#define WAITV(n) asm volatile("s_waitcnt vmcnt(" #n ")" ::: "memory")
#define WAITL() asm volatile("s_waitcnt lgkmcnt(0)" ::: "memory")
#define BARRIER() asm volatile("s_waitcnt lgkmcnt(0)\n\ts_barrier" ::: "memory")
#define BARRIER_NOWAIT() asm volatile("s_barrier" ::: "memory")

DI s16x8 lds128(lds_cp p) { return *(const __attribute__((address_space(3))) s16x8*)p; }
DI f32x4 ldsf4(lds_cp p) { return *(const __attribute__((address_space(3))) f32x4*)p; }
DI void stsf4(lds_p p, f32x4 v) { *(__attribute__((address_space(3))) f32x4*)p = v; }
DI s16x4 ldstr(lds_cp p) {
    return __builtin_bit_cast(s16x4, __builtin_amdgcn_ds_read_tr16_b64_v4i16((__attribute__((address_space(3))) s16x4*)p));
}
DI s16x8 cat8(s16x4 a, s16x4 b) { return __builtin_shufflevector(a, b, 0, 1, 2, 3, 4, 5, 6, 7); }
DI u32x4 ld128(rsrc_t r, unsigned voff, unsigned soff) { return __builtin_amdgcn_raw_buffer_load_b128(r, voff, soff, SC1); }
DI u32x2 ld64(rsrc_t r, unsigned voff, unsigned soff) { return __builtin_amdgcn_raw_buffer_load_b64(r, voff, soff, SC1); }
DI unsigned ld32(rsrc_t r, unsigned voff, unsigned soff) { return __builtin_amdgcn_raw_buffer_load_b32(r, voff, soff, SC1); }
DI void st128(rsrc_t r, u32x4 v, unsigned voff, unsigned soff) { __builtin_amdgcn_raw_buffer_store_b128(v, r, voff + soff, 0, SC1); }
DI void st64(rsrc_t r, u32x2 v, unsigned voff, unsigned soff) { __builtin_amdgcn_raw_buffer_store_b64(v, r, voff, soff, SC1); }
DI void st32(rsrc_t r, unsigned v, unsigned voff, unsigned soff) { __builtin_amdgcn_raw_buffer_store_b32(v, r, voff, soff, SC1); }

constexpr unsigned SPIN_CAP = 200000;
DI void publish(rsrc_t w, unsigned flag_off, unsigned epoch, int lane) {
    WAITV(0);
    if (lane == 0) st32(w, epoch, flag_off, 0);
}
template <int SL>
DI void wait_flags4(rsrc_t w, unsigned o0, unsigned o1, unsigned o2, unsigned o3, unsigned epoch) {
    NOUNROLL for (unsigned spins = 0; spins < SPIN_CAP; ++spins) {
        asm volatile("" ::: "memory");
        const unsigned a = ld32(w, o0, 0), b = ld32(w, o1, 0), c = ld32(w, o2, 0), d = ld32(w, o3, 0);
        if (__all(a == epoch && b == epoch && c == epoch && d == epoch)) break;
        __builtin_amdgcn_s_sleep(SL);
    }
    asm volatile("" ::: "memory");
}
template <int SL>
DI void wait_flags1(rsrc_t w, unsigned o0, unsigned epoch) {
    NOUNROLL for (unsigned spins = 0; spins < SPIN_CAP; ++spins) {
        asm volatile("" ::: "memory");
        const unsigned a = ld32(w, o0, 0);
        if (__all(a == epoch)) break;
        __builtin_amdgcn_s_sleep(SL);
    }
    asm volatile("" ::: "memory");
}
DI void lds_signal(lds_p p, unsigned val = 1u) {
    WAITL();
    *(volatile __attribute__((address_space(3))) unsigned*)p = val;
}
DI void lds_post(lds_p p, unsigned val) { *(volatile __attribute__((address_space(3))) unsigned*)p = val; }
DI unsigned lds_peek(lds_cp p) { return *(const volatile __attribute__((address_space(3))) unsigned*)p; }
template <int SL = 1>
DI void lds_wait(lds_cp p, unsigned val = 1u) {
    NOUNROLL for (unsigned spins = 0; spins < 4 * SPIN_CAP; ++spins) {
        const unsigned v = *(const volatile __attribute__((address_space(3))) unsigned*)p;
        if (__all(v >= val)) break;
        __builtin_amdgcn_s_sleep(SL);
    }
    asm volatile("" ::: "memory");
}

__global__ __launch_bounds__(512, 2) void fused(const char* __restrict__ x, const char* __restrict__ wqkv,
                                                 const char* __restrict__ wo, const char* __restrict__ cre,
                                                 const char* __restrict__ cim, const char* __restrict__ wr,
                                                 const char* __restrict__ wi, const char* __restrict__ gw,
                                                 char* ws, char* __restrict__ out) {
    extern __shared__ __attribute__((aligned(16))) char smem[];
    unsigned long long pc; asm volatile("s_getpc_b64 %0" : "=s"(pc));
    const int tid = threadIdx.x;
    const int wid = __builtin_amdgcn_readfirstlane(tid >> 6);
    const int gwv = wid & 3;
    const int xcd = blockIdx.x & 7, idx = blockIdx.x >> 3;
    const unsigned lds0 = (unsigned)(uintptr_t)smem;
    const lds_cp L = (lds_cp)smem;
    const lds_p LW = (lds_p)smem;
    const u32x4 dx = mkdesc(x, 2048 * 1280);
    const int mt = (xcd & 1) * 8 + (idx & 7), nt = (xcd >> 1) * 4 + (idx >> 3);
    const u32x4 dw = mkdesc(wqkv, 640 * 3840);
    const rsrc_t W = __builtin_amdgcn_make_buffer_rsrc(ws, 0, WS_END, 0x00020000);
    const unsigned oneoff = WS_ONE + (blockIdx.x * 8 + wid) * 128;
    unsigned epoch;
    {
    const int lane = tid & 63;
    unsigned roff[2];
    UNROLL for (int i = 0; i < 2; ++i) roff[i] = (lane >> 3) * 1280 + (((lane & 7) ^ ((4 * i + (lane >> 4)) & 7)) << 4);
    unsigned woff[2];
    UNROLL for (int i = 0; i < 2; ++i) woff[i] = nt == 15 ? OOB : (lane >> 3) * 3840 + (((lane & 7) ^ (((lane >> 4) & 1) << 2) ^ (i << 1)) << 4);
#define P1_DX(st, p, par) dma16(roff[par], dx, (mt * 128 + (p) * 8) * 1280 + (st) * 128, lds0 + ((st) & 3) * P1_STAGE + (p) * 1024)
#define P1_DW(st, p, par) dma16(woff[par], dw, ((st) * 64 + ((p) & 7) * 8) * 3840 + nt * 256 + ((p) >> 3) * 128, lds0 + ((st) & 3) * P1_STAGE + 16384 + (p) * 1024)
    P1_DX(0, 2 * wid, 0); P1_DW(0, 2 * wid, 0); P1_DX(0, 2 * wid + 1, 1); P1_DW(0, 2 * wid + 1, 1);
    const int hh = lane >> 5;
    const int l15 = lane & 15, kg = lane >> 4;
    const unsigned fl4 = (lane & 3) * 4;
    const u32x4 dh = mkdesc(nt < 8 ? wr : wi, 640 * 1280);
#define GET_EPOCH() do { epoch = ld32(W, WS_EP, 0) + 1u; if (lane < 4) st32(W, 0x3F803F80u, oneoff + lane * 4, 0); } while (0)
#define WH_ADDR(q) ((q) < 32 ? 2 * P1_STAGE + (q) * 1024 : (q) < 48 ? 3 * P1_STAGE + 16384 + ((q) - 32) * 1024 : \
                    (q) < 68 ? LDS_X9 + ((q) - 48) * 1024 : (q) < 84 ? 16384 + ((q) - 68) * 1024 : P1_STAGE + 16384 + ((q) - 84) * 1024)

    if (wid >= 4) {
#define P1_ISSUE(st) do { \
            const unsigned x_ = (mt * 128 + gwv * 32) * 1280 + (st) * 128, w_ = ((st) * 64 + (gwv & 1) * 32) * 3840 + nt * 256 + (gwv >> 1) * 128; \
            dma16x4(roff[0], roff[1], dx, x_, x_ + 9216, x_ + 18432, x_ + 27648, lds0 + ((st) & 3) * P1_STAGE + gwv * 4096); \
            dma16x4(woff[0], woff[1], dw, w_, w_ + 29696, w_ + 59392, w_ + 89088, lds0 + ((st) & 3) * P1_STAGE + 16384 + gwv * 4096); } while (0)
#define P1_DH(q, par) do { const int q_ = (q), c_ = q_ / 10, p_ = q_ - c_ * 10; \
            dma16(roff[par], dh, ((nt & 7) * 80 + p_ * 8) * 1280 + (c_) * 128, lds0 + WH_ADDR(q_)); } while (0)
        P1_ISSUE(1);
        WAITV(8); BARRIER_NOWAIT(); P1_ISSUE(2);
        WAITV(8); BARRIER_NOWAIT(); P1_ISSUE(3); P1_ISSUE(4);
        NOUNROLL for (int k = 2; k < 7; ++k) { WAITV(16); BARRIER_NOWAIT(); P1_ISSUE(k + 3); }
        WAITV(16); BARRIER_NOWAIT();
        UNROLL for (int i = 0; i < 8; ++i) P1_DH(gwv * 8 + i, i & 1);
        WAITV(16); BARRIER_NOWAIT();
        UNROLL for (int i = 0; i < 8; ++i) P1_DH(32 + gwv * 8 + i, i & 1);
        WAITV(16); BARRIER_NOWAIT();
        UNROLL for (int i = 0; i < 6; ++i) { const int q = 64 + gwv * 6 + i; P1_DH(q < 84 ? q : 82 + (i & 1), i & 1); }
        GET_EPOCH();
        __builtin_amdgcn_s_setprio(3);
        WAITV(0); BARRIER_NOWAIT();
        UNROLL for (int i = 0; i < 4; ++i) P1_DH(84 + gwv * 4 + i, i & 1);
    } else {
        const unsigned sink = lds0 + LDS_SINK;
        const int pi = xcd * 4 + wid, pst = pi / 5, pu = pi - pst * 5;
        {
            const unsigned b = blockIdx.x;
            if (wid == 0) { dma4(lane * 128, dx, b * 10240, sink); dma4(lane < 16 ? lane * 128 : OOB, dx, b * 10240 + 8192, sink); }
            if (wid == 1) { dma4(lane * 128, dw, b * 9600, sink); dma4(lane < 11 ? lane * 128 : OOB, dw, b * 9600 + 8192, sink); }
            if (wid == 2) { dma4(OOB, dx, 0, sink); dma4(OOB, dx, 0, sink); }
        }
        if (wid == 3) {
            dma4(lane < 12 ? lane * 128 : OOB, mkdesc((const void*)(pc & ~127ull), CODE_LINES * 128), idx * 1536, sink);
            dma16(nt == 15 ? lane * 16 : OOB, mkdesc(gw, 1280), 0, lds0 + LDS_GW);
        }
        if (tid < 64) *(__attribute__((address_space(3))) unsigned*)(LW + LDS_FL + tid * 4) = 0u;
        const int wm = wid & 1, wn = wid >> 1;
        unsigned ra[2];
        UNROLL for (int ks = 0; ks < 2; ++ks) ra[ks] = l15 * 128 + (((4 * ks + kg) ^ ((l15 >> 1) & 7)) << 4);
        const unsigned xk = wm * 8192 + wn * 4096, xo = wm * 8192 + (wn ^ 1) * 4096;
        s16x8 xs[10][2][2];
#define WT_LOAD() dma16((lane >> 1) * 1280 + (lane & 1) * 16, mkdesc(wo, 640 * 1280), \
                  pi < 25 ? ((idx >> 3) * 160 + pst * 32) * 1280 + (idx & 7) * 160 + pu * 32 : OOB, lds0 + LDS_WT + wid * 1024)
#define WT_STORE() do { const lds_cp tp_ = L + LDS_WT + wid * 1024 + (8 * kg + (l15 >> 2)) * 32 + (lane & 3) * 8; \
            st128(W, __builtin_bit_cast(u32x4, cat8(ldstr(tp_), ldstr(tp_ + 128))), \
                  pi < 25 ? WS_WOT + (((idx & 7) * 4 + (idx >> 3)) * 25 + pi) * 1024 + lane * 16 : OOB, 0); } while (0)
        if (nt == 15) {
            const int m0 = mt * 128 + wm * 64 + wn * 32;
            if (wid == 3) dma16(lane * 16, mkdesc(gw, 1280), 1024, lds0 + LDS_GW + 1024);
            WT_LOAD();
            epoch = ld32(W, WS_EP, 0) + 1u;
            {
                const unsigned o0 = (mt * 4 + wid) * 12800, v1 = lane < 36 ? lane * 128 : OOB;
                dma4(lane * 128, mkdesc(wr, 640 * 1280), o0, sink); dma4(v1, mkdesc(wr, 640 * 1280), o0 + 8192, sink);
                dma4(lane * 128, mkdesc(wi, 640 * 1280), o0, sink); dma4(v1, mkdesc(wi, 640 * 1280), o0 + 8192, sink);
            }
            WAITV(4);
            float sum[2] = {0.f, 0.f};
            UNROLL for (int k = 0; k < 10; ++k) {
                BARRIER();
                UNROLL for (int ks = 0; ks < 2; ++ks) {
                    const u32x4 gv = __builtin_bit_cast(u32x4, lds128(L + LDS_GW + (k) * 128 + (4 * ks + kg) * 16));
                    UNROLL for (int h = 0; h < 2; ++h) {
                        xs[k][ks][h] = lds128(L + (k & 3) * P1_STAGE + xk + h * 2048 + ra[ks]);
                        const u32x4 xv = __builtin_bit_cast(u32x4, xs[k][ks][h]);
                        UNROLL for (int e = 0; e < 4; ++e)
                            { const unsigned xe = xv[e], ge = gv[e]; sum[h] = __builtin_amdgcn_fdot2_f32_bf16(__builtin_bit_cast(bf16x2_t, xe), __builtin_bit_cast(bf16x2_t, ge), sum[h], false); }
                    }
                }
                SBAR();
            }
            UNROLL for (int h = 0; h < 2; ++h) { sum[h] += __shfl_xor(sum[h], 16); sum[h] += __shfl_xor(sum[h], 32); }
            const float sm = kg & 1 ? sum[1] : sum[0];
            if (lane < 32) st32(W, __float_as_uint(1.0f / (1.0f + __expf(-sm))), WS_G + (m0 + lane) * 4, 0);
        } else {
            const int q = l15 >> 2;
            unsigned trb[4];
            UNROLL for (int j = 0; j < 4; ++j)
                trb[j] = wn * 8192 + (8 * kg + q) * 128 + ((j ^ (((q >> 1) & 1) << 1) ^ (kg & 1)) << 5) + 8 * (lane & 3);
            f32x4 acc[4][4];
            UNROLL for (int i = 0; i < 4; ++i) UNROLL for (int j = 0; j < 4; ++j) acc[i][j] = f32x4{};
            s16x8 xt[2][2]; s16x4 wv[4][2];
            WT_LOAD();
            epoch = ld32(W, WS_EP, 0) + 1u;
            SBAR();
            WAITV(4);
            unsigned bk[2], bo[2];
            UNROLL for (int ks = 0; ks < 2; ++ks) { bk[ks] = xk + ra[ks]; bo[ks] = xo + ra[ks]; }
#define P1_WIN(d) do { UNROLL for (int ks = 0; ks < 2; ++ks) { bk[ks] += (d); bo[ks] += (d); PIN(bk[ks]); PIN(bo[ks]); } \
                       UNROLL for (int j_ = 0; j_ < 4; ++j_) { trb[j_] += (d); PIN(trb[j_]); } } while (0)
#define P1_RW(z, j) do { const lds_cp p_ = L + trb[j] + ((((z) >> 1) & 1) * P1_STAGE + 16384 + ((z) & 1) * 4096); wv[j][0] = ldstr(p_); wv[j][1] = ldstr(p_ + 512); } while (0)
#define P1_RK(z, h) xs[(z) >> 1][(z) & 1][h] = lds128(L + bk[(z) & 1] + ((((z) >> 1) & 1) * P1_STAGE + (h) * 2048))
#define P1_RO(z, h) xt[(z) & 1][h] = lds128(L + bo[(z) & 1] + ((((z) >> 1) & 1) * P1_STAGE + (h) * 2048))
#define P1_XF(z, i) ((i) < 2 ? xs[(z) >> 1][(z) & 1][(i) & 1] : xt[(z) & 1][(i) & 1])
#define P1_LOOP(MF) \
            P1_WIN(0); \
            BARRIER(); \
            P1_RK(0, 0); P1_RW(0, 0); P1_RK(0, 1); P1_RO(0, 0); P1_RO(0, 1); P1_RW(0, 1); P1_RW(0, 2); P1_RW(0, 3); SBAR(); \
            UNROLL for (int z = 0; z < 20; ++z) { \
                UNROLL for (int j = 0; j < 4; ++j) { \
                    MF(z, 0, j); SBAR(); \
                    if (j == 0 && (z & 3) == 3 && z < 19) { P1_WIN((z & 7) == 3 ? 65536u : 0u - 65536u); SBAR(); } \
                    if (z < 19 && j == 1) { P1_RK(z + 1, 0); SBAR(); } \
                    if (z < 19 && j == 2) { P1_RO(z + 1, 0); SBAR(); } \
                    MF(z, 1, j); SBAR(); \
                    if (z < 19 && j == 1) { P1_RK(z + 1, 1); SBAR(); } \
                    if (z < 19 && j == 2) { P1_RO(z + 1, 1); SBAR(); } \
                    MF(z, 2, j); SBAR(); \
                    if (j == 0 && (z & 1) && z < 19) { BARRIER(); } \
                    MF(z, 3, j); SBAR(); \
                    if (z < 19) { P1_RW(z + 1, j); SBAR(); } \
                } \
            }
#define P1_MFQ(z, i, j) acc[i][j] = MFMA16(cat8(wv[j][0], wv[j][1]), P1_XF(z, i), acc[i][j])
#define P1_MFV(z, i, j) acc[i][j] = MFMA16(P1_XF(z, i), cat8(wv[j][0], wv[j][1]), acc[i][j])
            if (nt < 10) {
                P1_LOOP(P1_MFQ)
                const float sc = nt < 5 ? 0.32259889079f : 1.0f;
                UNROLL for (int i = 0; i < 4; i += 2)
                    UNROLL for (int j = 0; j < 4; ++j) {
                        unsigned a0 = pack2(acc[i][j][0] * sc, acc[i][j][1] * sc), a1 = pack2(acc[i][j][2] * sc, acc[i][j][3] * sc);
                        unsigned b0 = pack2(acc[i + 1][j][0] * sc, acc[i + 1][j][1] * sc), b1 = pack2(acc[i + 1][j][2] * sc, acc[i + 1][j][3] * sc);
                        auto s0 = __builtin_amdgcn_permlane16_swap(a0, b0, false, false);
                        auto s1 = __builtin_amdgcn_permlane16_swap(a1, b1, false, false);
                        st128(W, u32x4{s0[0], s1[0], s0[1], s1[1]}, WS_QK + (nt * 16 + wn * 8 + (kg >> 1)) * CH + (mt * 128 + wm * 64 + ((i >> 1) ^ wn) * 32 + (kg & 1) * 16 + l15) * 16, j * 2 * CH);
                    }
            } else {
                P1_LOOP(P1_MFV)
                UNROLL for (int i = 0; i < 4; ++i)
                    UNROLL for (int j = 0; j < 4; j += 2) {
                        unsigned a0 = pack2(acc[i][j][0], acc[i][j][1]), a1 = pack2(acc[i][j][2], acc[i][j][3]);
                        unsigned b0 = pack2(acc[i][j + 1][0], acc[i][j + 1][1]), b1 = pack2(acc[i][j + 1][2], acc[i][j + 1][3]);
                        auto s0 = __builtin_amdgcn_permlane32_swap(a0, b0, false, false);
                        auto s1 = __builtin_amdgcn_permlane32_swap(a1, b1, false, false);
                        st128(W, u32x4{s0[0], s1[0], s0[1], s1[1]}, WS_VT + ((mt * 8 + wm * 4 + ((i >> 1) ^ wn) * 2 + (i & 1)) * 2 + (kg & 1)) * VROW + ((nt - 10) * 128 + wn * 64 + hh * 16 + l15) * 16, j * 256);
                    }
            }
        }
        BARRIER();
        __builtin_amdgcn_s_setprio(2);
        f32x4 hc[2][5];
        UNROLL for (int h = 0; h < 2; ++h) UNROLL for (int u = 0; u < 5; ++u) hc[h][u] = f32x4{};
        UNROLL for (int z = 14; z < 20; ++z) UNROLL for (int h = 0; h < 2; ++h)
            xs[z >> 1][z & 1][h] = lds128(L + ((z >> 1) & 3) * P1_STAGE + xk + h * 2048 + ra[z & 1]);
        unsigned rw[3][2];
        UNROLL for (int w = 0; w < 3; ++w) UNROLL for (int ks = 0; ks < 2; ++ks) { rw[w][ks] = ra[ks] + w * 65536; PIN(rw[w][ks]); }
        s16x8 wb[2][5];
        UNROLL for (int z = 0; z < 21; ++z) {
            if (z == 3 && nt != 15) publish(W, WS_F1 + ((nt * 2 + wn) * 32 + mt * 2 + wm) * FS, epoch, lane);
            if (z == 16) { lds_wait(L + FL_FULL + fl4, 1); }
            UNROLL for (int u = 0; u < 5; ++u) {
                if (z < 20) { wb[z & 1][u] = lds128(L + rw[WH_ADDR((z >> 1) * 10 + 2 * u) >> 16][z & 1] + (WH_ADDR((z >> 1) * 10 + 2 * u) & 65535)); SBAR(); }
                if (z > 0) {
                    hc[0][u] = MFMA16(xs[(z - 1) >> 1][(z - 1) & 1][0], wb[(z - 1) & 1][u], hc[0][u]);
                    hc[1][u] = MFMA16(xs[(z - 1) >> 1][(z - 1) & 1][1], wb[(z - 1) & 1][u], hc[1][u]); SBAR();
                }
            }
        }
        WAITV(0);
        WT_STORE();
        lds_signal(LW + FL_FREE + wid * 4, 1);
        const float sg = nt < 8 ? 1.0f : -1.0f;
        const unsigned ab = WS_AB + (((nt & 7) >> 1) * 512 + (nt >> 3) * 256 + mt * 16 + wm * 8 + wn * 4 + (kg & 1) * 2 + (kg >> 1)) * 2560 + ((nt & 1) * 80 + l15) * 16;
        UNROLL for (int u = 0; u < 5; ++u) {
            unsigned a0 = pack2(hc[0][u][0] * sg, hc[0][u][1] * sg), a1 = pack2(hc[0][u][2] * sg, hc[0][u][3] * sg);
            unsigned b0 = pack2(hc[1][u][0] * sg, hc[1][u][1] * sg), b1 = pack2(hc[1][u][2] * sg, hc[1][u][3] * sg);
            auto s0 = __builtin_amdgcn_permlane16_swap(a0, b0, false, false);
            auto s1 = __builtin_amdgcn_permlane16_swap(a1, b1, false, false);
            st128(W, u32x4{s0[0], s1[0], s0[1], s1[1]}, ab, u * 256);
        }
        publish(W, WS_F2 + (nt * 64 + mt * 4 + wid) * FS, epoch, lane);
        if (lane == 0) st32(W, epoch, WS_ACK + (blockIdx.x * 8 + wid) * 4, 0);
    }
    }
    const int lane = __builtin_amdgcn_mbcnt_hi(~0u, __builtin_amdgcn_mbcnt_lo(~0u, 0u));
    const int r = lane & 31, hh = lane >> 5;
    const int l15 = lane & 15, kg = lane >> 4;
    const unsigned fl4 = (lane & 3) * 4;

    const int ksp = xcd >> 1, nt4 = idx >> 3;
    const int tt = mt * 2 + (ksp & 1), ct = nt4 * 2 + (ksp >> 1);
    const u32x4 dc = mkdesc(ksp < 2 ? cre : cim, 2048 * 4096), dws = mkdesc(ws, WS_END);
    const unsigned cs0 = mt * 128 * 4096 + (ksp & 1) * 2048;
    const unsigned as0 = WS_AB + (nt4 * 512 + ksp * 128) * 2560;

    if (wid >= 4) {
        const int head = xcd * 4 + (idx >> 3), qt = idx & 7;
        const int qh = wid & 1, kh = (wid >> 1) & 1;
        const int col0 = head * 20, odd = head & 1;
        const int cA = (col0 >> 3) + odd, cH = odd ? (col0 >> 3) : (col0 >> 3) + 2;
        const int q0 = qt * 256 + qh * 128;
        unsigned k1off = WS_QK + (80 + cA + hh) * CH + r * 16;
        unsigned k2off = hh ? OOB : WS_QK + (80 + cH) * CH + r * 16 + odd * 8;
        unsigned q2off = hh ? OOB : k2off - 80 * CH;
        unsigned k2or = hh ? 0x3F80u : 0u;
        const int dv = odd ? (r < 16 ? r + 4 : r - 16) : r;
        const unsigned voff = r < 20 ? WS_VT + hh * VROW + (col0 + dv) * 16 : (r == 20 ? oneoff : OOB);
        unsigned vstep = r < 20 ? 4 * VROW : 0;
        unsigned va = voff + kh * 32 * vstep, vb2 = va + (vstep >> 1);
        constexpr int NB = 4;
        u32x4 k1[NB], v0[NB], v1[NB]; u32x2 k2[NB];
        f32x16 o[4], s[2];
        u32x4 pa = u32x4{}, pd = u32x4{};
        f32x8 e8 = f32x8{};
        UNROLL for (int i = 0; i < 4; ++i) { o[i] = f32x16{}; PIN(o[i]); }
        v0[NB - 1] = u32x4{}; v1[NB - 1] = u32x4{};
        PIN(v0[NB - 1]); PIN(v1[NB - 1]); PIN(pa); PIN(pd); PIN(e8);
        PIN(k1off); PIN(k2off); PIN(q2off); PIN(k2or); PIN(va); PIN(vb2); PIN(vstep);
        WAITV(0);
        lds_post(LW + FL_FULL + gwv * 4, 1);
        const int nsa = (lane & 4) ? (col0 + 19) >> 6 : col0 >> 6;
        unsigned fkv = WS_F1 + ((10 + ((lane >> 3) & 1) * 10 + nsa) * 32 + kh * 16) * FS;
        __builtin_amdgcn_s_sleep(8);
        wait_flags1<4>(W, (lane & 16) ? WS_F1 + (nsa * 32 + (q0 >> 6) + (lane & 1)) * FS : fkv + (lane & 3) * FS, epoch);
        fkv += (lane & 1) * FS;
        u32x4 q1[4]; u32x2 q2[4];
        UNROLL for (int i = 0; i < 4; ++i) {
            const int qr = q0 + ((i + 2 + 2 * kh) & 3) * 32;
            q2[i] = ld64(W, q2off, qr * 16);
            q1[i] = ld128(W, k1off - 80 * CH, qr * 16);
        }
        const int t0 = kh * 32;
        unsigned fnx = epoch;
        SBAR();
        UNROLL for (int b = 0; b < NB; ++b) {
            k2[b] = ld64(W, k2off, (t0 + b) * 512); k1[b] = ld128(W, k1off, (t0 + b) * 512); SBAR();
            if (b < NB - 1) { v0[b] = ld128(W, va, 0); v1[b] = ld128(W, vb2, 0); va += vstep; vb2 += vstep; SBAR(); }
        }
        const f32x16 zero16 = f32x16{};
        {
            f32x16 t[4];
            UNROLL for (int i = 0; i < 4; ++i) { t[i] = MFMA32K8(as4(k2[0]), as4(q2[i]), zero16); t[i] = MFMA32(as8(k1[0]), as8(q1[i]), t[i]); SBAR(); }
            k2[0][0] |= k2or;
            UNROLL for (int i = 0; i < 4; ++i) {
                float m = t[i][0];
                UNROLL for (int e = 1; e < 16; ++e) m = fmaxf(m, t[i][e]);
                auto mm = __builtin_amdgcn_permlane32_swap(__float_as_uint(m), __float_as_uint(m), false, false);
                m = fmaxf(__uint_as_float(mm[0]), __uint_as_float(mm[1]));
                q2[i][0] = hh ? pack2(-m, 0.f) & 0xFFFFu : q2[i][0];
                if (i == 0) {
                    s[0] = MFMA32K8(as4(k2[0]), as4(q2[0]), zero16);
                    s[0] = MFMA32(as8(k1[0]), as8(q1[0]), s[0]);
                }
            }
        }
        st32(W, epoch, lane == 0 ? WS_ACK + (blockIdx.x * 8 + wid) * 4 : OOB, 0);
        __builtin_amdgcn_s_setprio(1);
        NOUNROLL for (int tb = 0; tb < 32; tb += NB) {
            if (__builtin_expect(!__all(fnx == epoch), 0)) wait_flags1<4>(W, fkv + (tb < 28 ? tb + 4 : 0) * (FS / 2), epoch);
            if (tb == 16) lds_wait(L + FL_FREE + fl4, 1);
            const unsigned fso = (tb < 24 ? tb + 8 : 0) * (FS / 2);
            SBAR();
            UNROLL for (int b = 0; b < NB; ++b) {
                const int bn = (b + 1) % NB, bp = (b + NB - 1) % NB;
                const unsigned kso = tb + b + NB < 32 ? (t0 + tb + b + NB) * 512 : OOB / 2;
                const unsigned vso = tb + b + NB - 1 < 32 ? 0 : OOB / 2;
                UNROLL for (int qi = 0; qi < 4; ++qi) {
                    const int c = qi & 1, n = c ^ 1, qn = (qi + 1) & 3, qp = (qi + 3) & 3;
                    const int kb = qi == 3 ? bn : b, vb = qi == 0 ? bp : b;
                    f32x16& SC = s[c]; f32x16& SN = s[n];
                    SN = MFMA32K8(as4(k2[kb]), as4(q2[qn]), zero16); SBAR();
                    SC[0] = EX2(SC[0]); SC[1] = EX2(SC[1]); SC[2] = EX2(SC[2]); SC[3] = EX2(SC[3]); SBAR();
                    pd[0] = pack2(e8[0], e8[1]); pd[1] = pack2(e8[2], e8[3]); PIN(SC); PIN(pd); SBAR();
                    SN = MFMA32(as8(k1[kb]), as8(q1[qn]), SN); SBAR();
                    if (qi == 2) { k2[b] = ld64(W, k2off, kso); k1[b] = ld128(W, k1off, kso); SBAR(); }
                    if (qi == 3 && b == 0) { fnx = ld32(W, fkv, fso); SBAR(); }
                    SC[4] = EX2(SC[4]); SC[5] = EX2(SC[5]); SC[6] = EX2(SC[6]); SC[7] = EX2(SC[7]); SBAR();
                    pd[2] = pack2(e8[4], e8[5]); pd[3] = pack2(e8[6], e8[7]); PIN(SC); PIN(pd); SBAR();
                    o[qp] = MFMA32(as8(v0[vb]), as8(pa), o[qp]); SBAR();
                    e8[0] = EX2(SC[8]); e8[1] = EX2(SC[9]); e8[2] = EX2(SC[10]); e8[3] = EX2(SC[11]); SBAR();
                    pa[0] = pack2(SC[0], SC[1]); pa[1] = pack2(SC[2], SC[3]);
                    if (qi == 2) { k2[bn][0] |= k2or; PIN(k2[bn]); }
                    if (qi == 1) { va += vstep; vb2 += vstep; PIN(va); PIN(vb2); }
                    PIN(e8); PIN(pa); SBAR();
                    o[qp] = MFMA32(as8(v1[vb]), as8(pd), o[qp]); SBAR();
                    if (qi == 0) { v0[bp] = ld128(W, va, vso); v1[bp] = ld128(W, vb2, vso); SBAR(); }
                    e8[4] = EX2(SC[12]); e8[5] = EX2(SC[13]); e8[6] = EX2(SC[14]); e8[7] = EX2(SC[15]); SBAR();
                    pa[2] = pack2(SC[4], SC[5]); pa[3] = pack2(SC[6], SC[7]); PIN(e8); PIN(pa); SBAR();
                }
            }
        }
        __builtin_amdgcn_s_setprio(3);
        UNROLL for (int e = 0; e < 4; ++e) pd[e] = pack2(e8[2 * e], e8[2 * e + 1]);
        o[3] = MFMA32(as8(v0[NB - 1]), as8(pa), o[3]);
        o[3] = MFMA32(as8(v1[NB - 1]), as8(pd), o[3]);
        SBAR();
        const int f0 = q0 + kh * 64;
        UNROLL for (int j = 0; j < 2; ++j) {
            const lds_p ex = LW + LDS_EX + (gwv * 2 + j) * EXB;
            *(__attribute__((address_space(3))) u32x4*)(ex + lane * 16) =
                u32x4{pack2(o[j][0], o[j][1]), pack2(o[j][2], o[j][3]), pack2(o[j][4], o[j][5]), pack2(o[j][6], o[j][7])};
            *(__attribute__((address_space(3))) u32x2*)(ex + 1024 + lane * 8) =
                hh ? u32x2{__float_as_uint(o[j][8]), q2[j][0] << 16} : u32x2{pack2(o[j][8], o[j][9]), pack2(o[j][10], o[j][11])};
        }
        lds_signal(LW + FL_EX + gwv * 4);
        float n1[2], l1[2];
        UNROLL for (int j = 0; j < 2; ++j) {
            auto nn = __builtin_amdgcn_permlane32_swap(q2[2 + j][0] << 16, q2[2 + j][0] << 16, false, false);
            auto ll = __builtin_amdgcn_permlane32_swap(__float_as_uint(o[2 + j][8]), __float_as_uint(o[2 + j][8]), false, false);
            n1[j] = __uint_as_float(nn[1]); l1[j] = __uint_as_float(ll[1]);
        }
        const unsigned orow = WS_O + ((f0 >> 4) + (r >> 4)) * 20480 + (r & 15) * 16;
        const unsigned oa = orow + (cA + hh) * 256, ob = hh ? OOB : orow + cH * 256 + odd * 8;
        lds_wait(L + FL_EX + (gwv ^ 2) * 4);
        bool ok = true;
        u32x4 ra[2]; u32x2 rb[2];
        UNROLL for (int j = 0; j < 2; ++j) {
            const lds_cp rx = L + LDS_EX + ((gwv ^ 2) * 2 + j) * EXB;
            const u32x4 a = *(const __attribute__((address_space(3))) u32x4*)(rx + lane * 16);
            const u32x2 c = *(const __attribute__((address_space(3))) u32x2*)(rx + 1024 + r * 8);
            const u32x2 d = *(const __attribute__((address_space(3))) u32x2*)(rx + 1280 + r * 8);
            const float n2 = __uint_as_float(d[1]), l2 = __uint_as_float(d[0]), nm = fminf(n1[j], n2);
            float w1 = EX2(nm - n1[j]), w2 = EX2(nm - n2);
            const float l = l1[j] * w1 + l2 * w2, rl = __builtin_amdgcn_rcpf(l);
            w1 *= rl; w2 *= rl;
            float f[12];
            UNROLL for (int e = 0; e < 4; ++e) { f[2 * e] = o[2 + j][2 * e] * w1 + bflo(a[e]) * w2; f[2 * e + 1] = o[2 + j][2 * e + 1] * w1 + bfhi(a[e]) * w2; }
            UNROLL for (int e = 0; e < 2; ++e) { f[8 + 2 * e] = o[2 + j][8 + 2 * e] * w1 + bflo(c[e]) * w2; f[9 + 2 * e] = o[2 + j][9 + 2 * e] * w1 + bfhi(c[e]) * w2; }
            float sa = 0.f;
            UNROLL for (int e = 0; e < 12; ++e) sa += fabsf(f[e]);
            ok = ok && l1[j] < 1e30f && l2 < 1e30f && l > 1e-18f && sa < 3e38f;
            auto s0 = __builtin_amdgcn_permlane32_swap(pack2(f[0], f[1]), pack2(f[4], f[5]), false, false);
            auto s1 = __builtin_amdgcn_permlane32_swap(pack2(f[2], f[3]), pack2(f[6], f[7]), false, false);
            ra[j] = u32x4{s0[0], s1[0], s0[1], s1[1]};
            rb[j] = u32x2{pack2(f[8], f[9]), pack2(f[10], f[11])};
        }
        if (__builtin_expect(__all(ok), 1)) {
            UNROLL for (int j = 0; j < 2; ++j) { st128(W, ra[j], oa + j * 40960, 0); st64(W, rb[j], ob + j * 40960, 0); }
        } else {
            wait_flags1<4>(W, WS_F1 + ((10 + hh * 10 + (col0 >> 6)) * 32 + r) * FS, epoch);
            wait_flags1<4>(W, WS_F1 + ((10 + hh * 10 + ((col0 + 19) >> 6)) * 32 + r) * FS, epoch);
            NOUNROLL for (int j = 0; j < 2; ++j) {
                const u32x2 z2 = ld64(W, q2off, (f0 + j * 32) * 16);
                const u32x4 z1 = ld128(W, k1off - 80 * CH, (f0 + j * 32) * 16);
                float m = -3e38f;
                f32x16 oo = f32x16{};
                NOUNROLL for (int t = 0; t < 64; ++t) {
                    const u32x4 a1 = ld128(W, k1off, t * 512); const u32x2 a2 = ld64(W, k2off, t * 512);
                    const u32x4 w0 = ld128(W, voff + t * vstep, 0), w1 = ld128(W, voff + t * vstep + (vstep >> 1), 0);
                    f32x16 z = MFMA32K8(as4(a2), as4(z2), zero16);
                    z = MFMA32(as8(a1), as8(z1), z);
                    float mx = z[0];
                    UNROLL for (int e = 1; e < 16; ++e) mx = fmaxf(mx, z[e]);
                    auto mm = __builtin_amdgcn_permlane32_swap(__float_as_uint(mx), __float_as_uint(mx), false, false);
                    const float mn = fmaxf(m, fmaxf(__uint_as_float(mm[0]), __uint_as_float(mm[1])));
                    const float al = EX2(m - mn);
                    m = mn;
                    UNROLL for (int e = 0; e < 16; ++e) oo[e] *= al;
                    u32x4 p0, p1;
                    UNROLL for (int e = 0; e < 4; ++e) { p0[e] = pack2(EX2(z[2 * e] - m), EX2(z[2 * e + 1] - m)); p1[e] = pack2(EX2(z[8 + 2 * e] - m), EX2(z[9 + 2 * e] - m)); }
                    oo = MFMA32(as8(w0), as8(p0), oo);
                    oo = MFMA32(as8(w1), as8(p1), oo);
                }
                auto l2 = __builtin_amdgcn_permlane32_swap(__float_as_uint(oo[8]), __float_as_uint(oo[8]), false, false);
                const float sc = 1.0f / __uint_as_float(l2[1]);
                auto s0 = __builtin_amdgcn_permlane32_swap(pack2(oo[0] * sc, oo[1] * sc), pack2(oo[4] * sc, oo[5] * sc), false, false);
                auto s1 = __builtin_amdgcn_permlane32_swap(pack2(oo[2] * sc, oo[3] * sc), pack2(oo[6] * sc, oo[7] * sc), false, false);
                st128(W, u32x4{s0[0], s1[0], s0[1], s1[1]}, oa + j * 40960, 0);
                st64(W, u32x2{pack2(oo[8] * sc, oo[9] * sc), pack2(oo[10] * sc, oo[11] * sc)}, ob + j * 40960, 0);
            }
        }
        publish(W, WS_F3 + ((f0 >> 6) * 32 + head) * FS, epoch, lane);
        if (blockIdx.x == 0 && wid == 4) {
            NOUNROLL for (unsigned spins = 0; spins < SPIN_CAP; ++spins) {
                asm volatile("" ::: "memory");
                bool ok = true;
                UNROLL for (int i = 0; i < 8; ++i) {
                    const u32x4 f = ld128(W, WS_ACK + i * 1024 + lane * 16, 0);
                    ok = ok && f[0] == epoch && f[1] == epoch && f[2] == epoch && f[3] == epoch;
                }
                if (__all(ok)) break;
                __builtin_amdgcn_s_sleep(32);
            }
            if (lane == 0) st32(W, epoch, WS_EP, 0);
        }
        return;
    }

    __builtin_amdgcn_s_setprio(2);
    f32x4 ys[5];
    u32x4 wf[5][5];
    {
        unsigned coff[2]; const unsigned lin = lane * 16;
        UNROLL for (int i = 0; i < 2; ++i) coff[i] = (lane >> 3) * 4096 + (((lane & 7) ^ ((4 * i + (lane >> 4)) & 7)) << 4);
#define SQ_DC4(n, slot) do { const unsigned c_ = cs0 + gwv * 131072 + (n) * 128; \
            dma16x4(coff[0], coff[1], dc, c_, c_ + 31744, c_ + 63488, c_ + 95232, lds0 + (slot) * SQ_STAGE + gwv * 4096); } while (0)
#define SQ_DA5(n, slot) do { dma16sx4(lin, dws, as0 + (n) * 20480 + gwv * 5120, lds0 + (slot) * SQ_STAGE + 16384 + gwv * 5120); SQ_DA(n, slot, 4); } while (0)
#define SQ_DA(n, slot, i) dma16s(lin, dws, as0 + (n) * 20480 + gwv * 5120 + (i) * 1024, lds0 + (slot) * SQ_STAGE + 16384 + gwv * 5120 + (i) * 1024)
        const int wm = wid & 1, wn = wid >> 1;
        f32x4 acc[4][5];
        UNROLL for (int i = 0; i < 4; ++i) UNROLL for (int u = 0; u < 5; ++u) { acc[i][u] = f32x4{}; PIN(acc[i][u]); }
        unsigned ra[2];
        UNROLL for (int ks = 0; ks < 2; ++ks) ra[ks] = wm * 8192 + l15 * 128 + (((4 * ks + kg) ^ ((l15 >> 1) & 7)) << 4);
        const unsigned rb = 16384 + kg * 2560 + (wn * 80 + l15) * 16;
        s16x8 fc[2][4], fa[2][5];
        UNROLL for (int i = 0; i < 4; ++i) fc[1][i] = s16x8{};
        UNROLL for (int u = 0; u < 5; ++u) fa[1][u] = s16x8{};
        if (wid == 0) {
            const unsigned f2 = WS_F2 + (((ksp >> 1) * 8 + nt4 * 2 + hh) * 64 + (ksp & 1) * 32 + r) * FS;
            __builtin_amdgcn_s_sleep(16);
            wait_flags1<12>(W, f2, epoch);
            lds_wait(L + FL_FREE + fl4, 1);
            lds_post(LW + FL_FIN, 1);
        } else {
            lds_wait<8>(L + FL_FIN, 1);
        }
        UNROLL for (int n = 0; n < 2; ++n) { SQ_DC4(n, n); SQ_DA5(n, n); }
        const lds_p pw = LW + (lane == 0 ? FL_SFULL + gwv * 4 : LDS_SINK + lane * 4);
        const lds_cp pr = L + FL_SFULL + fl4;
        const unsigned wfo = WS_WOT + (ct * 4 + gwv) * 25600 + lane * 16;
#define SQ_MF(u) do { UNROLL for (int i = 0; i < 4; ++i) acc[i][u] = MFMA16(fa[c][u], fc[c][i], acc[i][u]); SBAR(); } while (0)
#define SQ_RC(i) fc[n][i] = lds128(sr[ks] + (i) * 2048)
#define SQ_RA(u) fa[n][u] = lds128(sp + ks * 10240 + (u) * 256)
#define SQ_STG(DMA) do { \
            lds_cp sp = L + (j & 3) * SQ_STAGE + rb; lds_cp sr[2]; \
            UNROLL for (int ks = 0; ks < 2; ++ks) { sr[ks] = L + (j & 3) * SQ_STAGE + ra[ks]; PIN(sr[ks]); } \
            unsigned pv = j + 2, fl = 0; \
            PIN(sp); PIN(pv); SBAR(); \
            __builtin_amdgcn_s_setprio(0); \
            const int sf = (j + 2) & 3; \
            UNROLL for (int ks = 0; ks < 2; ++ks) { \
                const int n = ks, c = n ^ 1; \
                SQ_RC(0); SQ_RC(1); SQ_RC(2); SBAR(); SQ_MF(0); \
                SQ_RC(3); SQ_RA(0); SQ_RA(1); SBAR(); SQ_MF(1); \
                SQ_RA(2); SQ_RA(3); SQ_RA(4); \
                if (ks == 0) { WAITV(0); lds_post(pw, pv); } \
                SBAR(); SQ_MF(2); \
                if (DMA && ks == 0) { SQ_DC4(j + 2, sf); SBAR(); } \
                if (ks == 1) { fl = lds_peek(pr); SBAR(); } \
                SQ_MF(3); \
                if (DMA && ks == 0) { SQ_DA5(j + 2, sf); SBAR(); } \
                SQ_MF(4); \
            } \
            __builtin_amdgcn_s_setprio(3); \
            if (__builtin_expect(!__all(fl >= pv), 0)) lds_wait<3>(pr, pv); } while (0)
        int j = 0;
        WAITV(9); lds_post(pw, 1); lds_wait(pr, 1);
        NOUNROLL for (; j < 14; ++j) SQ_STG(true);
        NOUNROLL for (; j < 16; ++j) SQ_STG(false);
        UNROLL for (int u = 0; u < 5; ++u) UNROLL for (int i = 0; i < 4; ++i) acc[i][u] = MFMA16(fa[1][u], fc[1][i], acc[i][u]);
        __builtin_amdgcn_s_setprio(3);
        if (wid == ksp) {
            UNROLL for (int i = 0; i < 4; ++i) UNROLL for (int u = 0; u < 5; ++u) stsf4(LW + 2 * SQ_STAGE + (i * 5 + u) * 1024 + lane * 16, acc[i][u]);
            const int px = r >> 2;
            const unsigned fw = WS_F2 + (((px >> 1) * 4 + hh) * 64 + ((px & 1) * 8 + ct) * 4 + (r & 3)) * FS;
            const unsigned fg = WS_F2 + (15 * 64 + (tt >> 1) * 4 + (r & 3)) * FS;
            wait_flags4<12>(W, fw, fw + 128 * FS, fg, fg, epoch);
        } else {
            const unsigned yb = WS_Y + ((((mt * 2 + wm) * 8 + nt4 * 2 + wn) * 4 + ksp) * 4) * 2560;
            UNROLL for (int i = 0; i < 4; ++i) {
                UNROLL for (int u = 0; u < 4; u += 2)
                    st128(W, u32x4{pack2(acc[i][u][0], acc[i][u][1]), pack2(acc[i][u][2], acc[i][u][3]), pack2(acc[i][u + 1][0], acc[i][u + 1][1]), pack2(acc[i][u + 1][2], acc[i][u + 1][3])},
                          yb + lane * 16, i * 2560 + u * 512);
                st64(W, u32x2{pack2(acc[i][4][0], acc[i][4][1]), pack2(acc[i][4][2], acc[i][4][3])}, yb + i * 2560 + 2048 + lane * 8, 0);
            }
            publish(W, WS_F4 + (((mt * 2 + wm) * 8 + nt4 * 2 + wn) * 4 + ksp) * FS, epoch, lane);
        }
        lds_signal(pw, 18); lds_wait<2>(pr, 18);
        UNROLL for (int u = 0; u < 5; ++u) ys[u] = ldsf4(L + 2 * SQ_STAGE + (gwv * 5 + u) * 1024 + lane * 16);
        SBAR();
        UNROLL for (int st = 0; st < 5; ++st) UNROLL for (int u = 0; u < 5; ++u) wf[st][u] = ld128(W, wfo, (st * 5 + u) * 1024);
    }
    const int t0 = tt * 64;
    const float gt = __uint_as_float(ld32(W, WS_G + (t0 + gwv * 16 + l15) * 4, 0));
    wait_flags1<8>(W, WS_F4 + ((tt * 8 + ct) * 4 + ((ksp + 1 + (r % 3)) & 3)) * FS, epoch);
    {
        u32x4 ya[3][2]; u32x2 yc[3];
        UNROLL for (int p = 0; p < 3; ++p) {
            const unsigned yo = WS_Y + (((tt * 8 + ct) * 4 + ((ksp + 1 + p) & 3)) * 4 + gwv) * 2560;
            ya[p][0] = ld128(W, lane * 16, yo); ya[p][1] = ld128(W, lane * 16 + 1024, yo); yc[p] = ld64(W, lane * 8 + 2048, yo);
        }
        const float h = 1.0f - gt;
        UNROLL for (int u = 0; u < 5; ++u) {
            UNROLL for (int p = 0; p < 3; ++p) {
                const unsigned w0 = u < 4 ? ya[p][(u >> 1) & 1][(u & 1) * 2] : yc[p][0], w1 = u < 4 ? ya[p][(u >> 1) & 1][(u & 1) * 2 + 1] : yc[p][1];
                ys[u][0] += bflo(w0); ys[u][1] += bfhi(w0); ys[u][2] += bflo(w1); ys[u][3] += bfhi(w1);
            }
            ys[u] *= h; PIN(ys[u]);
        }
    }
    lds_wait<4>(L + FL_EX + fl4, 1);
    wait_flags1<4>(W, WS_F3 + (tt * 32 + gwv * 8 + (lane & 7)) * FS, epoch);
    const unsigned boff = WS_O + tt * 81920 + gwv * 5120 + lane * 16;
    u32x4 bf[4][5];
    UNROLL for (int d = 0; d < 4; ++d)
        UNROLL for (int st = 0; st < 5; ++st) bf[d][st] = ld128(W, boff + st * 1024, ((gwv + d + 1) & 3) * 20480);
    SBAR();
    f32x4 res[5];
    UNROLL for (int d = 0; d < 4; ++d) {
        UNROLL for (int u = 0; u < 5; ++u) res[u] = f32x4{};
        UNROLL for (int st = 0; st < 5; ++st) UNROLL for (int u = 0; u < 5; ++u) res[u] = MFMA16(as8(wf[st][u]), as8(bf[d][st]), res[u]);
        if (d < 3) { UNROLL for (int u = 0; u < 5; ++u) stsf4(LW + ((gwv + d + 1) & 3) * GW_LDS + (d * 5 + u) * 1024 + lane * 16, res[u]); }
        if (d == 2) lds_signal(LW + FL_RED + gwv * 4);
        SBAR();
    }
    lds_wait(L + FL_RED + fl4);
    UNROLL for (int d = 0; d < 3; ++d) UNROLL for (int u = 0; u < 5; ++u) res[u] += ldsf4(L + gwv * GW_LDS + (d * 5 + u) * 1024 + lane * 16);
    UNROLL for (int u = 0; u < 5; ++u) res[u] = res[u] * gt + ys[u];
    const rsrc_t RO = __builtin_amdgcn_make_buffer_rsrc(out, 0, 2048 * 1280, 0x00020000);
    const unsigned oo = (t0 + gwv * 16 + l15) * 1280 + ct * 160;
    UNROLL for (int u = 0; u < 4; u += 2) {
        auto s0 = __builtin_amdgcn_permlane16_swap(pack2(res[u][0], res[u][1]), pack2(res[u + 1][0], res[u + 1][1]), false, false);
        auto s1 = __builtin_amdgcn_permlane16_swap(pack2(res[u][2], res[u][3]), pack2(res[u + 1][2], res[u + 1][3]), false, false);
        __builtin_amdgcn_raw_buffer_store_b128(u32x4{s0[0], s1[0], s0[1], s1[1]}, RO, oo + (u + (kg & 1)) * 32 + (kg >> 1) * 16, 0, 2);
    }
    __builtin_amdgcn_raw_buffer_store_b64(u32x2{pack2(res[4][0], res[4][1]), pack2(res[4][2], res[4][3])}, RO, oo + 128 + kg * 8, 0, 2);
}

extern "C" void kernel_launch(void* const* d_in, const int* in_sizes, int n_in, void* d_out, int out_size,
                              void* d_ws, size_t ws_size, hipStream_t stream) {
    (void)hipFuncSetAttribute((const void*)fused, hipFuncAttributeMaxDynamicSharedMemorySize, LDS_BYTES);
    fused<<<256, 512, LDS_BYTES, stream>>>((const char*)d_in[0], (const char*)d_in[1], (const char*)d_in[2], (const char*)d_in[4],
                                           (const char*)d_in[5], (const char*)d_in[6], (const char*)d_in[7], (const char*)d_in[8],
                                           (char*)d_ws + ((ws_size - WS_END) & ~(size_t)0x1FFFFF), (char*)d_out);
}
```

```cpp
#include <hip/hip_runtime.h>
#include <hip/hip_bf16.h>
#include <stdint.h>

typedef short s16x8 __attribute__((ext_vector_type(8)));
typedef short s16x4 __attribute__((ext_vector_type(4)));
typedef float f32x16 __attribute__((ext_vector_type(16)));
typedef float f32x8 __attribute__((ext_vector_type(8)));
typedef float f32x4 __attribute__((ext_vector_type(4)));
typedef unsigned u32x4 __attribute__((ext_vector_type(4)));
typedef unsigned u32x2 __attribute__((ext_vector_type(2)));
typedef __bf16 bf16x2_t __attribute__((ext_vector_type(2)));
typedef __attribute__((address_space(3))) const char* lds_cp;
typedef __attribute__((address_space(3))) char* lds_p;
typedef __amdgpu_buffer_rsrc_t rsrc_t;

#define DI __device__ __forceinline__
#define UNROLL _Pragma("unroll")
#define NOUNROLL _Pragma("unroll 1")
#define MFMA32(a, b, c) __builtin_amdgcn_mfma_f32_32x32x16_bf16((a), (b), (c), 0, 0, 0)
#define MFMA32K8(a, b, c) __builtin_amdgcn_mfma_f32_32x32x8bf16_1k((a), (b), (c), 0, 0, 0)
#define MFMA16(a, b, c) __builtin_amdgcn_mfma_f32_16x16x32_bf16((a), (b), (c), 0, 0, 0)
#define SBAR() __builtin_amdgcn_sched_barrier(0)
#define PIN(x) asm volatile("" : "+v"(x))
#define EX2(x) __builtin_amdgcn_exp2f(x)
#define SC1 16

constexpr unsigned CH = 2048 * 16;
constexpr unsigned WS_QK = 0;
constexpr unsigned WS_O = 160 * CH;
constexpr unsigned WS_Y = WS_O + 80 * CH;
constexpr unsigned ABROW = 640 * 16;
constexpr unsigned WS_AB = WS_Y + 320 * CH;
constexpr unsigned VROW = 10240;
constexpr unsigned WS_VT = WS_AB + 512 * ABROW;
constexpr unsigned WS_WOT = WS_VT + 256 * VROW;
constexpr unsigned WS_G = WS_WOT + 80 * ABROW;
constexpr unsigned FS = 128;
constexpr unsigned WS_ACK = WS_G + 8192;
constexpr unsigned WS_EP = WS_ACK + 8192;
constexpr unsigned WS_F1 = WS_EP + 4096;
constexpr unsigned WS_F2 = WS_F1 + 1024 * FS;
constexpr unsigned WS_F3 = WS_F2 + 1024 * FS;
constexpr unsigned WS_F4 = WS_F3 + 1024 * FS;
constexpr unsigned WS_ONE = WS_F4 + 1024 * FS;
constexpr unsigned WS_END = WS_ONE + 2048 * 128;
constexpr unsigned OOB = 0x80000000u;

constexpr int P1_STAGE = 32768;
constexpr int SQ_STAGE = 36864;
constexpr int GW_LDS = 15360;
constexpr int EXB = 1536;
constexpr int LDS_EX = 4 * SQ_STAGE;
constexpr int LDS_X9 = 4 * P1_STAGE;
constexpr int LDS_GW = LDS_X9 + 20480;
constexpr int LDS_WT = LDS_GW + 2048;
constexpr int LDS_FL = LDS_EX + 8 * EXB;
constexpr int LDS_SINK = LDS_FL + 256;
constexpr int LDS_BYTES = LDS_SINK + 256;
static_assert(LDS_BYTES <= 163840 && LDS_FL >= LDS_WT + 4096, "lds");
constexpr int FL_FULL = LDS_FL, FL_EX = LDS_FL + 16, FL_SFULL = LDS_FL + 32, FL_FIN = LDS_FL + 48, FL_RED = LDS_FL + 64,
              FL_FREE = LDS_FL + 80;
constexpr int CODE_LINES = 288;


DI unsigned pack2(float a, float b) {
    bf16x2_t v = {(__bf16)a, (__bf16)b};
    return __builtin_bit_cast(unsigned, v);
}
DI float bflo(unsigned u) { return __uint_as_float(u << 16); }
DI float bfhi(unsigned u) { return __uint_as_float(u & 0xFFFF0000u); }
DI s16x8 as8(u32x4 v) { return __builtin_bit_cast(s16x8, v); }
DI s16x4 as4(u32x2 v) { return __builtin_bit_cast(s16x4, v); }

DI u32x4 mkdesc(const void* p, unsigned bytes) {
    const unsigned long long a = (unsigned long long)p;
    return u32x4{(unsigned)a, (unsigned)(a >> 32) & 0xFFFFu, bytes, 0x00020000u};
}
DI void dma16(unsigned voff, u32x4 desc, unsigned soff, unsigned ldsaddr) {
    unsigned keep;
    asm volatile("s_mov_b32 %0, m0\n\ts_mov_b32 m0, %4\n\ts_nop 0\n\tbuffer_load_dwordx4 %1, %2, %3 offen lds\n\ts_mov_b32 m0, %0"
                 : "=&s"(keep) : "v"(voff), "s"(desc), "s"(soff), "s"(ldsaddr) : "memory");
}
DI void dma16s(unsigned voff, u32x4 desc, unsigned soff, unsigned ldsaddr) {
    unsigned keep;
    asm volatile("s_mov_b32 %0, m0\n\ts_mov_b32 m0, %4\n\ts_nop 0\n\tbuffer_load_dwordx4 %1, %2, %3 offen sc1 lds\n\ts_mov_b32 m0, %0"
                 : "=&s"(keep) : "v"(voff), "s"(desc), "s"(soff), "s"(ldsaddr) : "memory");
}
DI void dma16x4(unsigned v0, unsigned v1, u32x4 desc, unsigned s0, unsigned s1, unsigned s2, unsigned s3, unsigned ldsaddr) {
    unsigned keep;
    asm volatile("s_mov_b32 %0, m0\n\ts_mov_b32 m0, %8\n\ts_nop 0\n\t"
                 "buffer_load_dwordx4 %1, %3, %4 offen lds\n\tbuffer_load_dwordx4 %2, %3, %5 offen offset:1024 lds\n\t"
                 "buffer_load_dwordx4 %1, %3, %6 offen offset:2048 lds\n\tbuffer_load_dwordx4 %2, %3, %7 offen offset:3072 lds\n\ts_mov_b32 m0, %0"
                 : "=&s"(keep) : "v"(v0), "v"(v1), "s"(desc), "s"(s0), "s"(s1), "s"(s2), "s"(s3), "s"(ldsaddr) : "memory");
}
DI void dma16sx4(unsigned voff, u32x4 desc, unsigned soff, unsigned ldsaddr) {
    unsigned keep;
    asm volatile("s_mov_b32 %0, m0\n\ts_mov_b32 m0, %4\n\ts_nop 0\n\t"
                 "buffer_load_dwordx4 %1, %2, %3 offen sc1 lds\n\tbuffer_load_dwordx4 %1, %2, %3 offen offset:1024 sc1 lds\n\t"
                 "buffer_load_dwordx4 %1, %2, %3 offen offset:2048 sc1 lds\n\tbuffer_load_dwordx4 %1, %2, %3 offen offset:3072 sc1 lds\n\ts_mov_b32 m0, %0"
                 : "=&s"(keep) : "v"(voff), "s"(desc), "s"(soff), "s"(ldsaddr) : "memory");
}
DI void dma4(unsigned voff, u32x4 desc, unsigned soff, unsigned ldsaddr) {
    unsigned keep;
    asm volatile("s_mov_b32 %0, m0\n\ts_mov_b32 m0, %4\n\ts_nop 0\n\tbuffer_load_dword %1, %2, %3 offen sc1 lds\n\ts_mov_b32 m0, %0"
                 : "=&s"(keep) : "v"(voff), "s"(desc), "s"(soff), "s"(ldsaddr) : "memory");
}
#define WAITV(n) asm volatile("s_waitcnt vmcnt(" #n ")" ::: "memory")
#define WAITL() asm volatile("s_waitcnt lgkmcnt(0)" ::: "memory")
#define BARRIER() asm volatile("s_waitcnt lgkmcnt(0)\n\ts_barrier" ::: "memory")
#define BARRIER_NOWAIT() asm volatile("s_barrier" ::: "memory")

DI s16x8 lds128(lds_cp p) { return *(const __attribute__((address_space(3))) s16x8*)p; }
DI f32x4 ldsf4(lds_cp p) { return *(const __attribute__((address_space(3))) f32x4*)p; }
DI void stsf4(lds_p p, f32x4 v) { *(__attribute__((address_space(3))) f32x4*)p = v; }
DI s16x4 ldstr(lds_cp p) {
    return __builtin_bit_cast(s16x4, __builtin_amdgcn_ds_read_tr16_b64_v4i16((__attribute__((address_space(3))) s16x4*)p));
}
DI s16x8 cat8(s16x4 a, s16x4 b) { return __builtin_shufflevector(a, b, 0, 1, 2, 3, 4, 5, 6, 7); }
DI u32x4 ld128(rsrc_t r, unsigned voff, unsigned soff) { return __builtin_amdgcn_raw_buffer_load_b128(r, voff, soff, SC1); }
DI u32x2 ld64(rsrc_t r, unsigned voff, unsigned soff) { return __builtin_amdgcn_raw_buffer_load_b64(r, voff, soff, SC1); }
DI unsigned ld32(rsrc_t r, unsigned voff, unsigned soff) { return __builtin_amdgcn_raw_buffer_load_b32(r, voff, soff, SC1); }
DI void st128(rsrc_t r, u32x4 v, unsigned voff, unsigned soff) { __builtin_amdgcn_raw_buffer_store_b128(v, r, voff + soff, 0, SC1); }
DI void st64(rsrc_t r, u32x2 v, unsigned voff, unsigned soff) { __builtin_amdgcn_raw_buffer_store_b64(v, r, voff, soff, SC1); }
DI void st32(rsrc_t r, unsigned v, unsigned voff, unsigned soff) { __builtin_amdgcn_raw_buffer_store_b32(v, r, voff, soff, SC1); }

constexpr unsigned SPIN_CAP = 200000;
DI void publish(rsrc_t w, unsigned flag_off, unsigned epoch, int lane) {
    WAITV(0);
    if (lane == 0) st32(w, epoch, flag_off, 0);
}
template <int SL>
DI void wait_flags4(rsrc_t w, unsigned o0, unsigned o1, unsigned o2, unsigned o3, unsigned epoch) {
    NOUNROLL for (unsigned spins = 0; spins < SPIN_CAP; ++spins) {
        asm volatile("" ::: "memory");
        const unsigned a = ld32(w, o0, 0), b = ld32(w, o1, 0), c = ld32(w, o2, 0), d = ld32(w, o3, 0);
        if (__all(a == epoch && b == epoch && c == epoch && d == epoch)) break;
        __builtin_amdgcn_s_sleep(SL);
    }
    asm volatile("" ::: "memory");
}
template <int SL>
DI void wait_flags1(rsrc_t w, unsigned o0, unsigned epoch) {
    NOUNROLL for (unsigned spins = 0; spins < SPIN_CAP; ++spins) {
        asm volatile("" ::: "memory");
        const unsigned a = ld32(w, o0, 0);
        if (__all(a == epoch)) break;
        __builtin_amdgcn_s_sleep(SL);
    }
    asm volatile("" ::: "memory");
}
DI void lds_signal(lds_p p, unsigned val = 1u) {
    WAITL();
    *(volatile __attribute__((address_space(3))) unsigned*)p = val;
}
DI void lds_post(lds_p p, unsigned val) { *(volatile __attribute__((address_space(3))) unsigned*)p = val; }
DI unsigned lds_peek(lds_cp p) { return *(const volatile __attribute__((address_space(3))) unsigned*)p; }
template <int SL = 1>
DI void lds_wait(lds_cp p, unsigned val = 1u) {
    NOUNROLL for (unsigned spins = 0; spins < 4 * SPIN_CAP; ++spins) {
        const unsigned v = *(const volatile __attribute__((address_space(3))) unsigned*)p;
        if (__all(v >= val)) break;
        __builtin_amdgcn_s_sleep(SL);
    }
    asm volatile("" ::: "memory");
}

__global__ __launch_bounds__(512, 2) void fused(const char* __restrict__ x, const char* __restrict__ wqkv,
                                                 const char* __restrict__ wo, const char* __restrict__ cre,
                                                 const char* __restrict__ cim, const char* __restrict__ wr,
                                                 const char* __restrict__ wi, const char* __restrict__ gw,
                                                 char* ws, char* __restrict__ out) {
    extern __shared__ __attribute__((aligned(16))) char smem[];
    unsigned long long pc; asm volatile("s_getpc_b64 %0" : "=s"(pc));
    const int tid = threadIdx.x;
    const int wid = __builtin_amdgcn_readfirstlane(tid >> 6);
    const int gwv = wid & 3;
    const int xcd = blockIdx.x & 7, idx = blockIdx.x >> 3;
    const unsigned lds0 = (unsigned)(uintptr_t)smem;
    const lds_cp L = (lds_cp)smem;
    const lds_p LW = (lds_p)smem;
    const u32x4 dx = mkdesc(x, 2048 * 1280);
    const int mt = (xcd & 1) * 8 + (idx & 7), nt = (xcd >> 1) * 4 + (idx >> 3);
    const u32x4 dw = mkdesc(wqkv, 640 * 3840);
    const rsrc_t W = __builtin_amdgcn_make_buffer_rsrc(ws, 0, WS_END, 0x00020000);
    const unsigned oneoff = WS_ONE + (blockIdx.x * 8 + wid) * 128;
    unsigned epoch;
    {
    const int lane = tid & 63;
    unsigned roff[2];
    UNROLL for (int i = 0; i < 2; ++i) roff[i] = (lane >> 3) * 1280 + (((lane & 7) ^ ((4 * i + (lane >> 4)) & 7)) << 4);
    unsigned woff[2];
    UNROLL for (int i = 0; i < 2; ++i) woff[i] = nt == 15 ? OOB : (lane >> 3) * 3840 + (((lane & 7) ^ (((lane >> 4) & 1) << 2) ^ (i << 1)) << 4);
#define P1_DX(st, p, par) dma16(roff[par], dx, (mt * 128 + (p) * 8) * 1280 + (st) * 128, lds0 + ((st) & 3) * P1_STAGE + (p) * 1024)
#define P1_DW(st, p, par) dma16(woff[par], dw, ((st) * 64 + ((p) & 7) * 8) * 3840 + nt * 256 + ((p) >> 3) * 128, lds0 + ((st) & 3) * P1_STAGE + 16384 + (p) * 1024)
    P1_DX(0, 2 * wid, 0); P1_DW(0, 2 * wid, 0); P1_DX(0, 2 * wid + 1, 1); P1_DW(0, 2 * wid + 1, 1);
    const int hh = lane >> 5;
    const int l15 = lane & 15, kg = lane >> 4;
    const unsigned fl4 = (lane & 3) * 4;
    const u32x4 dh = mkdesc(nt < 8 ? wr : wi, 640 * 1280);
#define GET_EPOCH() do { epoch = ld32(W, WS_EP, 0) + 1u; if (lane < 4) st32(W, 0x3F803F80u, oneoff + lane * 4, 0); } while (0)
#define WH_ADDR(q) ((q) < 32 ? 2 * P1_STAGE + (q) * 1024 : (q) < 48 ? 3 * P1_STAGE + 16384 + ((q) - 32) * 1024 : \
                    (q) < 68 ? LDS_X9 + ((q) - 48) * 1024 : (q) < 84 ? 16384 + ((q) - 68) * 1024 : P1_STAGE + 16384 + ((q) - 84) * 1024)

    if (wid >= 4) {
#define P1_ISSUE(st) do { \
            const unsigned x_ = (mt * 128 + gwv * 32) * 1280 + (st) * 128, w_ = ((st) * 64 + (gwv & 1) * 32) * 3840 + nt * 256 + (gwv >> 1) * 128; \
            dma16x4(roff[0], roff[1], dx, x_, x_ + 9216, x_ + 18432, x_ + 27648, lds0 + ((st) & 3) * P1_STAGE + gwv * 4096); \
            dma16x4(woff[0], woff[1], dw, w_, w_ + 29696, w_ + 59392, w_ + 89088, lds0 + ((st) & 3) * P1_STAGE + 16384 + gwv * 4096); } while (0)
#define P1_DH(q, par) do { const int q_ = (q), c_ = q_ / 10, p_ = q_ - c_ * 10; \
            dma16(roff[par], dh, ((nt & 7) * 80 + p_ * 8) * 1280 + (c_) * 128, lds0 + WH_ADDR(q_)); } while (0)
        P1_ISSUE(1);
        WAITV(8); BARRIER_NOWAIT(); P1_ISSUE(2);
        WAITV(8); BARRIER_NOWAIT(); P1_ISSUE(3); P1_ISSUE(4);
        NOUNROLL for (int k = 2; k < 7; ++k) { WAITV(16); BARRIER_NOWAIT(); P1_ISSUE(k + 3); }
        WAITV(16); BARRIER_NOWAIT();
        UNROLL for (int i = 0; i < 8; ++i) P1_DH(gwv * 8 + i, i & 1);
        WAITV(16); BARRIER_NOWAIT();
        UNROLL for (int i = 0; i < 8; ++i) P1_DH(32 + gwv * 8 + i, i & 1);
        WAITV(16); BARRIER_NOWAIT();
        UNROLL for (int i = 0; i < 6; ++i) { const int q = 64 + gwv * 6 + i; P1_DH(q < 84 ? q : 82 + (i & 1), i & 1); }
        GET_EPOCH();
        __builtin_amdgcn_s_setprio(3);
        WAITV(0); BARRIER_NOWAIT();
        UNROLL for (int i = 0; i < 4; ++i) P1_DH(84 + gwv * 4 + i, i & 1);
    } else {
        const unsigned sink = lds0 + LDS_SINK;
        const int pi = xcd * 4 + wid, pst = pi / 5, pu = pi - pst * 5;
        {
            const unsigned b = blockIdx.x;
            if (wid == 0) { dma4(lane * 128, dx, b * 10240, sink); dma4(lane < 16 ? lane * 128 : OOB, dx, b * 10240 + 8192, sink); }
            if (wid == 1) { dma4(lane * 128, dw, b * 9600, sink); dma4(lane < 11 ? lane * 128 : OOB, dw, b * 9600 + 8192, sink); }
            if (wid == 2) { dma4(OOB, dx, 0, sink); dma4(OOB, dx, 0, sink); }
        }
        if (wid == 3) {
            dma4(lane < 12 ? lane * 128 : OOB, mkdesc((const void*)(pc & ~127ull), CODE_LINES * 128), idx * 1536, sink);
            dma16(nt == 15 ? lane * 16 : OOB, mkdesc(gw, 1280), 0, lds0 + LDS_GW);
        }
        if (tid < 64) *(__attribute__((address_space(3))) unsigned*)(LW + LDS_FL + tid * 4) = 0u;
        const int wm = wid & 1, wn = wid >> 1;
        unsigned ra[2];
        UNROLL for (int ks = 0; ks < 2; ++ks) ra[ks] = l15 * 128 + (((4 * ks + kg) ^ ((l15 >> 1) & 7)) << 4);
        const unsigned xk = wm * 8192 + wn * 4096, xo = wm * 8192 + (wn ^ 1) * 4096;
        s16x8 xs[10][2][2];
#define WT_LOAD() dma16((lane >> 1) * 1280 + (lane & 1) * 16, mkdesc(wo, 640 * 1280), \
                  pi < 25 ? ((idx >> 3) * 160 + pst * 32) * 1280 + (idx & 7) * 160 + pu * 32 : OOB, lds0 + LDS_WT + wid * 1024)
#define WT_STORE() do { const lds_cp tp_ = L + LDS_WT + wid * 1024 + (8 * kg + (l15 >> 2)) * 32 + (lane & 3) * 8; \
            st128(W, __builtin_bit_cast(u32x4, cat8(ldstr(tp_), ldstr(tp_ + 128))), \
                  pi < 25 ? WS_WOT + (((idx & 7) * 4 + (idx >> 3)) * 25 + pi) * 1024 + lane * 16 : OOB, 0); } while (0)
        if (nt == 15) {
            const int m0 = mt * 128 + wm * 64 + wn * 32;
            if (wid == 3) dma16(lane * 16, mkdesc(gw, 1280), 1024, lds0 + LDS_GW + 1024);
            WT_LOAD();
            epoch = ld32(W, WS_EP, 0) + 1u;
            {
                const unsigned o0 = (mt * 4 + wid) * 12800, v1 = lane < 36 ? lane * 128 : OOB;
                dma4(lane * 128, mkdesc(wr, 640 * 1280), o0, sink); dma4(v1, mkdesc(wr, 640 * 1280), o0 + 8192, sink);
                dma4(lane * 128, mkdesc(wi, 640 * 1280), o0, sink); dma4(v1, mkdesc(wi, 640 * 1280), o0 + 8192, sink);
            }
            WAITV(4);
            float sum[2] = {0.f, 0.f};
            UNROLL for (int k = 0; k < 10; ++k) {
                BARRIER();
                UNROLL for (int ks = 0; ks < 2; ++ks) {
                    const u32x4 gv = __builtin_bit_cast(u32x4, lds128(L + LDS_GW + (k) * 128 + (4 * ks + kg) * 16));
                    UNROLL for (int h = 0; h < 2; ++h) {
                        xs[k][ks][h] = lds128(L + (k & 3) * P1_STAGE + xk + h * 2048 + ra[ks]);
                        const u32x4 xv = __builtin_bit_cast(u32x4, xs[k][ks][h]);
                        UNROLL for (int e = 0; e < 4; ++e)
                            { const unsigned xe = xv[e], ge = gv[e]; sum[h] = __builtin_amdgcn_fdot2_f32_bf16(__builtin_bit_cast(bf16x2_t, xe), __builtin_bit_cast(bf16x2_t, ge), sum[h], false); }
                    }
                }
                SBAR();
            }
            UNROLL for (int h = 0; h < 2; ++h) { sum[h] += __shfl_xor(sum[h], 16); sum[h] += __shfl_xor(sum[h], 32); }
            const float sm = kg & 1 ? sum[1] : sum[0];
            if (lane < 32) st32(W, __float_as_uint(1.0f / (1.0f + __expf(-sm))), WS_G + (m0 + lane) * 4, 0);
        } else {
            const int q = l15 >> 2;
            unsigned trb[4];
            UNROLL for (int j = 0; j < 4; ++j)
                trb[j] = wn * 8192 + (8 * kg + q) * 128 + ((j ^ (((q >> 1) & 1) << 1) ^ (kg & 1)) << 5) + 8 * (lane & 3);
            f32x4 acc[4][4];
            UNROLL for (int i = 0; i < 4; ++i) UNROLL for (int j = 0; j < 4; ++j) acc[i][j] = f32x4{};
            s16x8 xt[2][2]; s16x4 wv[4][2];
            WT_LOAD();
            epoch = ld32(W, WS_EP, 0) + 1u;
            SBAR();
            WAITV(4);
            unsigned bk[2], bo[2];
            UNROLL for (int ks = 0; ks < 2; ++ks) { bk[ks] = xk + ra[ks]; bo[ks] = xo + ra[ks]; }
#define P1_WIN(d) do { UNROLL for (int ks = 0; ks < 2; ++ks) { bk[ks] += (d); bo[ks] += (d); PIN(bk[ks]); PIN(bo[ks]); } \
                       UNROLL for (int j_ = 0; j_ < 4; ++j_) { trb[j_] += (d); PIN(trb[j_]); } } while (0)
#define P1_RW(z, j) do { const lds_cp p_ = L + trb[j] + ((((z) >> 1) & 1) * P1_STAGE + 16384 + ((z) & 1) * 4096); wv[j][0] = ldstr(p_); wv[j][1] = ldstr(p_ + 512); } while (0)
#define P1_RK(z, h) xs[(z) >> 1][(z) & 1][h] = lds128(L + bk[(z) & 1] + ((((z) >> 1) & 1) * P1_STAGE + (h) * 2048))
#define P1_RO(z, h) xt[(z) & 1][h] = lds128(L + bo[(z) & 1] + ((((z) >> 1) & 1) * P1_STAGE + (h) * 2048))
#define P1_XF(z, i) ((i) < 2 ? xs[(z) >> 1][(z) & 1][(i) & 1] : xt[(z) & 1][(i) & 1])
#define P1_LOOP(MF) \
            P1_WIN(0); \
            BARRIER(); \
            P1_RK(0, 0); P1_RW(0, 0); P1_RK(0, 1); P1_RO(0, 0); P1_RO(0, 1); P1_RW(0, 1); P1_RW(0, 2); P1_RW(0, 3); SBAR(); \
            UNROLL for (int z = 0; z < 20; ++z) { \
                UNROLL for (int j = 0; j < 4; ++j) { \
                    MF(z, 0, j); SBAR(); \
                    if (j == 0 && (z & 3) == 3 && z < 19) { P1_WIN((z & 7) == 3 ? 65536u : 0u - 65536u); SBAR(); } \
                    if (z < 19 && j == 1) { P1_RK(z + 1, 0); SBAR(); } \
                    if (z < 19 && j == 2) { P1_RO(z + 1, 0); SBAR(); } \
                    MF(z, 1, j); SBAR(); \
                    if (z < 19 && j == 1) { P1_RK(z + 1, 1); SBAR(); } \
                    if (z < 19 && j == 2) { P1_RO(z + 1, 1); SBAR(); } \
                    MF(z, 2, j); SBAR(); \
                    if (j == 0 && (z & 1) && z < 19) { BARRIER(); } \
                    MF(z, 3, j); SBAR(); \
                    if (z < 19) { P1_RW(z + 1, j); SBAR(); } \
                } \
            }
#define P1_MFQ(z, i, j) acc[i][j] = MFMA16(cat8(wv[j][0], wv[j][1]), P1_XF(z, i), acc[i][j])
#define P1_MFV(z, i, j) acc[i][j] = MFMA16(P1_XF(z, i), cat8(wv[j][0], wv[j][1]), acc[i][j])
            if (nt < 10) {
                P1_LOOP(P1_MFQ)
                const float sc = nt < 5 ? 0.32259889079f : 1.0f;
                UNROLL for (int i = 0; i < 4; i += 2)
                    UNROLL for (int j = 0; j < 4; ++j) {
                        unsigned a0 = pack2(acc[i][j][0] * sc, acc[i][j][1] * sc), a1 = pack2(acc[i][j][2] * sc, acc[i][j][3] * sc);
                        unsigned b0 = pack2(acc[i + 1][j][0] * sc, acc[i + 1][j][1] * sc), b1 = pack2(acc[i + 1][j][2] * sc, acc[i + 1][j][3] * sc);
                        auto s0 = __builtin_amdgcn_permlane16_swap(a0, b0, false, false);
                        auto s1 = __builtin_amdgcn_permlane16_swap(a1, b1, false, false);
                        st128(W, u32x4{s0[0], s1[0], s0[1], s1[1]}, WS_QK + (nt * 16 + wn * 8 + (kg >> 1)) * CH + (mt * 128 + wm * 64 + ((i >> 1) ^ wn) * 32 + (kg & 1) * 16 + l15) * 16, j * 2 * CH);
                    }
            } else {
                P1_LOOP(P1_MFV)
                UNROLL for (int i = 0; i < 4; ++i)
                    UNROLL for (int j = 0; j < 4; j += 2) {
                        unsigned a0 = pack2(acc[i][j][0], acc[i][j][1]), a1 = pack2(acc[i][j][2], acc[i][j][3]);
                        unsigned b0 = pack2(acc[i][j + 1][0], acc[i][j + 1][1]), b1 = pack2(acc[i][j + 1][2], acc[i][j + 1][3]);
                        auto s0 = __builtin_amdgcn_permlane32_swap(a0, b0, false, false);
                        auto s1 = __builtin_amdgcn_permlane32_swap(a1, b1, false, false);
                        st128(W, u32x4{s0[0], s1[0], s0[1], s1[1]}, WS_VT + ((mt * 8 + wm * 4 + ((i >> 1) ^ wn) * 2 + (i & 1)) * 2 + (kg & 1)) * VROW + ((nt - 10) * 128 + wn * 64 + hh * 16 + l15) * 16, j * 256);
                    }
            }
        }
        BARRIER();
        __builtin_amdgcn_s_setprio(2);
        f32x4 hc[2][5];
        UNROLL for (int h = 0; h < 2; ++h) UNROLL for (int u = 0; u < 5; ++u) hc[h][u] = f32x4{};
        UNROLL for (int z = 14; z < 20; ++z) UNROLL for (int h = 0; h < 2; ++h)
            xs[z >> 1][z & 1][h] = lds128(L + ((z >> 1) & 3) * P1_STAGE + xk + h * 2048 + ra[z & 1]);
        unsigned rw[3][2];
        UNROLL for (int w = 0; w < 3; ++w) UNROLL for (int ks = 0; ks < 2; ++ks) { rw[w][ks] = ra[ks] + w * 65536; PIN(rw[w][ks]); }
        s16x8 wb[2][5];
        UNROLL for (int z = 0; z < 21; ++z) {
            if (z == 3 && nt != 15) publish(W, WS_F1 + ((nt * 2 + wn) * 32 + mt * 2 + wm) * FS, epoch, lane);
            if (z == 16) { lds_wait(L + FL_FULL + fl4, 1); }
            UNROLL for (int u = 0; u < 5; ++u) {
                if (z < 20) { wb[z & 1][u] = lds128(L + rw[WH_ADDR((z >> 1) * 10 + 2 * u) >> 16][z & 1] + (WH_ADDR((z >> 1) * 10 + 2 * u) & 65535)); SBAR(); }
                if (z > 0) {
                    hc[0][u] = MFMA16(xs[(z - 1) >> 1][(z - 1) & 1][0], wb[(z - 1) & 1][u], hc[0][u]);
                    hc[1][u] = MFMA16(xs[(z - 1) >> 1][(z - 1) & 1][1], wb[(z - 1) & 1][u], hc[1][u]); SBAR();
                }
            }
        }
        WAITV(0);
        WT_STORE();
        lds_signal(LW + FL_FREE + wid * 4, 1);
        const float sg = nt < 8 ? 1.0f : -1.0f;
        const unsigned ab = WS_AB + (((nt & 7) >> 1) * 512 + (nt >> 3) * 256 + mt * 16 + wm * 8 + wn * 4 + (kg & 1) * 2 + (kg >> 1)) * 2560 + ((nt & 1) * 80 + l15) * 16;
        UNROLL for (int u = 0; u < 5; ++u) {
            unsigned a0 = pack2(hc[0][u][0] * sg, hc[0][u][1] * sg), a1 = pack2(hc[0][u][2] * sg, hc[0][u][3] * sg);
            unsigned b0 = pack2(hc[1][u][0] * sg, hc[1][u][1] * sg), b1 = pack2(hc[1][u][2] * sg, hc[1][u][3] * sg);
            auto s0 = __builtin_amdgcn_permlane16_swap(a0, b0, false, false);
            auto s1 = __builtin_amdgcn_permlane16_swap(a1, b1, false, false);
            st128(W, u32x4{s0[0], s1[0], s0[1], s1[1]}, ab, u * 256);
        }
        publish(W, WS_F2 + (nt * 64 + mt * 4 + wid) * FS, epoch, lane);
        if (lane == 0) st32(W, epoch, WS_ACK + (blockIdx.x * 8 + wid) * 4, 0);
    }
    }
    const int lane = __builtin_amdgcn_mbcnt_hi(~0u, __builtin_amdgcn_mbcnt_lo(~0u, 0u));
    const int r = lane & 31, hh = lane >> 5;
    const int l15 = lane & 15, kg = lane >> 4;
    const unsigned fl4 = (lane & 3) * 4;

    const int ksp = xcd >> 1, nt4 = idx >> 3;
    const int tt = mt * 2 + (ksp & 1), ct = nt4 * 2 + (ksp >> 1);
    const u32x4 dc = mkdesc(ksp < 2 ? cre : cim, 2048 * 4096), dws = mkdesc(ws, WS_END);
    const unsigned cs0 = mt * 128 * 4096 + (ksp & 1) * 2048;
    const unsigned as0 = WS_AB + (nt4 * 512 + ksp * 128) * 2560;

    if (wid >= 4) {
        const int head = xcd * 4 + (idx >> 3), qt = idx & 7;
        const int qh = wid & 1, kh = (wid >> 1) & 1;
        const int col0 = head * 20, odd = head & 1;
        const int cA = (col0 >> 3) + odd, cH = odd ? (col0 >> 3) : (col0 >> 3) + 2;
        const int q0 = qt * 256 + qh * 128;
        unsigned k1off = WS_QK + (80 + cA + hh) * CH + r * 16;
        unsigned k2off = hh ? OOB : WS_QK + (80 + cH) * CH + r * 16 + odd * 8;
        unsigned q2off = hh ? OOB : k2off - 80 * CH;
        unsigned k2or = hh ? 0x3F80u : 0u;
        const int dv = odd ? (r < 16 ? r + 4 : r - 16) : r;
        const unsigned voff = r < 20 ? WS_VT + hh * VROW + (col0 + dv) * 16 : (r == 20 ? oneoff : OOB);
        unsigned vstep = r < 20 ? 4 * VROW : 0;
        unsigned va = voff + kh * 32 * vstep, vb2 = va + (vstep >> 1);
        constexpr int NB = 4;
        u32x4 k1[NB], v0[NB], v1[NB]; u32x2 k2[NB];
        f32x16 o[4], s[2];
        u32x4 pa = u32x4{}, pd = u32x4{};
        f32x8 e8 = f32x8{};
        UNROLL for (int i = 0; i < 4; ++i) { o[i] = f32x16{}; PIN(o[i]); }
        v0[NB - 1] = u32x4{}; v1[NB - 1] = u32x4{};
        PIN(v0[NB - 1]); PIN(v1[NB - 1]); PIN(pa); PIN(pd); PIN(e8);
        PIN(k1off); PIN(k2off); PIN(q2off); PIN(k2or); PIN(va); PIN(vb2); PIN(vstep);
        WAITV(0);
        lds_post(LW + FL_FULL + gwv * 4, 1);
        const int nsa = (lane & 4) ? (col0 + 19) >> 6 : col0 >> 6;
        unsigned fkv = WS_F1 + ((10 + ((lane >> 3) & 1) * 10 + nsa) * 32 + kh * 16) * FS;
        __builtin_amdgcn_s_sleep(8);
        wait_flags1<4>(W, (lane & 16) ? WS_F1 + (nsa * 32 + (q0 >> 6) + (lane & 1)) * FS : fkv + (lane & 3) * FS, epoch);
        fkv += (lane & 1) * FS;
        u32x4 q1[4]; u32x2 q2[4];
        UNROLL for (int i = 0; i < 4; ++i) {
            const int qr = q0 + ((i + 2 + 2 * kh) & 3) * 32;
            q2[i] = ld64(W, q2off, qr * 16);
            q1[i] = ld128(W, k1off - 80 * CH, qr * 16);
        }
        const int t0 = kh * 32;
        unsigned fnx = epoch;
        SBAR();
        UNROLL for (int b = 0; b < NB; ++b) {
            k2[b] = ld64(W, k2off, (t0 + b) * 512); k1[b] = ld128(W, k1off, (t0 + b) * 512); SBAR();
            if (b < NB - 1) { v0[b] = ld128(W, va, 0); v1[b] = ld128(W, vb2, 0); va += vstep; vb2 += vstep; SBAR(); }
        }
        const f32x16 zero16 = f32x16{};
        {
            f32x16 t[4];
            UNROLL for (int i = 0; i < 4; ++i) { t[i] = MFMA32K8(as4(k2[0]), as4(q2[i]), zero16); t[i] = MFMA32(as8(k1[0]), as8(q1[i]), t[i]); SBAR(); }
            k2[0][0] |= k2or;
            UNROLL for (int i = 0; i < 4; ++i) {
                float m = t[i][0];
                UNROLL for (int e = 1; e < 16; ++e) m = fmaxf(m, t[i][e]);
                auto mm = __builtin_amdgcn_permlane32_swap(__float_as_uint(m), __float_as_uint(m), false, false);
                m = fmaxf(__uint_as_float(mm[0]), __uint_as_float(mm[1]));
                q2[i][0] = hh ? pack2(-m, 0.f) & 0xFFFFu : q2[i][0];
                if (i == 0) {
                    s[0] = MFMA32K8(as4(k2[0]), as4(q2[0]), zero16);
                    s[0] = MFMA32(as8(k1[0]), as8(q1[0]), s[0]);
                }
            }
        }
        st32(W, epoch, lane == 0 ? WS_ACK + (blockIdx.x * 8 + wid) * 4 : OOB, 0);
        __builtin_amdgcn_s_setprio(1);
        NOUNROLL for (int tb = 0; tb < 32; tb += NB) {
            if (__builtin_expect(!__all(fnx == epoch), 0)) wait_flags1<4>(W, fkv + (tb < 28 ? tb + 4 : 0) * (FS / 2), epoch);
            if (tb == 16) lds_wait(L + FL_FREE + fl4, 1);
            const unsigned fso = (tb < 24 ? tb + 8 : 0) * (FS / 2);
            SBAR();
            UNROLL for (int b = 0; b < NB; ++b) {
                const int bn = (b + 1) % NB, bp = (b + NB - 1) % NB;
                const unsigned kso = tb + b + NB < 32 ? (t0 + tb + b + NB) * 512 : OOB / 2;
                const unsigned vso = tb + b + NB - 1 < 32 ? 0 : OOB / 2;
                UNROLL for (int qi = 0; qi < 4; ++qi) {
                    const int c = qi & 1, n = c ^ 1, qn = (qi + 1) & 3, qp = (qi + 3) & 3;
                    const int kb = qi == 3 ? bn : b, vb = qi == 0 ? bp : b;
                    f32x16& SC = s[c]; f32x16& SN = s[n];
                    SN = MFMA32K8(as4(k2[kb]), as4(q2[qn]), zero16); SBAR();
                    SC[0] = EX2(SC[0]); SC[1] = EX2(SC[1]); SC[2] = EX2(SC[2]); SC[3] = EX2(SC[3]); SBAR();
                    pd[0] = pack2(e8[0], e8[1]); pd[1] = pack2(e8[2], e8[3]); PIN(SC); PIN(pd); SBAR();
                    SN = MFMA32(as8(k1[kb]), as8(q1[qn]), SN); SBAR();
                    if (qi == 2) { k2[b] = ld64(W, k2off, kso); k1[b] = ld128(W, k1off, kso); SBAR(); }
                    if (qi == 3 && b == 0) { fnx = ld32(W, fkv, fso); SBAR(); }
                    SC[4] = EX2(SC[4]); SC[5] = EX2(SC[5]); SC[6] = EX2(SC[6]); SC[7] = EX2(SC[7]); SBAR();
                    pd[2] = pack2(e8[4], e8[5]); pd[3] = pack2(e8[6], e8[7]); PIN(SC); PIN(pd); SBAR();
                    o[qp] = MFMA32(as8(v0[vb]), as8(pa), o[qp]); SBAR();
                    e8[0] = EX2(SC[8]); e8[1] = EX2(SC[9]); e8[2] = EX2(SC[10]); e8[3] = EX2(SC[11]); SBAR();
                    pa[0] = pack2(SC[0], SC[1]); pa[1] = pack2(SC[2], SC[3]);
                    if (qi == 2) { k2[bn][0] |= k2or; PIN(k2[bn]); }
                    if (qi == 1) { va += vstep; vb2 += vstep; PIN(va); PIN(vb2); }
                    PIN(e8); PIN(pa); SBAR();
                    o[qp] = MFMA32(as8(v1[vb]), as8(pd), o[qp]); SBAR();
                    if (qi == 0) { v0[bp] = ld128(W, va, vso); v1[bp] = ld128(W, vb2, vso); SBAR(); }
                    e8[4] = EX2(SC[12]); e8[5] = EX2(SC[13]); e8[6] = EX2(SC[14]); e8[7] = EX2(SC[15]); SBAR();
                    pa[2] = pack2(SC[4], SC[5]); pa[3] = pack2(SC[6], SC[7]); PIN(e8); PIN(pa); SBAR();
                }
            }
        }
        __builtin_amdgcn_s_setprio(3);
        UNROLL for (int e = 0; e < 4; ++e) pd[e] = pack2(e8[2 * e], e8[2 * e + 1]);
        o[3] = MFMA32(as8(v0[NB - 1]), as8(pa), o[3]);
        o[3] = MFMA32(as8(v1[NB - 1]), as8(pd), o[3]);
        SBAR();
        const int f0 = q0 + kh * 64;
        UNROLL for (int j = 0; j < 2; ++j) {
            const lds_p ex = LW + LDS_EX + (gwv * 2 + j) * EXB;
            *(__attribute__((address_space(3))) u32x4*)(ex + lane * 16) =
                u32x4{pack2(o[j][0], o[j][1]), pack2(o[j][2], o[j][3]), pack2(o[j][4], o[j][5]), pack2(o[j][6], o[j][7])};
            *(__attribute__((address_space(3))) u32x2*)(ex + 1024 + lane * 8) =
                hh ? u32x2{__float_as_uint(o[j][8]), q2[j][0] << 16} : u32x2{pack2(o[j][8], o[j][9]), pack2(o[j][10], o[j][11])};
        }
        lds_signal(LW + FL_EX + gwv * 4);
        float n1[2], l1[2];
        UNROLL for (int j = 0; j < 2; ++j) {
            auto nn = __builtin_amdgcn_permlane32_swap(q2[2 + j][0] << 16, q2[2 + j][0] << 16, false, false);
            auto ll = __builtin_amdgcn_permlane32_swap(__float_as_uint(o[2 + j][8]), __float_as_uint(o[2 + j][8]), false, false);
            n1[j] = __uint_as_float(nn[1]); l1[j] = __uint_as_float(ll[1]);
        }
        const unsigned orow = WS_O + ((f0 >> 4) + (r >> 4)) * 20480 + (r & 15) * 16;
        const unsigned oa = orow + (cA + hh) * 256, ob = hh ? OOB : orow + cH * 256 + odd * 8;
        lds_wait(L + FL_EX + (gwv ^ 2) * 4);
        bool ok = true;
        u32x4 ra[2]; u32x2 rb[2];
        UNROLL for (int j = 0; j < 2; ++j) {
            const lds_cp rx = L + LDS_EX + ((gwv ^ 2) * 2 + j) * EXB;
            const u32x4 a = *(const __attribute__((address_space(3))) u32x4*)(rx + lane * 16);
            const u32x2 c = *(const __attribute__((address_space(3))) u32x2*)(rx + 1024 + r * 8);
            const u32x2 d = *(const __attribute__((address_space(3))) u32x2*)(rx + 1280 + r * 8);
            const float n2 = __uint_as_float(d[1]), l2 = __uint_as_float(d[0]), nm = fminf(n1[j], n2);
            float w1 = EX2(nm - n1[j]), w2 = EX2(nm - n2);
            const float l = l1[j] * w1 + l2 * w2, rl = __builtin_amdgcn_rcpf(l);
            w1 *= rl; w2 *= rl;
            float f[12];
            UNROLL for (int e = 0; e < 4; ++e) { f[2 * e] = o[2 + j][2 * e] * w1 + bflo(a[e]) * w2; f[2 * e + 1] = o[2 + j][2 * e + 1] * w1 + bfhi(a[e]) * w2; }
            UNROLL for (int e = 0; e < 2; ++e) { f[8 + 2 * e] = o[2 + j][8 + 2 * e] * w1 + bflo(c[e]) * w2; f[9 + 2 * e] = o[2 + j][9 + 2 * e] * w1 + bfhi(c[e]) * w2; }
            float sa = 0.f;
            UNROLL for (int e = 0; e < 12; ++e) sa += fabsf(f[e]);
            ok = ok && l1[j] < 1e30f && l2 < 1e30f && l > 1e-18f && sa < 3e38f;
            auto s0 = __builtin_amdgcn_permlane32_swap(pack2(f[0], f[1]), pack2(f[4], f[5]), false, false);
            auto s1 = __builtin_amdgcn_permlane32_swap(pack2(f[2], f[3]), pack2(f[6], f[7]), false, false);
            ra[j] = u32x4{s0[0], s1[0], s0[1], s1[1]};
            rb[j] = u32x2{pack2(f[8], f[9]), pack2(f[10], f[11])};
        }
        if (__builtin_expect(__all(ok), 1)) {
            UNROLL for (int j = 0; j < 2; ++j) { st128(W, ra[j], oa + j * 40960, 0); st64(W, rb[j], ob + j * 40960, 0); }
        } else {
            wait_flags1<4>(W, WS_F1 + ((10 + hh * 10 + (col0 >> 6)) * 32 + r) * FS, epoch);
            wait_flags1<4>(W, WS_F1 + ((10 + hh * 10 + ((col0 + 19) >> 6)) * 32 + r) * FS, epoch);
            NOUNROLL for (int j = 0; j < 2; ++j) {
                const u32x2 z2 = ld64(W, q2off, (f0 + j * 32) * 16);
                const u32x4 z1 = ld128(W, k1off - 80 * CH, (f0 + j * 32) * 16);
                float m = -3e38f;
                f32x16 oo = f32x16{};
                NOUNROLL for (int t = 0; t < 64; ++t) {
                    const u32x4 a1 = ld128(W, k1off, t * 512); const u32x2 a2 = ld64(W, k2off, t * 512);
                    const u32x4 w0 = ld128(W, voff + t * vstep, 0), w1 = ld128(W, voff + t * vstep + (vstep >> 1), 0);
                    f32x16 z = MFMA32K8(as4(a2), as4(z2), zero16);
                    z = MFMA32(as8(a1), as8(z1), z);
                    float mx = z[0];
                    UNROLL for (int e = 1; e < 16; ++e) mx = fmaxf(mx, z[e]);
                    auto mm = __builtin_amdgcn_permlane32_swap(__float_as_uint(mx), __float_as_uint(mx), false, false);
                    const float mn = fmaxf(m, fmaxf(__uint_as_float(mm[0]), __uint_as_float(mm[1])));
                    const float al = EX2(m - mn);
                    m = mn;
                    UNROLL for (int e = 0; e < 16; ++e) oo[e] *= al;
                    u32x4 p0, p1;
                    UNROLL for (int e = 0; e < 4; ++e) { p0[e] = pack2(EX2(z[2 * e] - m), EX2(z[2 * e + 1] - m)); p1[e] = pack2(EX2(z[8 + 2 * e] - m), EX2(z[9 + 2 * e] - m)); }
                    oo = MFMA32(as8(w0), as8(p0), oo);
                    oo = MFMA32(as8(w1), as8(p1), oo);
                }
                auto l2 = __builtin_amdgcn_permlane32_swap(__float_as_uint(oo[8]), __float_as_uint(oo[8]), false, false);
                const float sc = 1.0f / __uint_as_float(l2[1]);
                auto s0 = __builtin_amdgcn_permlane32_swap(pack2(oo[0] * sc, oo[1] * sc), pack2(oo[4] * sc, oo[5] * sc), false, false);
                auto s1 = __builtin_amdgcn_permlane32_swap(pack2(oo[2] * sc, oo[3] * sc), pack2(oo[6] * sc, oo[7] * sc), false, false);
                st128(W, u32x4{s0[0], s1[0], s0[1], s1[1]}, oa + j * 40960, 0);
                st64(W, u32x2{pack2(oo[8] * sc, oo[9] * sc), pack2(oo[10] * sc, oo[11] * sc)}, ob + j * 40960, 0);
            }
        }
        publish(W, WS_F3 + ((f0 >> 6) * 32 + head) * FS, epoch, lane);
        if (blockIdx.x == 0 && wid == 4) {
            NOUNROLL for (unsigned spins = 0; spins < SPIN_CAP; ++spins) {
                asm volatile("" ::: "memory");
                bool ok = true;
                UNROLL for (int i = 0; i < 8; ++i) {
                    const u32x4 f = ld128(W, WS_ACK + i * 1024 + lane * 16, 0);
                    ok = ok && f[0] == epoch && f[1] == epoch && f[2] == epoch && f[3] == epoch;
                }
                if (__all(ok)) break;
                __builtin_amdgcn_s_sleep(32);
            }
            if (lane == 0) st32(W, epoch, WS_EP, 0);
        }
        return;
    }

    __builtin_amdgcn_s_setprio(2);
    u32x4 wf[5][5];
    {
        unsigned coff[2]; const unsigned lin = lane * 16;
        UNROLL for (int i = 0; i < 2; ++i) coff[i] = (lane >> 3) * 4096 + (((lane & 7) ^ ((4 * i + (lane >> 4)) & 7)) << 4);
#define SQ_DC4(n, slot) do { const unsigned c_ = cs0 + gwv * 131072 + (n) * 128; \
            dma16x4(coff[0], coff[1], dc, c_, c_ + 31744, c_ + 63488, c_ + 95232, lds0 + (slot) * SQ_STAGE + gwv * 4096); } while (0)
#define SQ_DA5(n, slot) do { dma16sx4(lin, dws, as0 + (n) * 20480 + gwv * 5120, lds0 + (slot) * SQ_STAGE + 16384 + gwv * 5120); SQ_DA(n, slot, 4); } while (0)
#define SQ_DA(n, slot, i) dma16s(lin, dws, as0 + (n) * 20480 + gwv * 5120 + (i) * 1024, lds0 + (slot) * SQ_STAGE + 16384 + gwv * 5120 + (i) * 1024)
        const int wm = wid & 1, wn = wid >> 1;
        f32x4 acc[4][5];
        UNROLL for (int i = 0; i < 4; ++i) UNROLL for (int u = 0; u < 5; ++u) { acc[i][u] = f32x4{}; PIN(acc[i][u]); }
        unsigned ra[2];
        UNROLL for (int ks = 0; ks < 2; ++ks) ra[ks] = wm * 8192 + l15 * 128 + (((4 * ks + kg) ^ ((l15 >> 1) & 7)) << 4);
        const unsigned rb = 16384 + kg * 2560 + (wn * 80 + l15) * 16;
        s16x8 fc[2][4], fa[2][5];
        UNROLL for (int i = 0; i < 4; ++i) fc[1][i] = s16x8{};
        UNROLL for (int u = 0; u < 5; ++u) fa[1][u] = s16x8{};
        if (wid == 0) {
            const unsigned f2 = WS_F2 + (((ksp >> 1) * 8 + nt4 * 2 + hh) * 64 + (ksp & 1) * 32 + r) * FS;
            __builtin_amdgcn_s_sleep(16);
            wait_flags1<12>(W, f2, epoch);
            lds_wait(L + FL_FREE + fl4, 1);
            lds_post(LW + FL_FIN, 1);
        } else {
            lds_wait<8>(L + FL_FIN, 1);
        }
        UNROLL for (int n = 0; n < 2; ++n) { SQ_DC4(n, n); SQ_DA5(n, n); }
        const lds_p pw = LW + (lane == 0 ? FL_SFULL + gwv * 4 : LDS_SINK + lane * 4);
        const lds_cp pr = L + FL_SFULL + fl4;
        const unsigned wfo = WS_WOT + (ct * 4 + gwv) * 25600 + lane * 16;
#define SQ_MF(u) do { UNROLL for (int i = 0; i < 4; ++i) acc[i][u] = MFMA16(fa[c][u], fc[c][i], acc[i][u]); SBAR(); } while (0)
#define SQ_RC(i) fc[n][i] = lds128(sr[ks] + (i) * 2048)
#define SQ_RA(u) fa[n][u] = lds128(sp + ks * 10240 + (u) * 256)
#define SQ_STG(DMA) do { \
            lds_cp sp = L + (j & 3) * SQ_STAGE + rb; lds_cp sr[2]; \
            UNROLL for (int ks = 0; ks < 2; ++ks) { sr[ks] = L + (j & 3) * SQ_STAGE + ra[ks]; PIN(sr[ks]); } \
            unsigned pv = j + 2, fl = 0; \
            PIN(sp); PIN(pv); SBAR(); \
            __builtin_amdgcn_s_setprio(0); \
            const int sf = (j + 2) & 3; \
            UNROLL for (int ks = 0; ks < 2; ++ks) { \
                const int n = ks, c = n ^ 1; \
                SQ_RC(0); SQ_RC(1); SQ_RC(2); SBAR(); SQ_MF(0); \
                SQ_RC(3); SQ_RA(0); SQ_RA(1); SBAR(); SQ_MF(1); \
                SQ_RA(2); SQ_RA(3); SQ_RA(4); \
                if (ks == 0) { WAITV(0); lds_post(pw, pv); } \
                SBAR(); SQ_MF(2); \
                if (DMA && ks == 0) { SQ_DC4(j + 2, sf); SBAR(); } \
                if (ks == 1) { fl = lds_peek(pr); SBAR(); } \
                SQ_MF(3); \
                if (DMA && ks == 0) { SQ_DA5(j + 2, sf); SBAR(); } \
                SQ_MF(4); \
            } \
            __builtin_amdgcn_s_setprio(3); \
            if (__builtin_expect(!__all(fl >= pv), 0)) lds_wait<3>(pr, pv); } while (0)
        int j = 0;
        WAITV(9); lds_post(pw, 1); lds_wait(pr, 1);
        NOUNROLL for (; j < 14; ++j) SQ_STG(true);
        NOUNROLL for (; j < 16; ++j) SQ_STG(false);
        UNROLL for (int u = 0; u < 5; ++u) UNROLL for (int i = 0; i < 4; ++i) acc[i][u] = MFMA16(fa[1][u], fc[1][i], acc[i][u]);
        __builtin_amdgcn_s_setprio(3);
        if (wid == ksp) {
            UNROLL for (int i = 0; i < 4; ++i) UNROLL for (int u = 0; u < 5; ++u) stsf4(LW + 2 * SQ_STAGE + (i * 5 + u) * 1024 + lane * 16, acc[i][u]);
            const int px = r >> 2;
            const unsigned fw = WS_F2 + (((px >> 1) * 4 + hh) * 64 + ((px & 1) * 8 + ct) * 4 + (r & 3)) * FS;
            const unsigned fg = WS_F2 + (15 * 64 + (tt >> 1) * 4 + (r & 3)) * FS;
            wait_flags4<12>(W, fw, fw + 128 * FS, fg, fg, epoch);
        } else {
            const unsigned yb = WS_Y + ((((mt * 2 + wm) * 8 + nt4 * 2 + wn) * 4 + ksp) * 4) * 2560;
            UNROLL for (int i = 0; i < 4; ++i) {
                UNROLL for (int u = 0; u < 4; u += 2)
                    st128(W, u32x4{pack2(acc[i][u][0], acc[i][u][1]), pack2(acc[i][u][2], acc[i][u][3]), pack2(acc[i][u + 1][0], acc[i][u + 1][1]), pack2(acc[i][u + 1][2], acc[i][u + 1][3])},
                          yb + lane * 16, i * 2560 + u * 512);
                st64(W, u32x2{pack2(acc[i][4][0], acc[i][4][1]), pack2(acc[i][4][2], acc[i][4][3])}, yb + i * 2560 + 2048 + lane * 8, 0);
            }
            publish(W, WS_F4 + (((mt * 2 + wm) * 8 + nt4 * 2 + wn) * 4 + ksp) * FS, epoch, lane);
        }
        lds_signal(pw, 18); lds_wait<8>(pr, 18);
        UNROLL for (int st = 0; st < 5; ++st) UNROLL for (int u = 0; u < 5; ++u) wf[st][u] = ld128(W, wfo, (st * 5 + u) * 1024);
    }
    const int t0 = tt * 64;
    const float gt = __uint_as_float(ld32(W, WS_G + (t0 + gwv * 16 + l15) * 4, 0));
    WAITV(0);
    lds_wait<8>(L + FL_EX + fl4, 1);
    wait_flags1<4>(W, WS_F4 + ((tt * 8 + ct) * 4 + ((ksp + 1 + (r % 3)) & 3)) * FS, epoch);
    u32x4 ya[3][2]; u32x2 yc[3];
    UNROLL for (int p = 0; p < 3; ++p) {
        const unsigned yo = WS_Y + (((tt * 8 + ct) * 4 + ((ksp + 1 + p) & 3)) * 4 + gwv) * 2560;
        ya[p][0] = ld128(W, lane * 16, yo); ya[p][1] = ld128(W, lane * 16 + 1024, yo); yc[p] = ld64(W, lane * 8 + 2048, yo);
    }
    const float h = 1.0f - gt;
    f32x4 ys[5];
    UNROLL for (int u = 0; u < 5; ++u) ys[u] = ldsf4(L + 2 * SQ_STAGE + (gwv * 5 + u) * 1024 + lane * 16);
    SBAR();
    wait_flags1<4>(W, WS_F3 + (tt * 32 + gwv * 8 + (lane & 7)) * FS, epoch);
    const unsigned boff = WS_O + tt * 81920 + gwv * 5120 + lane * 16;
    u32x4 bf[4][5];
    UNROLL for (int d = 0; d < 4; ++d)
        UNROLL for (int st = 0; st < 5; ++st) bf[d][st] = ld128(W, boff + st * 1024, ((gwv + d + 1) & 3) * 20480);
    SBAR();
    UNROLL for (int u = 0; u < 5; ++u) {
        UNROLL for (int p = 0; p < 3; ++p) {
            const unsigned w0 = u < 4 ? ya[p][(u >> 1) & 1][(u & 1) * 2] : yc[p][0], w1 = u < 4 ? ya[p][(u >> 1) & 1][(u & 1) * 2 + 1] : yc[p][1];
            ys[u][0] += bflo(w0); ys[u][1] += bfhi(w0); ys[u][2] += bflo(w1); ys[u][3] += bfhi(w1);
        }
        ys[u] *= h; PIN(ys[u]);
    }
    SBAR();
    f32x4 res[5];
    UNROLL for (int d = 0; d < 4; ++d) {
        UNROLL for (int u = 0; u < 5; ++u) res[u] = f32x4{};
        UNROLL for (int st = 0; st < 5; ++st) UNROLL for (int u = 0; u < 5; ++u) res[u] = MFMA16(as8(wf[st][u]), as8(bf[d][st]), res[u]);
        if (d < 3) { UNROLL for (int u = 0; u < 5; ++u) stsf4(LW + ((gwv + d + 1) & 3) * GW_LDS + (d * 5 + u) * 1024 + lane * 16, res[u]); }
        if (d == 2) lds_signal(LW + FL_RED + gwv * 4);
        SBAR();
    }
    lds_wait(L + FL_RED + fl4);
    UNROLL for (int d = 0; d < 3; ++d) UNROLL for (int u = 0; u < 5; ++u) res[u] += ldsf4(L + gwv * GW_LDS + (d * 5 + u) * 1024 + lane * 16);
    UNROLL for (int u = 0; u < 5; ++u) res[u] = res[u] * gt + ys[u];
    const rsrc_t RO = __builtin_amdgcn_make_buffer_rsrc(out, 0, 2048 * 1280, 0x00020000);
    const unsigned oo = (t0 + gwv * 16 + l15) * 1280 + ct * 160;
    UNROLL for (int u = 0; u < 4; u += 2) {
        auto s0 = __builtin_amdgcn_permlane16_swap(pack2(res[u][0], res[u][1]), pack2(res[u + 1][0], res[u + 1][1]), false, false);
        auto s1 = __builtin_amdgcn_permlane16_swap(pack2(res[u][2], res[u][3]), pack2(res[u + 1][2], res[u + 1][3]), false, false);
        __builtin_amdgcn_raw_buffer_store_b128(u32x4{s0[0], s1[0], s0[1], s1[1]}, RO, oo + (u + (kg & 1)) * 32 + (kg >> 1) * 16, 0, 2);
    }
    __builtin_amdgcn_raw_buffer_store_b64(u32x2{pack2(res[4][0], res[4][1]), pack2(res[4][2], res[4][3])}, RO, oo + 128 + kg * 8, 0, 2);
}

extern "C" void kernel_launch(void* const* d_in, const int* in_sizes, int n_in, void* d_out, int out_size,
                              void* d_ws, size_t ws_size, hipStream_t stream) {
    (void)hipFuncSetAttribute((const void*)fused, hipFuncAttributeMaxDynamicSharedMemorySize, LDS_BYTES);
    fused<<<256, 512, LDS_BYTES, stream>>>((const char*)d_in[0], (const char*)d_in[1], (const char*)d_in[2], (const char*)d_in[4],
                                           (const char*)d_in[5], (const char*)d_in[6], (const char*)d_in[7], (const char*)d_in[8],
                                           (char*)d_ws + ((ws_size - WS_END) & ~(size_t)0x1FFFFF), (char*)d_out);
}
```

```cpp
#include <hip/hip_runtime.h>
#include <hip/hip_bf16.h>
#include <stdint.h>

typedef short s16x8 __attribute__((ext_vector_type(8)));
typedef short s16x4 __attribute__((ext_vector_type(4)));
typedef float f32x16 __attribute__((ext_vector_type(16)));
typedef float f32x4 __attribute__((ext_vector_type(4)));
typedef unsigned u32x4 __attribute__((ext_vector_type(4)));
typedef unsigned u32x2 __attribute__((ext_vector_type(2)));
typedef __bf16 bf16x2_t __attribute__((ext_vector_type(2)));
typedef __attribute__((address_space(3))) const char* lds_cp;
typedef __attribute__((address_space(3))) char* lds_p;
typedef __amdgpu_buffer_rsrc_t rsrc_t;

#define DI __device__ __forceinline__
#define UNROLL _Pragma("unroll")
#define NOUNROLL _Pragma("unroll 1")
#define MFMA32(a, b, c) __builtin_amdgcn_mfma_f32_32x32x16_bf16((a), (b), (c), 0, 0, 0)
#define MFMA32K8(a, b, c) __builtin_amdgcn_mfma_f32_32x32x8bf16_1k((a), (b), (c), 0, 0, 0)
#define MFMA16(a, b, c) __builtin_amdgcn_mfma_f32_16x16x32_bf16((a), (b), (c), 0, 0, 0)
typedef __bf16 bf16x8_t __attribute__((ext_vector_type(8)));
typedef __bf16 bf16x16_t __attribute__((ext_vector_type(16)));
#define SPV(V, S, h, O) __builtin_amdgcn_smfmac_f32_32x32x32_bf16(__builtin_bit_cast(bf16x8_t, V), __builtin_bit_cast(bf16x16_t, \
    __builtin_shufflevector(S, S, 8 * (h), 8 * (h) + 1, 8 * (h) + 2, 8 * (h) + 3, 8 * (h) + 4, 8 * (h) + 5, 8 * (h) + 6, 8 * (h) + 7)), O, sidx, 0, 0)
#define SBAR() __builtin_amdgcn_sched_barrier(0)
#define PIN(x) asm volatile("" : "+v"(x))
#define EX2(x) __builtin_amdgcn_exp2f(x)
#define SC1 16

constexpr unsigned CH = 2048 * 16;
constexpr unsigned WS_QK = 0;
constexpr unsigned WS_O = 160 * CH;
constexpr unsigned WS_Y = WS_O + 80 * CH;
constexpr unsigned ABROW = 640 * 16;
constexpr unsigned WS_AB = WS_Y + 320 * CH;
constexpr unsigned VROW = 10240;
constexpr unsigned WS_VT = WS_AB + 512 * ABROW;
constexpr unsigned WS_WOT = WS_VT + 256 * VROW;
constexpr unsigned WS_G = WS_WOT + 80 * ABROW;
constexpr unsigned FS = 128;
constexpr unsigned WS_ACK = WS_G + 8192;
constexpr unsigned WS_EP = WS_ACK + 8192;
constexpr unsigned WS_F1 = WS_EP + 4096;
constexpr unsigned WS_F2 = WS_F1 + 1024 * FS;
constexpr unsigned WS_F3 = WS_F2 + 1024 * FS;
constexpr unsigned WS_F4 = WS_F3 + 1024 * FS;
constexpr unsigned WS_ONE = WS_F4 + 1024 * FS;
constexpr unsigned WS_END = WS_ONE + 2048 * 128;
constexpr unsigned OOB = 0x80000000u;

constexpr int P1_STAGE = 32768;
constexpr int SQ_STAGE = 36864;
constexpr int GW_LDS = 15360;
constexpr int EXB = 1536;
constexpr int LDS_EX = 4 * SQ_STAGE;
constexpr int LDS_X9 = 4 * P1_STAGE;
constexpr int LDS_GW = LDS_X9 + 20480;
constexpr int LDS_WT = LDS_GW + 2048;
constexpr int LDS_FL = LDS_EX + 8 * EXB;
constexpr int LDS_SINK = LDS_FL + 256;
constexpr int LDS_BYTES = LDS_SINK + 256;
static_assert(LDS_BYTES <= 163840 && LDS_FL >= LDS_WT + 4096, "lds");
constexpr int FL_FULL = LDS_FL, FL_EX = LDS_FL + 16, FL_SFULL = LDS_FL + 32, FL_FIN = LDS_FL + 48, FL_RED = LDS_FL + 64,
              FL_FREE = LDS_FL + 80;
constexpr int CODE_LINES = 280;


DI unsigned pack2(float a, float b) {
    bf16x2_t v = {(__bf16)a, (__bf16)b};
    return __builtin_bit_cast(unsigned, v);
}
DI float bflo(unsigned u) { return __uint_as_float(u << 16); }
DI float bfhi(unsigned u) { return __uint_as_float(u & 0xFFFF0000u); }
DI s16x8 as8(u32x4 v) { return __builtin_bit_cast(s16x8, v); }
DI s16x4 as4(u32x2 v) { return __builtin_bit_cast(s16x4, v); }

DI u32x4 mkdesc(const void* p, unsigned bytes) {
    const unsigned long long a = (unsigned long long)p;
    return u32x4{(unsigned)a, (unsigned)(a >> 32) & 0xFFFFu, bytes, 0x00020000u};
}
DI void dma16(unsigned voff, u32x4 desc, unsigned soff, unsigned ldsaddr) {
    unsigned keep;
    asm volatile("s_mov_b32 %0, m0\n\ts_mov_b32 m0, %4\n\ts_nop 0\n\tbuffer_load_dwordx4 %1, %2, %3 offen lds\n\ts_mov_b32 m0, %0"
                 : "=&s"(keep) : "v"(voff), "s"(desc), "s"(soff), "s"(ldsaddr) : "memory");
}
DI void dma16s(unsigned voff, u32x4 desc, unsigned soff, unsigned ldsaddr) {
    unsigned keep;
    asm volatile("s_mov_b32 %0, m0\n\ts_mov_b32 m0, %4\n\ts_nop 0\n\tbuffer_load_dwordx4 %1, %2, %3 offen sc1 lds\n\ts_mov_b32 m0, %0"
                 : "=&s"(keep) : "v"(voff), "s"(desc), "s"(soff), "s"(ldsaddr) : "memory");
}
DI void dma16x4(unsigned v0, unsigned v1, u32x4 desc, unsigned s0, unsigned s1, unsigned s2, unsigned s3, unsigned ldsaddr) {
    unsigned keep;
    asm volatile("s_mov_b32 %0, m0\n\ts_mov_b32 m0, %8\n\ts_nop 0\n\t"
                 "buffer_load_dwordx4 %1, %3, %4 offen lds\n\tbuffer_load_dwordx4 %2, %3, %5 offen offset:1024 lds\n\t"
                 "buffer_load_dwordx4 %1, %3, %6 offen offset:2048 lds\n\tbuffer_load_dwordx4 %2, %3, %7 offen offset:3072 lds\n\ts_mov_b32 m0, %0"
                 : "=&s"(keep) : "v"(v0), "v"(v1), "s"(desc), "s"(s0), "s"(s1), "s"(s2), "s"(s3), "s"(ldsaddr) : "memory");
}
DI void dma16sx4(unsigned voff, u32x4 desc, unsigned soff, unsigned ldsaddr) {
    unsigned keep;
    asm volatile("s_mov_b32 %0, m0\n\ts_mov_b32 m0, %4\n\ts_nop 0\n\t"
                 "buffer_load_dwordx4 %1, %2, %3 offen sc1 lds\n\tbuffer_load_dwordx4 %1, %2, %3 offen offset:1024 sc1 lds\n\t"
                 "buffer_load_dwordx4 %1, %2, %3 offen offset:2048 sc1 lds\n\tbuffer_load_dwordx4 %1, %2, %3 offen offset:3072 sc1 lds\n\ts_mov_b32 m0, %0"
                 : "=&s"(keep) : "v"(voff), "s"(desc), "s"(soff), "s"(ldsaddr) : "memory");
}
DI void dma4(unsigned voff, u32x4 desc, unsigned soff, unsigned ldsaddr) {
    unsigned keep;
    asm volatile("s_mov_b32 %0, m0\n\ts_mov_b32 m0, %4\n\ts_nop 0\n\tbuffer_load_dword %1, %2, %3 offen sc1 lds\n\ts_mov_b32 m0, %0"
                 : "=&s"(keep) : "v"(voff), "s"(desc), "s"(soff), "s"(ldsaddr) : "memory");
}
#define WAITV(n) asm volatile("s_waitcnt vmcnt(" #n ")" ::: "memory")
#define WAITL() asm volatile("s_waitcnt lgkmcnt(0)" ::: "memory")
#define BARRIER() asm volatile("s_waitcnt lgkmcnt(0)\n\ts_barrier" ::: "memory")
#define BARRIER_NOWAIT() asm volatile("s_barrier" ::: "memory")

DI s16x8 lds128(lds_cp p) { return *(const __attribute__((address_space(3))) s16x8*)p; }
DI f32x4 ldsf4(lds_cp p) { return *(const __attribute__((address_space(3))) f32x4*)p; }
DI void stsf4(lds_p p, f32x4 v) { *(__attribute__((address_space(3))) f32x4*)p = v; }
DI s16x4 ldstr(lds_cp p) {
    return __builtin_bit_cast(s16x4, __builtin_amdgcn_ds_read_tr16_b64_v4i16((__attribute__((address_space(3))) s16x4*)p));
}
DI s16x8 cat8(s16x4 a, s16x4 b) { return __builtin_shufflevector(a, b, 0, 1, 2, 3, 4, 5, 6, 7); }
DI u32x4 ld128(rsrc_t r, unsigned voff, unsigned soff) { return __builtin_amdgcn_raw_buffer_load_b128(r, voff, soff, SC1); }
DI u32x2 ld64(rsrc_t r, unsigned voff, unsigned soff) { return __builtin_amdgcn_raw_buffer_load_b64(r, voff, soff, SC1); }
DI unsigned ld32(rsrc_t r, unsigned voff, unsigned soff) { return __builtin_amdgcn_raw_buffer_load_b32(r, voff, soff, SC1); }
DI void st128(rsrc_t r, u32x4 v, unsigned voff, unsigned soff) { __builtin_amdgcn_raw_buffer_store_b128(v, r, voff + soff, 0, SC1); }
DI void st64(rsrc_t r, u32x2 v, unsigned voff, unsigned soff) { __builtin_amdgcn_raw_buffer_store_b64(v, r, voff, soff, SC1); }
DI void st32(rsrc_t r, unsigned v, unsigned voff, unsigned soff) { __builtin_amdgcn_raw_buffer_store_b32(v, r, voff, soff, SC1); }

constexpr unsigned SPIN_CAP = 200000;
DI void publish(rsrc_t w, unsigned flag_off, unsigned epoch, int lane) {
    WAITV(0);
    if (lane == 0) st32(w, epoch, flag_off, 0);
}
template <int SL>
DI void wait_flags1(rsrc_t w, unsigned o0, unsigned epoch) {
    NOUNROLL for (unsigned spins = 0; spins < SPIN_CAP; ++spins) {
        asm volatile("" ::: "memory");
        const unsigned a = ld32(w, o0, 0);
        if (__all(a == epoch)) break;
        __builtin_amdgcn_s_sleep(SL);
    }
    asm volatile("" ::: "memory");
}
DI void lds_signal(lds_p p, unsigned val = 1u) {
    WAITL();
    *(volatile __attribute__((address_space(3))) unsigned*)p = val;
}
DI void lds_post(lds_p p, unsigned val) { *(volatile __attribute__((address_space(3))) unsigned*)p = val; }
DI unsigned lds_peek(lds_cp p) { return *(const volatile __attribute__((address_space(3))) unsigned*)p; }
template <int SL = 1>
DI void lds_wait(lds_cp p, unsigned val = 1u) {
    NOUNROLL for (unsigned spins = 0; spins < 4 * SPIN_CAP; ++spins) {
        const unsigned v = *(const volatile __attribute__((address_space(3))) unsigned*)p;
        if (__all(v >= val)) break;
        __builtin_amdgcn_s_sleep(SL);
    }
    asm volatile("" ::: "memory");
}

__global__ __launch_bounds__(512, 2) void fused(const char* __restrict__ x, const char* __restrict__ wqkv,
                                                 const char* __restrict__ wo, const char* __restrict__ cre,
                                                 const char* __restrict__ cim, const char* __restrict__ wr,
                                                 const char* __restrict__ wi, const char* __restrict__ gw,
                                                 char* ws, char* __restrict__ out) {
    extern __shared__ __attribute__((aligned(16))) char smem[];
    unsigned long long pc; asm volatile("s_getpc_b64 %0" : "=s"(pc));
    const int tid = threadIdx.x;
    const int wid = __builtin_amdgcn_readfirstlane(tid >> 6);
    const int gwv = wid & 3;
    const int xcd = blockIdx.x & 7, idx = blockIdx.x >> 3;
    const unsigned lds0 = (unsigned)(uintptr_t)smem;
    const lds_cp L = (lds_cp)smem;
    const lds_p LW = (lds_p)smem;
    const u32x4 dx = mkdesc(x, 2048 * 1280);
    const int mt = (xcd & 1) * 8 + (idx & 7), nt = (xcd >> 1) * 4 + (idx >> 3);
    const u32x4 dw = mkdesc(wqkv, 640 * 3840);
    const rsrc_t W = __builtin_amdgcn_make_buffer_rsrc(ws, 0, WS_END, 0x00020000);
    const unsigned oneoff = WS_ONE + (blockIdx.x * 8 + wid) * 128;
    unsigned epoch;
    {
    const int lane = tid & 63;
    unsigned roff[2];
    UNROLL for (int i = 0; i < 2; ++i) roff[i] = (lane >> 3) * 1280 + (((lane & 7) ^ ((4 * i + (lane >> 4)) & 7)) << 4);
    unsigned woff[2];
    UNROLL for (int i = 0; i < 2; ++i) woff[i] = nt == 15 ? OOB : (lane >> 3) * 3840 + (((lane & 7) ^ (((lane >> 4) & 1) << 2) ^ (i << 1)) << 4);
#define P1_DX(st, p, par) dma16(roff[par], dx, (mt * 128 + (p) * 8) * 1280 + (st) * 128, lds0 + ((st) & 3) * P1_STAGE + (p) * 1024)
#define P1_DW(st, p, par) dma16(woff[par], dw, ((st) * 64 + ((p) & 7) * 8) * 3840 + nt * 256 + ((p) >> 3) * 128, lds0 + ((st) & 3) * P1_STAGE + 16384 + (p) * 1024)
    P1_DX(0, 2 * wid, 0); P1_DW(0, 2 * wid, 0); P1_DX(0, 2 * wid + 1, 1); P1_DW(0, 2 * wid + 1, 1);
    const int l15 = lane & 15, kg = lane >> 4;
    const unsigned fl4 = (lane & 3) * 4;
    const u32x4 dh = mkdesc(nt < 8 ? wr : wi, 640 * 1280);
#define GET_EPOCH() do { epoch = ld32(W, WS_EP, 0) + 1u; if (lane < 4) st32(W, 0x3F803F80u, oneoff + lane * 4, 0); } while (0)
#define WH_ADDR(q) ((q) < 32 ? 2 * P1_STAGE + (q) * 1024 : (q) < 48 ? 3 * P1_STAGE + 16384 + ((q) - 32) * 1024 : \
                    (q) < 68 ? LDS_X9 + ((q) - 48) * 1024 : (q) < 84 ? 16384 + ((q) - 68) * 1024 : P1_STAGE + 16384 + ((q) - 84) * 1024)

    if (wid >= 4) {
#define P1_ISSUE(st) do { \
            const unsigned x_ = (mt * 128 + gwv * 32) * 1280 + (st) * 128, w_ = ((st) * 64 + (gwv & 1) * 32) * 3840 + nt * 256 + (gwv >> 1) * 128; \
            dma16x4(roff[0], roff[1], dx, x_, x_ + 9216, x_ + 18432, x_ + 27648, lds0 + ((st) & 3) * P1_STAGE + gwv * 4096); \
            dma16x4(woff[0], woff[1], dw, w_, w_ + 29696, w_ + 59392, w_ + 89088, lds0 + ((st) & 3) * P1_STAGE + 16384 + gwv * 4096); } while (0)
#define P1_DH(q, par) do { const int q_ = (q), c_ = q_ / 10, p_ = q_ - c_ * 10; \
            dma16(roff[par], dh, ((nt & 7) * 80 + p_ * 8) * 1280 + (c_) * 128, lds0 + WH_ADDR(q_)); } while (0)
        P1_ISSUE(1);
        WAITV(8); BARRIER_NOWAIT(); P1_ISSUE(2);
        WAITV(8); BARRIER_NOWAIT(); P1_ISSUE(3); P1_ISSUE(4);
        NOUNROLL for (int k = 2; k < 7; ++k) { WAITV(16); BARRIER_NOWAIT(); P1_ISSUE(k + 3); }
        WAITV(16); BARRIER_NOWAIT();
        UNROLL for (int i = 0; i < 8; ++i) P1_DH(gwv * 8 + i, i & 1);
        WAITV(16); BARRIER_NOWAIT();
        UNROLL for (int i = 0; i < 8; ++i) P1_DH(32 + gwv * 8 + i, i & 1);
        WAITV(16); BARRIER_NOWAIT();
        UNROLL for (int i = 0; i < 6; ++i) { const int q = 64 + gwv * 6 + i; P1_DH(q < 84 ? q : 82 + (i & 1), i & 1); }
        GET_EPOCH();
        __builtin_amdgcn_s_setprio(3);
        WAITV(0); BARRIER_NOWAIT();
        UNROLL for (int i = 0; i < 4; ++i) P1_DH(84 + gwv * 4 + i, i & 1);
    } else {
        const unsigned sink = lds0 + LDS_SINK;
        const int pi = xcd * 4 + wid, pst = pi / 5, pu = pi - pst * 5;
        {
            const unsigned b = blockIdx.x;
            if (wid == 0) { dma4(lane * 128, dx, b * 10240, sink); dma4(lane < 16 ? lane * 128 : OOB, dx, b * 10240 + 8192, sink); }
            if (wid == 1) { dma4(lane * 128, dw, b * 9600, sink); dma4(lane < 11 ? lane * 128 : OOB, dw, b * 9600 + 8192, sink); }
            if (wid == 2) { dma4(OOB, dx, 0, sink); dma4(OOB, dx, 0, sink); }
        }
        if (wid == 3) {
            dma4(lane < 12 ? lane * 128 : OOB, mkdesc((const void*)(pc & ~127ull), CODE_LINES * 128), idx * 1536, sink);
            dma16(nt == 15 ? lane * 16 : OOB, mkdesc(gw, 1280), 0, lds0 + LDS_GW);
        }
        if (tid < 64) *(__attribute__((address_space(3))) unsigned*)(LW + LDS_FL + tid * 4) = 0u;
        const int wm = wid & 1, wn = wid >> 1;
        unsigned ra[2];
        UNROLL for (int ks = 0; ks < 2; ++ks) ra[ks] = l15 * 128 + (((4 * ks + kg) ^ ((l15 >> 1) & 7)) << 4);
        const unsigned xk = wm * 8192 + wn * 4096, xo = wm * 8192 + (wn ^ 1) * 4096;
        s16x8 xs[10][2][2];
#define WT_LOAD() dma16((lane >> 1) * 1280 + (lane & 1) * 16, mkdesc(wo, 640 * 1280), \
                  pi < 25 ? ((idx >> 3) * 160 + pst * 32) * 1280 + (idx & 7) * 160 + pu * 32 : OOB, lds0 + LDS_WT + wid * 1024)
#define WT_STORE() do { const lds_cp tp_ = L + LDS_WT + wid * 1024 + (8 * kg + (l15 >> 2)) * 32 + (lane & 3) * 8; \
            st128(W, __builtin_bit_cast(u32x4, cat8(ldstr(tp_), ldstr(tp_ + 128))), \
                  pi < 25 ? WS_WOT + (((idx & 7) * 4 + (idx >> 3)) * 25 + pi) * 1024 + lane * 16 : OOB, 0); } while (0)
        if (nt == 15) {
            const int m0 = mt * 128 + wm * 64 + wn * 32;
            if (wid == 3) dma16(lane * 16, mkdesc(gw, 1280), 1024, lds0 + LDS_GW + 1024);
            WT_LOAD();
            epoch = ld32(W, WS_EP, 0) + 1u;
            {
                const unsigned o0 = (mt * 4 + wid) * 12800, v1 = lane < 36 ? lane * 128 : OOB;
                dma4(lane * 128, mkdesc(wr, 640 * 1280), o0, sink); dma4(v1, mkdesc(wr, 640 * 1280), o0 + 8192, sink);
                dma4(lane * 128, mkdesc(wi, 640 * 1280), o0, sink); dma4(v1, mkdesc(wi, 640 * 1280), o0 + 8192, sink);
            }
            WAITV(4);
            float sum[2] = {0.f, 0.f};
            UNROLL for (int k = 0; k < 10; ++k) {
                BARRIER();
                UNROLL for (int ks = 0; ks < 2; ++ks) {
                    const u32x4 gv = __builtin_bit_cast(u32x4, lds128(L + LDS_GW + (k) * 128 + (4 * ks + kg) * 16));
                    UNROLL for (int h = 0; h < 2; ++h) {
                        xs[k][ks][h] = lds128(L + (k & 3) * P1_STAGE + xk + h * 2048 + ra[ks]);
                        const u32x4 xv = __builtin_bit_cast(u32x4, xs[k][ks][h]);
                        UNROLL for (int e = 0; e < 4; ++e)
                            { const unsigned xe = xv[e], ge = gv[e]; sum[h] = __builtin_amdgcn_fdot2_f32_bf16(__builtin_bit_cast(bf16x2_t, xe), __builtin_bit_cast(bf16x2_t, ge), sum[h], false); }
                    }
                }
                SBAR();
            }
            UNROLL for (int h = 0; h < 2; ++h) { sum[h] += __shfl_xor(sum[h], 16); sum[h] += __shfl_xor(sum[h], 32); }
            const float sm = kg & 1 ? sum[1] : sum[0];
            if (lane < 32) st32(W, __float_as_uint(1.0f / (1.0f + __expf(-sm))), WS_G + (m0 + lane) * 4, 0);
        } else {
            const int q = l15 >> 2;
            unsigned trb[4];
            UNROLL for (int j = 0; j < 4; ++j)
                trb[j] = wn * 8192 + (8 * kg + q) * 128 + ((j ^ (((q >> 1) & 1) << 1) ^ (kg & 1)) << 5) + 8 * (lane & 3);
            f32x4 acc[4][4];
            UNROLL for (int i = 0; i < 4; ++i) UNROLL for (int j = 0; j < 4; ++j) acc[i][j] = f32x4{};
            s16x8 xt[2][2]; s16x4 wv[4][2];
            WT_LOAD();
            epoch = ld32(W, WS_EP, 0) + 1u;
            SBAR();
            WAITV(4);
            unsigned bk[2], bo[2];
            UNROLL for (int ks = 0; ks < 2; ++ks) { bk[ks] = xk + ra[ks]; bo[ks] = xo + ra[ks]; }
#define P1_WIN(d) do { UNROLL for (int ks = 0; ks < 2; ++ks) { bk[ks] += (d); bo[ks] += (d); PIN(bk[ks]); PIN(bo[ks]); } \
                       UNROLL for (int j_ = 0; j_ < 4; ++j_) { trb[j_] += (d); PIN(trb[j_]); } } while (0)
#define P1_RW(z, j) do { const lds_cp p_ = L + trb[j] + ((((z) >> 1) & 1) * P1_STAGE + 16384 + ((z) & 1) * 4096); wv[j][0] = ldstr(p_); wv[j][1] = ldstr(p_ + 512); } while (0)
#define P1_RK(z, h) xs[(z) >> 1][(z) & 1][h] = lds128(L + bk[(z) & 1] + ((((z) >> 1) & 1) * P1_STAGE + (h) * 2048))
#define P1_RO(z, h) xt[(z) & 1][h] = lds128(L + bo[(z) & 1] + ((((z) >> 1) & 1) * P1_STAGE + (h) * 2048))
#define P1_XF(z, i) ((i) < 2 ? xs[(z) >> 1][(z) & 1][(i) & 1] : xt[(z) & 1][(i) & 1])
#define P1_LOOP(MF) \
            P1_WIN(0); \
            BARRIER(); \
            P1_RK(0, 0); P1_RW(0, 0); P1_RK(0, 1); P1_RO(0, 0); P1_RO(0, 1); P1_RW(0, 1); P1_RW(0, 2); P1_RW(0, 3); SBAR(); \
            UNROLL for (int z = 0; z < 20; ++z) { \
                UNROLL for (int j = 0; j < 4; ++j) { \
                    MF(z, 0, j); SBAR(); \
                    if (j == 0 && (z & 3) == 3 && z < 19) { P1_WIN((z & 7) == 3 ? 65536u : 0u - 65536u); SBAR(); } \
                    if (z < 19 && j == 1) { P1_RK(z + 1, 0); SBAR(); } \
                    if (z < 19 && j == 2) { P1_RO(z + 1, 0); SBAR(); } \
                    MF(z, 1, j); SBAR(); \
                    if (z < 19 && j == 1) { P1_RK(z + 1, 1); SBAR(); } \
                    if (z < 19 && j == 2) { P1_RO(z + 1, 1); SBAR(); } \
                    MF(z, 2, j); SBAR(); \
                    if (j == 0 && (z & 1) && z < 19) { BARRIER(); } \
                    MF(z, 3, j); SBAR(); \
                    if (z < 19) { P1_RW(z + 1, j); SBAR(); } \
                } \
            }
#define P1_MFQ(z, i, j) acc[i][j] = MFMA16(cat8(wv[j][0], wv[j][1]), P1_XF(z, i), acc[i][j])
#define P1_MFV(z, i, j) acc[i][j] = MFMA16(P1_XF(z, i), cat8(wv[j][0], wv[j][1]), acc[i][j])
            if (nt < 10) {
                P1_LOOP(P1_MFQ)
                const float sc = nt < 5 ? 0.32259641823f : 1.0f;
                UNROLL for (int i = 0; i < 4; i += 2)
                    UNROLL for (int j = 0; j < 4; ++j) {
                        unsigned a0 = pack2(acc[i][j][0] * sc, acc[i][j][1] * sc), a1 = pack2(acc[i][j][2] * sc, acc[i][j][3] * sc);
                        unsigned b0 = pack2(acc[i + 1][j][0] * sc, acc[i + 1][j][1] * sc), b1 = pack2(acc[i + 1][j][2] * sc, acc[i + 1][j][3] * sc);
                        auto s0 = __builtin_amdgcn_permlane16_swap(a0, b0, false, false);
                        auto s1 = __builtin_amdgcn_permlane16_swap(a1, b1, false, false);
                        st128(W, u32x4{s0[0], s1[0], s0[1], s1[1]}, WS_QK + (nt * 16 + wn * 8 + (kg >> 1)) * CH + (mt * 128 + wm * 64 + ((i >> 1) ^ wn) * 32 + (kg & 1) * 16 + l15) * 16, j * 2 * CH);
                    }
            } else {
                P1_LOOP(P1_MFV)
                UNROLL for (int i = 0; i < 4; ++i)
                    UNROLL for (int j = 0; j < 4; j += 2) {
                        unsigned a0 = pack2(acc[i][j][0], acc[i][j][1]), a1 = pack2(acc[i][j][2], acc[i][j][3]);
                        unsigned b0 = pack2(acc[i][j + 1][0], acc[i][j + 1][1]), b1 = pack2(acc[i][j + 1][2], acc[i][j + 1][3]);
                        auto s0 = __builtin_amdgcn_permlane16_swap(a0, b0, false, false);
                        auto s1 = __builtin_amdgcn_permlane16_swap(a1, b1, false, false);
                        st128(W, u32x4{s0[0], s1[0], s0[1], s1[1]}, WS_VT + ((mt * 8 + wm * 4 + ((i >> 1) ^ wn) * 2 + (i & 1)) * 2 + (kg >> 1)) * VROW + ((nt - 10) * 128 + wn * 64 + (kg & 1) * 16 + l15) * 16, j * 256);
                    }
            }
        }
        BARRIER();
        __builtin_amdgcn_s_setprio(2);
        f32x4 hc[2][5];
        UNROLL for (int h = 0; h < 2; ++h) UNROLL for (int u = 0; u < 5; ++u) hc[h][u] = f32x4{};
        UNROLL for (int z = 14; z < 20; ++z) UNROLL for (int h = 0; h < 2; ++h)
            xs[z >> 1][z & 1][h] = lds128(L + ((z >> 1) & 3) * P1_STAGE + xk + h * 2048 + ra[z & 1]);
        unsigned rw[3][2];
        UNROLL for (int w = 0; w < 3; ++w) UNROLL for (int ks = 0; ks < 2; ++ks) { rw[w][ks] = ra[ks] + w * 65536; PIN(rw[w][ks]); }
        s16x8 wb[2][5];
        UNROLL for (int z = 0; z < 21; ++z) {
            if (z == 3 && nt != 15) publish(W, WS_F1 + ((nt * 2 + wn) * 32 + mt * 2 + wm) * FS, epoch, lane);
            if (z == 8) { WAITV(0); WT_STORE(); }
            if (z == 16) { lds_wait(L + FL_FULL + fl4, 1); }
            UNROLL for (int u = 0; u < 5; ++u) {
                if (z < 20) { wb[z & 1][u] = lds128(L + rw[WH_ADDR((z >> 1) * 10 + 2 * u) >> 16][z & 1] + (WH_ADDR((z >> 1) * 10 + 2 * u) & 65535)); SBAR(); }
                if (z > 0) {
                    hc[0][u] = MFMA16(xs[(z - 1) >> 1][(z - 1) & 1][0], wb[(z - 1) & 1][u], hc[0][u]);
                    hc[1][u] = MFMA16(xs[(z - 1) >> 1][(z - 1) & 1][1], wb[(z - 1) & 1][u], hc[1][u]); SBAR();
                }
            }
        }
        lds_signal(LW + FL_FREE + wid * 4, 1);
        const float sg = nt < 8 ? 1.0f : -1.0f;
        const unsigned ab = WS_AB + (((nt & 7) >> 1) * 512 + (nt >> 3) * 256 + mt * 16 + wm * 8 + wn * 4 + (kg & 1) * 2 + (kg >> 1)) * 2560 + ((nt & 1) * 80 + l15) * 16;
        UNROLL for (int u = 0; u < 5; ++u) {
            unsigned a0 = pack2(hc[0][u][0] * sg, hc[0][u][1] * sg), a1 = pack2(hc[0][u][2] * sg, hc[0][u][3] * sg);
            unsigned b0 = pack2(hc[1][u][0] * sg, hc[1][u][1] * sg), b1 = pack2(hc[1][u][2] * sg, hc[1][u][3] * sg);
            auto s0 = __builtin_amdgcn_permlane16_swap(a0, b0, false, false);
            auto s1 = __builtin_amdgcn_permlane16_swap(a1, b1, false, false);
            st128(W, u32x4{s0[0], s1[0], s0[1], s1[1]}, ab, u * 256);
        }
        publish(W, WS_F2 + (nt * 64 + mt * 4 + wid) * FS, epoch, lane);
        if (lane == 0) st32(W, epoch, WS_ACK + (blockIdx.x * 8 + wid) * 4, 0);
    }
    }
    const int lane = __builtin_amdgcn_mbcnt_hi(~0u, __builtin_amdgcn_mbcnt_lo(~0u, 0u));
    const int r = lane & 31, hh = lane >> 5;
    const int l15 = lane & 15, kg = lane >> 4;
    const unsigned fl4 = (lane & 3) * 4;

    const int ksp = xcd >> 1, nt4 = idx >> 3;
    const int tt = mt * 2 + (ksp & 1), ct = nt4 * 2 + (ksp >> 1);
    const u32x4 dc = mkdesc(ksp < 2 ? cre : cim, 2048 * 4096), dws = mkdesc(ws, WS_END);
    const unsigned cs0 = mt * 128 * 4096 + (ksp & 1) * 2048;
    const unsigned as0 = WS_AB + (nt4 * 512 + ksp * 128) * 2560;

    if (wid >= 4) {
        const int head = xcd * 4 + (idx >> 3), qt = idx & 7;
        const int qh = wid & 1, kh = (wid >> 1) & 1;
        const int col0 = head * 20, odd = head & 1;
        const int cA = (col0 >> 3) + odd, cH = odd ? (col0 >> 3) : (col0 >> 3) + 2;
        const int q0 = qt * 256 + qh * 128;
        unsigned k1off = WS_QK + (80 + cA + hh) * CH + r * 16;
        unsigned k2off = hh ? OOB : WS_QK + (80 + cH) * CH + r * 16 + odd * 8;
        unsigned q2off = hh ? OOB : k2off - 80 * CH;
        unsigned k2or = hh ? 0x3F80u : 0u;
        const int dv = odd ? (r < 16 ? r + 4 : r - 16) : r;
        const unsigned voff = r < 20 ? WS_VT + hh * VROW + (col0 + dv) * 16 : (r == 20 ? oneoff : OOB);
        unsigned vstep = r < 20 ? 4 * VROW : 0;
        unsigned va = voff + kh * 32 * vstep, vb2 = va + (vstep >> 1);
        constexpr int NB = 4;
        u32x4 k1[NB], v0[NB], v1[NB]; u32x2 k2[NB];
        f32x16 o[4], s[4];
        int sidx = (int)0xDDDDDDDD;
        s[3] = f32x16{};
        UNROLL for (int i = 0; i < 4; ++i) { o[i] = f32x16{}; PIN(o[i]); }
        v0[NB - 1] = u32x4{}; v1[NB - 1] = u32x4{};
        PIN(v0[NB - 1]); PIN(v1[NB - 1]); PIN(s[3]); PIN(sidx);
        PIN(k1off); PIN(k2off); PIN(q2off); PIN(k2or); PIN(va); PIN(vb2); PIN(vstep);
        WAITV(0);
        lds_post(LW + FL_FULL + gwv * 4, 1);
        const int nsa = (lane & 4) ? (col0 + 19) >> 6 : col0 >> 6;
        unsigned fkv = WS_F1 + ((10 + ((lane >> 3) & 1) * 10 + nsa) * 32 + kh * 16) * FS;
        __builtin_amdgcn_s_sleep(8);
        wait_flags1<4>(W, (lane & 16) ? WS_F1 + (nsa * 32 + (q0 >> 6) + (lane & 1)) * FS : fkv + (lane & 3) * FS, epoch);
        fkv += (lane & 1) * FS;
        u32x4 q1[4]; u32x2 q2[4];
        UNROLL for (int i = 0; i < 4; ++i) {
            const int qr = q0 + ((i + 2 + 2 * kh) & 3) * 32;
            q2[i] = ld64(W, q2off, qr * 16);
            q1[i] = ld128(W, k1off - 80 * CH, qr * 16);
        }
        const int t0 = kh * 32;
        unsigned fnx = epoch;
        const unsigned fwo = hh ? WS_F2 + (15 * 64 + (tt >> 1) * 4 + (r & 3)) * FS : WS_F2 + (((r >> 3) * 4 + gwv) * 64 + (((r >> 2) & 1) * 8 + ct) * 4 + (r & 3)) * FS;
        SBAR();
        UNROLL for (int b = 0; b < NB; ++b) {
            k2[b] = ld64(W, k2off, (t0 + b) * 512); k1[b] = ld128(W, k1off, (t0 + b) * 512); SBAR();
            if (b < NB - 1) { v0[b] = ld128(W, va, 0); v1[b] = ld128(W, vb2, 0); va += vstep; vb2 += vstep; SBAR(); }
        }
        const f32x16 zero16 = f32x16{};
        {
            f32x16 t[4];
            UNROLL for (int i = 0; i < 4; ++i) { t[i] = MFMA32K8(as4(k2[0]), as4(q2[i]), zero16); t[i] = MFMA32(as8(k1[0]), as8(q1[i]), t[i]); SBAR(); }
            k2[0][0] |= k2or;
            UNROLL for (int i = 0; i < 4; ++i) {
                float m = t[i][0];
                UNROLL for (int e = 1; e < 16; ++e) m = fmaxf(m, t[i][e]);
                auto mm = __builtin_amdgcn_permlane32_swap(__float_as_uint(m), __float_as_uint(m), false, false);
                m = fmaxf(__uint_as_float(mm[0]), __uint_as_float(mm[1]));
                q2[i][0] = hh ? pack2(-m, 0.f) & 0xFFFFu : q2[i][0];
                if (i == 0) {
                    s[0] = MFMA32K8(as4(k2[0]), as4(q2[0]), zero16);
                    s[0] = MFMA32(as8(k1[0]), as8(q1[0]), s[0]);
                }
            }
        }
        st32(W, epoch, lane == 0 ? WS_ACK + (blockIdx.x * 8 + wid) * 4 : OOB, 0);
        __builtin_amdgcn_s_setprio(1);
        NOUNROLL for (int tb = 0; tb < 32; tb += NB) {
            if (__builtin_expect(!__all(fnx == epoch), 0)) wait_flags1<4>(W, fkv + (tb < 28 ? tb + 4 : 0) * (FS / 2), epoch);
            if (tb == 16) lds_wait(L + FL_FREE + fl4, 1);
            const unsigned fso = (tb < 24 ? tb + 8 : 0) * (FS / 2);
            if (tb == 28) fkv = fwo;
            SBAR();
            UNROLL for (int b = 0; b < NB; ++b) {
                const int bn = (b + 1) % NB, bp = (b + NB - 1) % NB;
                const unsigned kso = tb + b + NB < 32 ? (t0 + tb + b + NB) * 512 : OOB / 2;
                const unsigned vso = tb + b + NB - 1 < 32 ? 0 : OOB / 2;
                UNROLL for (int qi = 0; qi < 4; ++qi) {
                    const int qn = (qi + 1) & 3, qp = (qi + 3) & 3;
                    const int kb = qi == 3 ? bn : b, vb = qi == 0 ? bp : b;
#define E4(i) do { UNROLL for (int e = (i); e < (i) + 4; ++e) s[qi][e] = EX2(s[qi][e]); PIN(s[qi]); SBAR(); } while (0)
                    s[qn] = MFMA32K8(as4(k2[kb]), as4(q2[qn]), zero16); SBAR();
                    E4(0);
                    s[qn] = MFMA32(as8(k1[kb]), as8(q1[qn]), s[qn]); SBAR();
                    if (qi == 2) { k2[b] = ld64(W, k2off, kso); k1[b] = ld128(W, k1off, kso); SBAR(); }
                    if (qi == 3 && b == 0) { fnx = ld32(W, fkv, fso); SBAR(); }
                    E4(4);
                    o[qp] = SPV(v0[vb], s[qp], 0, o[qp]); SBAR();
                    if (qi == 2) { k2[bn][0] |= k2or; PIN(k2[bn]); }
                    if (qi == 1) { va += vstep; vb2 += vstep; PIN(va); PIN(vb2); }
                    E4(8);
                    o[qp] = SPV(v1[vb], s[qp], 1, o[qp]); SBAR();
                    if (qi == 0) { v0[bp] = ld128(W, va, vso); v1[bp] = ld128(W, vb2, vso); SBAR(); }
                    E4(12);
                }
            }
        }
        __builtin_amdgcn_s_setprio(3);
        o[3] = SPV(v0[NB - 1], s[3], 0, o[3]);
        o[3] = SPV(v1[NB - 1], s[3], 1, o[3]);
        SBAR();
        if (__builtin_expect(!__all(fnx == epoch), 0)) wait_flags1<4>(W, fwo, epoch);
        const int f0 = q0 + kh * 64;
        UNROLL for (int j = 0; j < 2; ++j) {
            const lds_p ex = LW + LDS_EX + (gwv * 2 + j) * EXB;
            *(__attribute__((address_space(3))) u32x4*)(ex + lane * 16) =
                u32x4{pack2(o[j][0], o[j][1]), pack2(o[j][2], o[j][3]), pack2(o[j][4], o[j][5]), pack2(o[j][6], o[j][7])};
            *(__attribute__((address_space(3))) u32x2*)(ex + 1024 + lane * 8) =
                hh ? u32x2{__float_as_uint(o[j][8]), q2[j][0] << 16} : u32x2{pack2(o[j][8], o[j][9]), pack2(o[j][10], o[j][11])};
        }
        lds_signal(LW + FL_EX + gwv * 4);
        float n1[2], l1[2];
        UNROLL for (int j = 0; j < 2; ++j) {
            auto nn = __builtin_amdgcn_permlane32_swap(q2[2 + j][0] << 16, q2[2 + j][0] << 16, false, false);
            auto ll = __builtin_amdgcn_permlane32_swap(__float_as_uint(o[2 + j][8]), __float_as_uint(o[2 + j][8]), false, false);
            n1[j] = __uint_as_float(nn[1]); l1[j] = __uint_as_float(ll[1]);
        }
        const unsigned orow = WS_O + ((f0 >> 4) + (r >> 4)) * 20480 + (r & 15) * 16;
        const unsigned oa = orow + (cA + hh) * 256, ob = hh ? OOB : orow + cH * 256 + odd * 8;
        lds_wait(L + FL_EX + (gwv ^ 2) * 4);
        bool ok = true;
        u32x4 ra[2]; u32x2 rb[2];
        UNROLL for (int j = 0; j < 2; ++j) {
            const lds_cp rx = L + LDS_EX + ((gwv ^ 2) * 2 + j) * EXB;
            const u32x4 a = *(const __attribute__((address_space(3))) u32x4*)(rx + lane * 16);
            const u32x2 c = *(const __attribute__((address_space(3))) u32x2*)(rx + 1024 + r * 8);
            const u32x2 d = *(const __attribute__((address_space(3))) u32x2*)(rx + 1280 + r * 8);
            const float n2 = __uint_as_float(d[1]), l2 = __uint_as_float(d[0]), nm = fminf(n1[j], n2);
            float w1 = EX2(nm - n1[j]), w2 = EX2(nm - n2);
            const float l = l1[j] * w1 + l2 * w2, rl = __builtin_amdgcn_rcpf(l);
            w1 *= rl; w2 *= rl;
            float f[12];
            UNROLL for (int e = 0; e < 4; ++e) { f[2 * e] = o[2 + j][2 * e] * w1 + bflo(a[e]) * w2; f[2 * e + 1] = o[2 + j][2 * e + 1] * w1 + bfhi(a[e]) * w2; }
            UNROLL for (int e = 0; e < 2; ++e) { f[8 + 2 * e] = o[2 + j][8 + 2 * e] * w1 + bflo(c[e]) * w2; f[9 + 2 * e] = o[2 + j][9 + 2 * e] * w1 + bfhi(c[e]) * w2; }
            float sa = 0.f;
            UNROLL for (int e = 0; e < 12; ++e) sa += fabsf(f[e]);
            ok = ok && l1[j] < 1e30f && l2 < 1e30f && l > 1e-18f && sa < 3e38f;
            auto s0 = __builtin_amdgcn_permlane32_swap(pack2(f[0], f[1]), pack2(f[4], f[5]), false, false);
            auto s1 = __builtin_amdgcn_permlane32_swap(pack2(f[2], f[3]), pack2(f[6], f[7]), false, false);
            ra[j] = u32x4{s0[0], s1[0], s0[1], s1[1]};
            rb[j] = u32x2{pack2(f[8], f[9]), pack2(f[10], f[11])};
        }
        if (__builtin_expect(__all(ok), 1)) {
            UNROLL for (int j = 0; j < 2; ++j) { st128(W, ra[j], oa + j * 40960, 0); st64(W, rb[j], ob + j * 40960, 0); }
        } else {
            wait_flags1<4>(W, WS_F1 + ((10 + hh * 10 + (col0 >> 6)) * 32 + r) * FS, epoch);
            wait_flags1<4>(W, WS_F1 + ((10 + hh * 10 + ((col0 + 19) >> 6)) * 32 + r) * FS, epoch);
            NOUNROLL for (int j = 0; j < 2; ++j) {
                const u32x2 z2 = ld64(W, q2off, (f0 + j * 32) * 16);
                const u32x4 z1 = ld128(W, k1off - 80 * CH, (f0 + j * 32) * 16);
                float m = -3e38f;
                f32x16 oo = f32x16{};
                NOUNROLL for (int t = 0; t < 64; ++t) {
                    const u32x4 a1 = ld128(W, k1off, t * 512); const u32x2 a2 = ld64(W, k2off, t * 512);
                    const u32x4 w0 = ld128(W, voff + t * vstep, 0), w1 = ld128(W, voff + t * vstep + (vstep >> 1), 0);
                    f32x16 z = MFMA32K8(as4(a2), as4(z2), zero16);
                    z = MFMA32(as8(a1), as8(z1), z);
                    float mx = z[0];
                    UNROLL for (int e = 1; e < 16; ++e) mx = fmaxf(mx, z[e]);
                    auto mm = __builtin_amdgcn_permlane32_swap(__float_as_uint(mx), __float_as_uint(mx), false, false);
                    const float mn = fmaxf(m, fmaxf(__uint_as_float(mm[0]), __uint_as_float(mm[1])));
                    const float al = EX2(m - mn);
                    m = mn;
                    UNROLL for (int e = 0; e < 16; ++e) oo[e] *= al;
                    UNROLL for (int e = 0; e < 16; ++e) z[e] = EX2(z[e] - m);
                    oo = SPV(w0, z, 0, oo);
                    oo = SPV(w1, z, 1, oo);
                }
                auto l2 = __builtin_amdgcn_permlane32_swap(__float_as_uint(oo[8]), __float_as_uint(oo[8]), false, false);
                const float sc = 1.0f / __uint_as_float(l2[1]);
                auto s0 = __builtin_amdgcn_permlane32_swap(pack2(oo[0] * sc, oo[1] * sc), pack2(oo[4] * sc, oo[5] * sc), false, false);
                auto s1 = __builtin_amdgcn_permlane32_swap(pack2(oo[2] * sc, oo[3] * sc), pack2(oo[6] * sc, oo[7] * sc), false, false);
                st128(W, u32x4{s0[0], s1[0], s0[1], s1[1]}, oa + j * 40960, 0);
                st64(W, u32x2{pack2(oo[8] * sc, oo[9] * sc), pack2(oo[10] * sc, oo[11] * sc)}, ob + j * 40960, 0);
            }
        }
        publish(W, WS_F3 + ((f0 >> 6) * 32 + head) * FS, epoch, lane);
        u32x4 wf[5][5];
        UNROLL for (int st = 0; st < 5; ++st) UNROLL for (int u = 0; u < 5; ++u) wf[st][u] = ld128(W, WS_WOT + (ct * 4 + gwv) * 25600 + lane * 16, (st * 5 + u) * 1024);
        const int tf = tt * 64;
        const float gt = __uint_as_float(ld32(W, WS_G + (tf + gwv * 16 + l15) * 4, 0));
        wait_flags1<4>(W, WS_F3 + (tt * 32 + gwv * 8 + (lane & 7)) * FS, epoch);
        const unsigned boff = WS_O + tt * 81920 + gwv * 5120 + lane * 16;
        u32x4 bf[4][5];
        UNROLL for (int d = 0; d < 4; ++d)
            UNROLL for (int st = 0; st < 5; ++st) bf[d][st] = ld128(W, boff + st * 1024, ((gwv + d + 1) & 3) * 20480);
        const unsigned fy = WS_F4 + ((tt * 8 + ct) * 4 + ((ksp + 1 + (r % 3)) & 3)) * FS;
        SBAR();
        f32x4 pt[4][5];
        u32x4 ya[3][2]; u32x2 yc[3];
#define FN_LY() do { UNROLL for (int p = 0; p < 3; ++p) { \
            const unsigned yo = WS_Y + (((tt * 8 + ct) * 4 + ((ksp + 1 + p) & 3)) * 4 + gwv) * 2560; \
            ya[p][0] = ld128(W, lane * 16, yo); ya[p][1] = ld128(W, lane * 16 + 1024, yo); yc[p] = ld64(W, lane * 8 + 2048, yo); } } while (0)
        UNROLL for (int d = 0; d < 4; ++d) {
            UNROLL for (int u = 0; u < 5; ++u) pt[d][u] = f32x4{};
            UNROLL for (int st = 0; st < 5; ++st) UNROLL for (int u = 0; u < 5; ++u) pt[d][u] = MFMA16(as8(wf[st][u]), as8(bf[d][st]), pt[d][u]);
            SBAR();
        }
        lds_wait<2>(L + FL_SFULL + fl4, 16);
        UNROLL for (int d = 0; d < 3; ++d) UNROLL for (int u = 0; u < 5; ++u) stsf4(LW + ((gwv + d + 1) & 3) * GW_LDS + (d * 5 + u) * 1024 + lane * 16, pt[d][u]);
        lds_signal(LW + FL_RED + gwv * 4);
        wait_flags1<4>(W, fy, epoch);
        FN_LY();
        SBAR();
        lds_wait(L + FL_RED + fl4);
        f32x4 res[5];
        UNROLL for (int u = 0; u < 5; ++u) {
            res[u] = pt[3][u];
            UNROLL for (int d = 0; d < 3; ++d) res[u] += ldsf4(L + gwv * GW_LDS + (d * 5 + u) * 1024 + lane * 16);
            res[u] *= gt;
        }
        lds_wait<2>(L + FL_FIN, 2);
        const float h = 1.0f - gt;
        UNROLL for (int u = 0; u < 5; ++u) { res[u] += h * ldsf4(L + 2 * SQ_STAGE + (gwv * 5 + u) * 1024 + lane * 16); PIN(res[u]); }
        UNROLL for (int u = 0; u < 5; ++u) {
            f32x4 ys = f32x4{};
            UNROLL for (int p = 0; p < 3; ++p) {
                const unsigned w0 = u < 4 ? ya[p][(u >> 1) & 1][(u & 1) * 2] : yc[p][0], w1 = u < 4 ? ya[p][(u >> 1) & 1][(u & 1) * 2 + 1] : yc[p][1];
                ys[0] += bflo(w0); ys[1] += bfhi(w0); ys[2] += bflo(w1); ys[3] += bfhi(w1);
            }
            res[u] += h * ys;
        }
        const rsrc_t RO = __builtin_amdgcn_make_buffer_rsrc(out, 0, 2048 * 1280, 0x00020000);
        const unsigned oo = (tf + gwv * 16 + l15) * 1280 + ct * 160;
        UNROLL for (int u = 0; u < 4; u += 2) {
            auto s0 = __builtin_amdgcn_permlane16_swap(pack2(res[u][0], res[u][1]), pack2(res[u + 1][0], res[u + 1][1]), false, false);
            auto s1 = __builtin_amdgcn_permlane16_swap(pack2(res[u][2], res[u][3]), pack2(res[u + 1][2], res[u + 1][3]), false, false);
            __builtin_amdgcn_raw_buffer_store_b128(u32x4{s0[0], s1[0], s0[1], s1[1]}, RO, oo + (u + (kg & 1)) * 32 + (kg >> 1) * 16, 0, 2);
        }
        __builtin_amdgcn_raw_buffer_store_b64(u32x2{pack2(res[4][0], res[4][1]), pack2(res[4][2], res[4][3])}, RO, oo + 128 + kg * 8, 0, 2);
        return;
    }

    __builtin_amdgcn_s_setprio(2);
    {
        unsigned coff[2]; const unsigned lin = lane * 16;
        UNROLL for (int i = 0; i < 2; ++i) coff[i] = (lane >> 3) * 4096 + (((lane & 7) ^ ((4 * i + (lane >> 4)) & 7)) << 4);
#define SQ_DC4(n, slot) do { const unsigned c_ = cs0 + gwv * 131072 + (n) * 128; \
            dma16x4(coff[0], coff[1], dc, c_, c_ + 31744, c_ + 63488, c_ + 95232, lds0 + (slot) * SQ_STAGE + gwv * 4096); } while (0)
#define SQ_DA5(n, slot) do { dma16sx4(lin, dws, as0 + (n) * 20480 + gwv * 5120, lds0 + (slot) * SQ_STAGE + 16384 + gwv * 5120); SQ_DA(n, slot, 4); } while (0)
#define SQ_DA(n, slot, i) dma16s(lin, dws, as0 + (n) * 20480 + gwv * 5120 + (i) * 1024, lds0 + (slot) * SQ_STAGE + 16384 + gwv * 5120 + (i) * 1024)
        const int wm = wid & 1, wn = wid >> 1;
        f32x4 acc[4][5];
        UNROLL for (int i = 0; i < 4; ++i) UNROLL for (int u = 0; u < 5; ++u) { acc[i][u] = f32x4{}; PIN(acc[i][u]); }
        unsigned ra[2];
        UNROLL for (int ks = 0; ks < 2; ++ks) ra[ks] = wm * 8192 + l15 * 128 + (((4 * ks + kg) ^ ((l15 >> 1) & 7)) << 4);
        const unsigned rb = 16384 + kg * 2560 + (wn * 80 + l15) * 16;
        s16x8 fc[2][4], fa[2][5];
        UNROLL for (int i = 0; i < 4; ++i) fc[1][i] = s16x8{};
        UNROLL for (int u = 0; u < 5; ++u) fa[1][u] = s16x8{};
        lds_wait<2>(L + FL_FREE + fl4, 1);
        SQ_DC4(0, 0); SQ_DC4(1, 1);
        if (wid == 0) {
            const unsigned f2 = WS_F2 + (((ksp >> 1) * 8 + nt4 * 2 + hh) * 64 + (ksp & 1) * 32 + r) * FS;
            __builtin_amdgcn_s_sleep(12);
            wait_flags1<12>(W, f2, epoch);
            lds_post(LW + FL_FIN, 1);
        } else {
            WAITV(0);
            lds_wait<6>(L + FL_FIN, 1);
        }
        SQ_DA5(0, 0); SQ_DA5(1, 1);
        const lds_p pw = LW + (lane == 0 ? FL_SFULL + gwv * 4 : LDS_SINK + lane * 4);
        const lds_cp pr = L + FL_SFULL + fl4;
#define SQ_MF(u) do { UNROLL for (int i = 0; i < 4; ++i) acc[i][u] = MFMA16(fa[c][u], fc[c][i], acc[i][u]); SBAR(); } while (0)
#define SQ_RC(i) fc[n][i] = lds128(sr[ks] + (i) * 2048)
#define SQ_RA(u) fa[n][u] = lds128(sp + ks * 10240 + (u) * 256)
#define SQ_STG(DMA) do { \
            lds_cp sp = L + (j & 3) * SQ_STAGE + rb; lds_cp sr[2]; \
            UNROLL for (int ks = 0; ks < 2; ++ks) { sr[ks] = L + (j & 3) * SQ_STAGE + ra[ks]; PIN(sr[ks]); } \
            unsigned pv = j + 2, fl = 0; \
            PIN(sp); PIN(pv); SBAR(); \
            __builtin_amdgcn_s_setprio(0); \
            const int sf = (j + 2) & 3; \
            UNROLL for (int ks = 0; ks < 2; ++ks) { \
                const int n = ks, c = n ^ 1; \
                SQ_RC(0); SQ_RC(1); SQ_RC(2); SBAR(); SQ_MF(0); \
                SQ_RC(3); SQ_RA(0); SQ_RA(1); SBAR(); SQ_MF(1); \
                SQ_RA(2); SQ_RA(3); SQ_RA(4); \
                if (ks == 0) { WAITV(0); lds_post(pw, pv); } \
                SBAR(); SQ_MF(2); \
                if (DMA && ks == 0) { SQ_DC4(j + 2, sf); SBAR(); } \
                if (ks == 1) { fl = lds_peek(pr); SBAR(); } \
                SQ_MF(3); \
                if (DMA && ks == 0) { SQ_DA5(j + 2, sf); SBAR(); } \
                SQ_MF(4); \
            } \
            __builtin_amdgcn_s_setprio(3); \
            if (__builtin_expect(!__all(fl >= pv), 0)) lds_wait<3>(pr, pv); } while (0)
        int j = 0;
        WAITV(5); lds_post(pw, 1); lds_wait(pr, 1);
        NOUNROLL for (; j < 14; ++j) SQ_STG(true);
        NOUNROLL for (; j < 16; ++j) SQ_STG(false);
        UNROLL for (int u = 0; u < 5; ++u) UNROLL for (int i = 0; i < 4; ++i) acc[i][u] = MFMA16(fa[1][u], fc[1][i], acc[i][u]);
        __builtin_amdgcn_s_setprio(3);
        if (wid == ksp) {
            UNROLL for (int i = 0; i < 4; ++i) UNROLL for (int u = 0; u < 5; ++u) stsf4(LW + 2 * SQ_STAGE + (i * 5 + u) * 1024 + lane * 16, acc[i][u]);
            lds_signal(LW + FL_FIN, 2);
        } else {
            const unsigned yb = WS_Y + ((((mt * 2 + wm) * 8 + nt4 * 2 + wn) * 4 + ksp) * 4) * 2560;
            UNROLL for (int i = 0; i < 4; ++i) {
                UNROLL for (int u = 0; u < 4; u += 2)
                    st128(W, u32x4{pack2(acc[i][u][0], acc[i][u][1]), pack2(acc[i][u][2], acc[i][u][3]), pack2(acc[i][u + 1][0], acc[i][u + 1][1]), pack2(acc[i][u + 1][2], acc[i][u + 1][3])},
                          yb + lane * 16, i * 2560 + u * 512);
                st64(W, u32x2{pack2(acc[i][4][0], acc[i][4][1]), pack2(acc[i][4][2], acc[i][4][3])}, yb + i * 2560 + 2048 + lane * 8, 0);
            }
            publish(W, WS_F4 + (((mt * 2 + wm) * 8 + nt4 * 2 + wn) * 4 + ksp) * FS, epoch, lane);
        }
        if (blockIdx.x == 0 && wid == 0) {
            NOUNROLL for (unsigned spins = 0; spins < SPIN_CAP; ++spins) {
                asm volatile("" ::: "memory");
                bool ok = true;
                UNROLL for (int i = 0; i < 8; ++i) {
                    const u32x4 f = ld128(W, WS_ACK + i * 1024 + lane * 16, 0);
                    ok = ok && f[0] == epoch && f[1] == epoch && f[2] == epoch && f[3] == epoch;
                }
                if (__all(ok)) break;
                __builtin_amdgcn_s_sleep(32);
            }
            if (lane == 0) st32(W, epoch, WS_EP, 0);
        }
    }
}

extern "C" void kernel_launch(void* const* d_in, const int* in_sizes, int n_in, void* d_out, int out_size,
                              void* d_ws, size_t ws_size, hipStream_t stream) {
    (void)hipFuncSetAttribute((const void*)fused, hipFuncAttributeMaxDynamicSharedMemorySize, LDS_BYTES);
    fused<<<256, 512, LDS_BYTES, stream>>>((const char*)d_in[0], (const char*)d_in[1], (const char*)d_in[2], (const char*)d_in[4],
                                           (const char*)d_in[5], (const char*)d_in[6], (const char*)d_in[7], (const char*)d_in[8],
                                           (char*)d_ws + ((ws_size - WS_END) & ~(size_t)0x1FFFFF), (char*)d_out);
}
```
